```python
import torch

def _softmax(x, axis=-1):
    return torch.softmax(x, dim=axis)
def _gelu(x):
    return torch.nn.functional.gelu(x, approximate="tanh")
def _silu(x):
    return torch.nn.functional.silu(x)
def _sigmoid(x):
    return torch.sigmoid(x)
def _layernorm(x, axis=-1, eps=1e-5):
    m = x.mean(dim=axis, keepdim=True)
    v = x.var(dim=axis, keepdim=True, unbiased=False)
    return (x - m) / torch.sqrt(v + eps)
def _scatter_add(tgt, idx, src, axis=0):
    return tgt.clone().index_add_(axis, idx, src)
def _segment_sum(src, seg, n):
    out = torch.zeros((n,) + src.shape[1:], dtype=src.dtype)
    return out.index_add_(0, seg, src)
def _pad(x, pw):
    flat = tuple(v for pair in reversed(pw) for v in pair)
    return torch.nn.functional.pad(x, flat)

kernel_name = 'interleave_layers__linear_kernel_hedgehog__sliding_block__prompt'

def setup_inputs():

    _w = (lambda a: (lambda t: t.to(torch.bfloat16) if t.is_floating_point() else t)(torch.as_tensor(a)))
    def _A_setup():
        B, S, D, H = 1, 2048, 448, 16
        Dh = D // H
        return {
            "x": _w((lambda *s: torch.randn(*s, dtype=torch.bfloat16) * 0.05)(B, S, D)),
            "wqkv": _w((lambda *s: torch.randn(*s, dtype=torch.bfloat16) * 0.4)(D, 3 * D)),
            "w_phi": _w((lambda *s: torch.randn(*s, dtype=torch.bfloat16) * 0.4)(H, Dh, Dh) * (Dh ** -0.5)),
            "wo": _w((lambda *s: torch.randn(*s, dtype=torch.bfloat16) * 0.05)(D, D)),
        }
    _A_in = _A_setup()

    def _B_setup():
        B, S, D = 1, 2048, 448
        return {
            "x": _w((lambda *s: torch.randn(*s, dtype=torch.bfloat16) * 0.05)(B, S, D)),
            "wqkv": _w((lambda *s: torch.randn(*s, dtype=torch.bfloat16) * 0.1)(D, 3 * D)),
            "wo": _w((lambda *s: torch.randn(*s, dtype=torch.bfloat16) * 0.05)(D, D)),
        }
    _B_in = _B_setup()

    return {
        "A_x": _A_in["x"],
        "A_wqkv": _A_in["wqkv"],
        "A_w_phi": _A_in["w_phi"],
        "A_wo": _A_in["wo"],
        "B_x": _B_in["x"],
        "B_wqkv": _B_in["wqkv"],
        "B_wo": _B_in["wo"],
    }

def reference(A_x, A_wqkv, A_w_phi, A_wo, B_x, B_wqkv, B_wo):
    def _A_ref(x, wqkv, w_phi, wo):
        B, S, D = x.shape
        H = 16; Dh = D // H
        qkv = (lambda x, *d: x.permute(*d))((x @ wqkv).reshape(B, S, 3, H, Dh), 2, 0, 3, 1, 4)
        q, k, v = qkv[0], qkv[1], qkv[2]
        pq = torch.einsum("bhsd,hdf->bhsf", q, w_phi)
        pk = torch.einsum("bhsd,hdf->bhsf", k, w_phi)
        phi_q = torch.concatenate([_softmax(pq, axis=-1), _softmax(-pq, axis=-1)], axis=-1)
        phi_k = torch.concatenate([_softmax(pk, axis=-1), _softmax(-pk, axis=-1)], axis=-1)
        kv = torch.einsum("bhsf,bhsd->bhfd", phi_k, v)
        z  = torch.einsum("bhsf->bhf", phi_k)
        num = torch.einsum("bhqf,bhfd->bhqd", phi_q, kv)
        den = torch.einsum("bhqf,bhf->bhq", phi_q, z)[..., None] + 1e-6
        o = num / den
        return (lambda x, *d: x.permute(*d))(o, 0, 2, 1, 3).reshape(B, S, D) @ wo

    def _B_ref(x, wqkv, wo):
        B, S, D = x.shape
        H = 16; Dh = D // H; W = 256
        qkv = (lambda x, *d: x.permute(*d))((x @ wqkv).reshape(B, S, 3, H, Dh), 2, 0, 3, 1, 4)
        q, k, v = qkv[0], qkv[1], qkv[2]
        scores = torch.einsum("bhqd,bhkd->bhqk", q, k) * (Dh ** -0.5)
        idx = torch.arange(S)
        mask = (idx[:, None] - idx[None, :] >= 0) & (idx[:, None] - idx[None, :] < W)
        scores = torch.where(mask[None, None], scores, -1e9)
        p = _softmax(scores, axis=-1)
        o = torch.einsum("bhqk,bhkd->bhqd", p, v)
        return (lambda x, *d: x.permute(*d))(o, 0, 2, 1, 3).reshape(B, S, D) @ wo

    x = A_x
    x = x + _A_ref(_layernorm(x, axis=-1), A_wqkv, A_w_phi, A_wo)
    x = x + _B_ref(_layernorm(x, axis=-1), B_wqkv, B_wo)
    x = x + _A_ref(_layernorm(x, axis=-1), A_wqkv, A_w_phi, A_wo)
    x = x + _B_ref(_layernorm(x, axis=-1), B_wqkv, B_wo)
    return x
```

```cpp
#include <hip/hip_runtime.h>
#include <hip/hip_bf16.h>

typedef unsigned short u16;
static constexpr int S = 2048, D = 448, H = 16, DH = 28, W = 256;

__device__ __forceinline__ float bf2f(u16 b) { return __uint_as_float(((unsigned)b) << 16); }
__device__ __forceinline__ u16 f2bf(float f) { unsigned u = __float_as_uint(f); u += 0x7fff + ((u >> 16) & 1); return (u16)(u >> 16); }
__device__ __forceinline__ float rbf(float f) { return f; }

__global__ void k_cvt_in(const u16* x, float* xf) { int i = blockIdx.x * 256 + threadIdx.x; if (i < S * D) xf[i] = bf2f(x[i]); }
__global__ void k_cvt_out(const float* xf, u16* o) { int i = blockIdx.x * 256 + threadIdx.x; if (i < S * D) o[i] = f2bf(xf[i]); }

__global__ void k_ln(const float* x, float* xn) {
  int r = blockIdx.x; int t = threadIdx.x;
  float s = 0; for (int c = t; c < D; c += 64) s += x[r * D + c];
  for (int o = 32; o; o >>= 1) s += __shfl_xor(s, o);
  float m = s / D; float v = 0;
  for (int c = t; c < D; c += 64) { float d = x[r * D + c] - m; v += d * d; }
  for (int o = 32; o; o >>= 1) v += __shfl_xor(v, o);
  v /= D; float rs = 1.0f / sqrtf(v + 1e-5f);
  for (int c = t; c < D; c += 64) xn[r * D + c] = rbf((x[r * D + c] - m) * rs);
}
__global__ void k_gemm(const float* A, const u16* B, float* C, const float* R, int M, int N, int K) {
  int n = blockIdx.x * 64 + threadIdx.x; int m = blockIdx.y;
  if (n >= N) return;
  float acc = 0; for (int k = 0; k < K; ++k) acc += A[m * K + k] * bf2f(B[k * N + n]);
  acc = rbf(acc);
  if (R) acc = rbf(acc + R[m * N + n]);
  C[m * N + n] = acc;
}
__global__ void k_phi(const float* qkv, const u16* wphi, float* phi) {
  int s = blockIdx.x, h = blockIdx.y, which = blockIdx.z; int f = threadIdx.x;
  float p = 0;
  if (f < DH) { for (int d = 0; d < DH; ++d) p += qkv[s * 3 * D + which * D + h * DH + d] * bf2f(wphi[h * DH * DH + d * DH + f]); p = rbf(p); }
  float mx = f < DH ? p : -1e30f, mn = f < DH ? -p : -1e30f;
  for (int o = 32; o; o >>= 1) { mx = fmaxf(mx, __shfl_xor(mx, o)); mn = fmaxf(mn, __shfl_xor(mn, o)); }
  float e1 = f < DH ? __expf(p - mx) : 0, e2 = f < DH ? __expf(-p - mn) : 0;
  float s1 = e1, s2 = e2;
  for (int o = 32; o; o >>= 1) { s1 += __shfl_xor(s1, o); s2 += __shfl_xor(s2, o); }
  if (f < DH) { float* o = phi + ((size_t)(which * H + h) * S + s) * 56; o[f] = rbf(e1 / s1); o[28 + f] = rbf(e2 / s2); }
}
__global__ void k_kv(const float* qkv, const float* phi, float* kv) {
  int h = blockIdx.y, f = blockIdx.x; int d = threadIdx.x; if (d > DH) return;
  float acc = 0;
  for (int s = 0; s < S; ++s) { float p = phi[((size_t)(1 * H + h) * S + s) * 56 + f]; acc += p * (d < DH ? qkv[s * 3 * D + 2 * D + h * DH + d] : 1.0f); }
  kv[(h * 56 + f) * 29 + d] = rbf(acc);
}
__global__ void k_o(const float* phi, const float* kv, float* o) {
  int s = blockIdx.x, h = blockIdx.y; int d = threadIdx.x; if (d >= DH) return;
  float num = 0, den = 0;
  for (int f = 0; f < 56; ++f) { float p = phi[((size_t)(0 * H + h) * S + s) * 56 + f]; num += p * kv[(h * 56 + f) * 29 + d]; den += p * kv[(h * 56 + f) * 29 + 28]; }
  o[s * D + h * DH + d] = rbf(rbf(num) / rbf(rbf(den) + 1e-6f));
}
__global__ void k_attn(const float* qkv, float* o) {
  int s = blockIdx.x, h = blockIdx.y; int t = threadIdx.x;
  __shared__ float p[256]; __shared__ float red[4];
  int j = s - t; float sc = -1e30f;
  if (j >= 0) { float a = 0; for (int d = 0; d < DH; ++d) a += qkv[s * 3 * D + h * DH + d] * qkv[j * 3 * D + D + h * DH + d]; sc = rbf(rbf(a) * rbf(0.18898223650461361f)); }
  float mx = sc; for (int o2 = 32; o2; o2 >>= 1) mx = fmaxf(mx, __shfl_xor(mx, o2));
  if ((t & 63) == 0) red[t >> 6] = mx; __syncthreads();
  mx = fmaxf(fmaxf(red[0], red[1]), fmaxf(red[2], red[3])); __syncthreads();
  float e = j >= 0 ? __expf(sc - mx) : 0; float sm = e;
  for (int o2 = 32; o2; o2 >>= 1) sm += __shfl_xor(sm, o2);
  if ((t & 63) == 0) red[t >> 6] = sm; __syncthreads();
  sm = red[0] + red[1] + red[2] + red[3];
  p[t] = rbf(e / sm); __syncthreads();
  if (t < DH) { float a = 0; for (int k = 0; k < 256 && s - k >= 0; ++k) a += p[k] * qkv[(s - k) * 3 * D + 2 * D + h * DH + t]; o[s * D + h * DH + t] = rbf(a); }
}

extern "C" void kernel_launch(void* const* d_in, const int* in_sizes, int n_in, void* d_out, int out_size, void* d_ws, size_t ws_size, hipStream_t stream) {
  const u16* Ax = (const u16*)d_in[0]; const u16* Awqkv = (const u16*)d_in[1]; const u16* Awphi = (const u16*)d_in[2]; const u16* Awo = (const u16*)d_in[3];
  const u16* Bwqkv = (const u16*)d_in[5]; const u16* Bwo = (const u16*)d_in[6];
  float* ws = (float*)d_ws;
  float* x = ws; float* x2 = x + S * D; float* xn = x2 + S * D; float* qkv = xn + S * D; float* phi = qkv + S * 3 * D; float* kv = phi + 2 * H * S * 56; float* o = kv + H * 56 * 29;
  k_cvt_in<<<(S * D + 255) / 256, 256, 0, stream>>>(Ax, x);
  for (int l = 0; l < 4; ++l) {
    k_ln<<<S, 64, 0, stream>>>(x, xn);
    k_gemm<<<dim3(21, S), 64, 0, stream>>>(xn, (l & 1) ? Bwqkv : Awqkv, qkv, nullptr, S, 3 * D, D);
    if (!(l & 1)) {
      k_phi<<<dim3(S, H, 2), 64, 0, stream>>>(qkv, Awphi, phi);
      k_kv<<<dim3(56, H), 64, 0, stream>>>(qkv, phi, kv);
      k_o<<<dim3(S, H), 64, 0, stream>>>(phi, kv, o);
    } else {
      k_attn<<<dim3(S, H), 256, 0, stream>>>(qkv, o);
    }
    k_gemm<<<dim3(7, S), 64, 0, stream>>>(o, (l & 1) ? Bwo : Awo, x2, x, S, D, D);
    float* t = x; x = x2; x2 = t;
  }
  k_cvt_out<<<(S * D + 255) / 256, 256, 0, stream>>>(x, (u16*)d_out);
}
```

```cpp
#define NL 4
#include <hip/hip_runtime.h>
#include <hip/hip_bf16.h>

typedef unsigned short u16;
typedef unsigned long long u64;
typedef __bf16 bf16x8 __attribute__((ext_vector_type(8)));
typedef __bf16 bf16x4 __attribute__((ext_vector_type(4)));
typedef __bf16 bf16x2 __attribute__((ext_vector_type(2)));
typedef short s16x4 __attribute__((ext_vector_type(4)));
typedef float f32x4 __attribute__((ext_vector_type(4)));
typedef float f32x2 __attribute__((ext_vector_type(2)));
typedef unsigned u32x4 __attribute__((ext_vector_type(4)));
typedef unsigned u32x2 __attribute__((ext_vector_type(2)));
#define LAS __attribute__((address_space(3)))
#define GAS __attribute__((address_space(1)))
#define DI __device__ __forceinline__
#define SB() __builtin_amdgcn_sched_barrier(0)
#define PIN(x) asm volatile("" : "+v"(x))
typedef __amdgpu_buffer_rsrc_t rsrc_t;
typedef LAS unsigned char* lptr;

namespace {
constexpr int SEQ = 2048, DM = 448;
constexpr int SC1 = 16;
constexpr unsigned WS_EPOCH = 0;
constexpr unsigned WS_FLAGS = 4096;
constexpr unsigned HOP_B = 256 * 128;
constexpr unsigned WS_XO = WS_FLAGS + 12 * HOP_B;
constexpr unsigned TILE_B = 16384;
constexpr unsigned XO_B = 128 * TILE_B;
constexpr unsigned WS_MID = WS_XO + 8 * XO_B;
constexpr unsigned MID_B = 16 * 128 * 2048;
constexpr unsigned WS_PROF = WS_MID + 4 * MID_B;
constexpr unsigned WS_END = WS_PROF + 256 * 2048;
constexpr unsigned SLAB = 448 * 64;
constexpr unsigned L_WQ = 0, L_WK = SLAB, L_WV = 2 * SLAB, L_WO = 3 * SLAB;
constexpr unsigned L_SCR = SLAB;
constexpr unsigned L_X0 = L_WO;
constexpr unsigned L_CS = L_X0 + 65536;
constexpr unsigned L_KVF = L_CS + 1024;
constexpr unsigned L_WPH = L_KVF + 4096;
constexpr unsigned L_PROF = L_WPH + 2048;
constexpr unsigned L_END = L_PROF + 2048;
static_assert(L_END <= 163840 && L_CS >= 5 * SLAB, "lds");
constexpr unsigned SPIN_CAP = 1u << 15;
constexpr unsigned SENT = 0xffffffffu;
constexpr unsigned OOB = 0x7ff00000u;
constexpr unsigned ONES = 0x3f803f80u;

DI f32x4 mfma(u32x4 a, u32x4 b, f32x4 c) {
  return __builtin_amdgcn_mfma_f32_16x16x32_bf16(__builtin_bit_cast(bf16x8, a), __builtin_bit_cast(bf16x8, b), c, 0, 0, 0);
}
DI f32x4 mfma16(u32x2 a, u32x2 b, f32x4 c) {
  return __builtin_amdgcn_mfma_f32_16x16x16bf16_1k(__builtin_bit_cast(s16x4, a), __builtin_bit_cast(s16x4, b), c, 0, 0, 0);
}
DI unsigned pk2(float a, float b) { f32x2 t = {a, b}; return __builtin_bit_cast(unsigned, __builtin_convertvector(t, bf16x2)); }
DI u32x2 pack4(f32x4 c) { return (u32x2){pk2(c[0], c[1]), pk2(c[2], c[3])}; }
DI u32x4 cat(u32x2 a, u32x2 b) { return (u32x4){a[0], a[1], b[0], b[1]}; }
DI u32x4 pack8(f32x4 a, f32x4 b) { return cat(pack4(a), pack4(b)); }
DI u32x2 lo2(u32x4 v) { return (u32x2){v[0], v[1]}; }
DI u32x2 hi2(u32x4 v) { return (u32x2){v[2], v[3]}; }
DI unsigned umax3(unsigned a, unsigned b, unsigned c) { return max(max(a, b), c); }
DI unsigned max4(unsigned m, u32x4 v) { return umax3(umax3(m, v[0], v[1]), v[2], v[3]); }
DI float fmax3(float a, float b, float c) { return fmaxf(fmaxf(a, b), c); }
DI float fmin3(float a, float b, float c) { return fminf(fminf(a, b), c); }
#define Z4 ((f32x4){0.f, 0.f, 0.f, 0.f})
DI u32x4 trfrag(lptr p) {
  bf16x4 a0 = __builtin_amdgcn_ds_read_tr16_b64_v4bf16((LAS bf16x4*)p);
  bf16x4 a1 = __builtin_amdgcn_ds_read_tr16_b64_v4bf16((LAS bf16x4*)(p + 256));
  return __builtin_bit_cast(u32x4, __builtin_shufflevector(a0, a1, 0, 1, 2, 3, 4, 5, 6, 7));
}
DI float U2F(unsigned u) { return __uint_as_float(u); }
DI unsigned F2U(float f) { return __float_as_uint(f); }
template <bool MX> DI void red2(float& a, float& b) {
  auto r = __builtin_amdgcn_permlane16_swap(F2U(a), F2U(b), false, false);
  float t = MX ? fmaxf(U2F(r[0]), U2F(r[1])) : U2F(r[0]) + U2F(r[1]);
  r = __builtin_amdgcn_permlane32_swap(F2U(t), F2U(t), false, false);
  t = MX ? fmaxf(U2F(r[0]), U2F(r[1])) : U2F(r[0]) + U2F(r[1]);
  r = __builtin_amdgcn_permlane16_swap(F2U(t), F2U(t), false, false);
  a = U2F(r[0]); b = U2F(r[1]);
}
DI float redmax(float a) {
  auto r = __builtin_amdgcn_permlane16_swap(F2U(a), F2U(a), false, false);
  float t = fmaxf(U2F(r[0]), U2F(r[1]));
  r = __builtin_amdgcn_permlane32_swap(F2U(t), F2U(t), false, false);
  return fmaxf(U2F(r[0]), U2F(r[1]));
}
DI float row3_val(float v) {
  auto r = __builtin_amdgcn_permlane32_swap(F2U(v), F2U(v), false, false);
  r = __builtin_amdgcn_permlane16_swap(r[1], r[1], false, false);
  return U2F(r[1]);
}
DI float dot2(unsigned u, unsigned v, float c) { return __builtin_amdgcn_fdot2_f32_bf16(__builtin_bit_cast(bf16x2, u), __builtin_bit_cast(bf16x2, v), c, false); }
DI float bflo(unsigned u) { return U2F(u << 16); }
DI float bfhi(unsigned u) { return U2F(u & 0xffff0000u); }
template <int AUX> DI void st128(u32x4 v, rsrc_t rs, unsigned voff, unsigned soff) {
  SB();
  __builtin_amdgcn_raw_buffer_store_b128(v, rs, voff, soff, AUX);
  SB();
  asm volatile("s_nop 1" :: "v"(v));
  SB();
}
DI void drain() { asm volatile("s_waitcnt vmcnt(0)" ::: "memory"); }
DI void bar() { SB(); asm volatile("s_waitcnt lgkmcnt(0)\n\ts_barrier" ::: "memory"); SB(); }
constexpr int ord(int i) { return i < 2 ? 12 + i : i - 2; }

#define STAMP(i) do { } while (0)
#define RET(i) do { } while (0)
#define RSTAMP(i) do { } while (0)
#define DUMP(buf, a0, a1, part) do { } while (0)

DI void dual_softmax(f32x4 p0, f32x4 p1, float wtail, u32x4& fp, u32x4& fm) {
  float mx = fmax3(fmax3(p0[0], p0[1], p0[2]), fmax3(p0[3], p1[0], p1[1]), fmaxf(p1[2], p1[3]));
  float mn = -fmin3(fmin3(p0[0], p0[1], p0[2]), fmin3(p0[3], p1[0], p1[1]), fminf(p1[2], p1[3]));
  red2<true>(mx, mn);
  const float L2E = 1.4426950408889634f;
  const float a = -mx * L2E, b = -mn * L2E;
  f32x4 e0 = p0 * L2E + a, e1 = p1 * L2E + a, n0 = p0 * (-L2E) + b, n1 = p1 * (-L2E) + b;
#pragma unroll
  for (int i = 0; i < 4; ++i) {
    e0[i] = __builtin_amdgcn_exp2f(e0[i]); e1[i] = __builtin_amdgcn_exp2f(e1[i]);
    n0[i] = __builtin_amdgcn_exp2f(n0[i]); n1[i] = __builtin_amdgcn_exp2f(n1[i]);
  }
  const f32x4 se = e1 * wtail + e0, sn = n1 * wtail + n0;
  float sp = (se[0] + se[1]) + (se[2] + se[3]), sm = (sn[0] + sn[1]) + (sn[2] + sn[3]);
  red2<false>(sp, sm);
  const float rp = __builtin_amdgcn_rcpf(sp), rm = __builtin_amdgcn_rcpf(sm);
  fp = pack8(e0 * rp, e1 * rp); fm = pack8(n0 * rm, n1 * rm);
}
template <typename F, typename G> DI void gemm1(f32x4& a0, f32x4& a1, lptr b0, lptr b1, const u32x4 (&xf)[14], F pre, G post) {
  u32x4 w0[3], w1[3];
#pragma unroll
  for (int i = 0; i < 2; ++i) { w0[i] = trfrag(b0 + ord(i) * 2048); w1[i] = trfrag(b1 + ord(i) * 2048); }
  SB();
#pragma unroll
  for (int i = 0; i < 14; ++i) {
    pre(i);
    if (i + 2 < 14) { w0[(i + 2) % 3] = trfrag(b0 + ord(i + 2) * 2048); w1[(i + 2) % 3] = trfrag(b1 + ord(i + 2) * 2048); }
    SB();
    a0 = mfma(w0[i % 3], xf[i], a0);
    a1 = mfma(w1[i % 3], xf[i], a1);
    SB();
    post(i);
    SB();
  }
}
template <int N, typename I, typename F, typename G>
DI void gemm2(f32x4 (&a)[4], lptr b0, lptr b1, const u32x4 (&xf)[14], I idx, F pre, G post) {
  u32x4 wr[2][4];
  auto rdw = [&](int s, int i, int half) {
    wr[s][2 * half] = trfrag(b0 + half * SLAB + ord(idx(i)) * 2048); wr[s][2 * half + 1] = trfrag(b1 + half * SLAB + ord(idx(i)) * 2048);
  };
  rdw(0, 0, 0); rdw(0, 0, 1);
  SB();
#pragma unroll
  for (int i = 0; i < N; ++i) {
    pre(i);
    if (i + 1 < N) rdw((i + 1) & 1, i + 1, 0);
    SB();
    a[0] = mfma(wr[i & 1][0], xf[idx(i)], a[0]);
    a[1] = mfma(wr[i & 1][1], xf[idx(i)], a[1]);
    SB();
    if (i + 1 < N) rdw((i + 1) & 1, i + 1, 1);
    post(i);
    SB();
    a[2] = mfma(wr[i & 1][2], xf[idx(i)], a[2]);
    a[3] = mfma(wr[i & 1][3], xf[idx(i)], a[3]);
    SB();
  }
}
#define LA 6

__global__ void __launch_bounds__(512, 2) fused_kernel(const u16* __restrict__ x0, const u16* __restrict__ wqkvA, const u16* __restrict__ wphi,
                                                       const u16* __restrict__ woA, const u16* __restrict__ wqkvB, const u16* __restrict__ woB,
                                                       u16* __restrict__ out, unsigned char* ws) {
  extern __shared__ __attribute__((aligned(16))) unsigned char smem[];
  const lptr lds = (lptr)smem;
  const int tid = threadIdx.x, lane = tid & 63, w = __builtin_amdgcn_readfirstlane(tid >> 6);
  const int b = blockIdx.x;
  const int tb = (b & 7) * 2 + (b >> 7), h = (b >> 3) & 15;
  const int odd = h & 1, pair = h >> 1;
  const int T = tb * 8 + w;
  const int wq = w & 3, wh = w >> 2;
  const rsrc_t rX0 = __builtin_amdgcn_make_buffer_rsrc((void*)x0, 0, SEQ * DM * 2, 0x00020000);
  const rsrc_t rWA = __builtin_amdgcn_make_buffer_rsrc((void*)wqkvA, 0, DM * 3 * DM * 2, 0x00020000);
  u32x4 raw[14];
  const int xoff = (lane >> 3) * 896 + (lane & 7) * 16;
#pragma unroll
  for (int i = 0; i < 8; ++i) { raw[i] = __builtin_amdgcn_raw_buffer_load_b128(rX0, xoff + (i >> 1) * 128, (T * 16 + (i & 1) * 8) * 896, 0); SB(); }
  const int hh = lane >> 5;
  const int col = h * 56 - odd * 8 + (lane & 3) * 16;
  auto krow_l = [&](int i, int lane) {
    const int cs = 8 * i + 2 * wq + (lane >> 5);
    const int p = (cs * 43) >> 8, m = cs - 6 * p;
    const int c = i < 6 ? 7 * p + m + (m > 2) : 7 * (cs - 48) + 3;
    return 8 * c + ((lane >> 2) & 7);
  };
  auto krow = [&](int i) { return krow_l(i, lane); };
  u32x4 pA[7], pB[7], pQ[7];
#pragma unroll
  for (int n = 0; n < 4; ++n) { const int i = (n + 6) % 7; pA[i] = __builtin_amdgcn_raw_buffer_load_b128(rWA, krow(i) * 2688 + col, 896 + wh * 896, 0); SB(); }
#pragma unroll
  for (int i = 8; i < 14; ++i) { raw[i] = __builtin_amdgcn_raw_buffer_load_b128(rX0, xoff + (i >> 1) * 128, (T * 16 + (i & 1) * 8) * 896, 0); SB(); }
#pragma unroll
  for (int i = 3; i < 6; ++i) { pA[i] = __builtin_amdgcn_raw_buffer_load_b128(rWA, krow(i) * 2688 + col, 896 + wh * 896, 0); SB(); }

  const int r = lane & 15, g = lane >> 4;
  const int me = tb * 16 + h;
  const rsrc_t rWS = __builtin_amdgcn_make_buffer_rsrc((void*)ws, 0, WS_END, 0x00020000);
  const u64 epoch = *(const GAS u64*)(ws + WS_EPOCH);
  u32x2 tag;
  {
    u64 z = epoch + 0x9E3779B97F4A7C15ull;
    z = (z ^ (z >> 30)) * 0xBF58476D1CE4E5B9ull; z = (z ^ (z >> 27)) * 0x94D049BB133111EBull; z ^= z >> 31;
    tag = (u32x2){(unsigned)z, (unsigned)(z >> 32)};
    PIN(tag);
  }
  unsigned budget = SPIN_CAP;
  lptr trb0, trb1;
  {
    const int q = (lane >> 2) & 3, p = lane & 3;
    const int pc = odd ? (p < 3 ? 2 * p + 2 : 1) : 2 * p;
    const int o = (8 * g + q) * 64 + ((pc ^ (g & 1)) * 8);
    trb0 = lds + o; trb1 = lds + (o ^ 8);
  }
  const bool junk = (lane & 3) == (odd ? 0 : 3);
  auto patch = [&](u32x4& v) {
    const unsigned a = (junk && odd) ? 0u : v[0], bb = (junk && odd) ? 0u : v[1], c = (junk && !odd) ? 0u : v[2], d = (junk && !odd) ? 0u : v[3];
    v = hh ? (u32x4){c, d, a, bb} : (u32x4){a, bb, c, d};
  };
  auto put = [&](u32x4 v, unsigned slab, int i) { *(LAS u32x4*)(lds + slab + i * 4096 + (tid & 255) * 16) = v; };
  auto write_slab = [&](const u32x4 (&p)[7], unsigned slab) {
    lptr d = lds + slab + (tid & 255) * 16; PIN(d);
#pragma unroll
    for (int i = 0; i < 7; ++i) *(LAS u32x4*)(d + i * 4096) = p[i];
  };
  auto put_cs = [&](f32x4 a, int type, int part, int t) { if (r == 0) *(LAS f32x4*)(lds + L_CS + type * 384 + part * 128 + t * 64 + g * 16) = a; };
  auto colsum = [&](int type, int part, int t) {
    f32x4 a = Z4;
    const u32x4 ones = {ONES, ONES, ONES, ONES};
    lptr wb = (t ? trb1 : trb0) + part * SLAB; PIN(wb);
    u32x4 wr[7];
#pragma unroll
    for (int s = 0; s < 2; ++s) {
#pragma unroll
      for (int i = 0; i < 7; ++i) wr[i] = trfrag(wb + (7 * s + i) * 2048);
#pragma unroll
      for (int i = 0; i < 7; ++i) a = mfma(wr[i], ones, a);
    }
    put_cs(a, type, part, t);
  };
  auto push_flag = [&](int hop, unsigned voff) { if (w == 0) __builtin_amdgcn_raw_buffer_store_b64(tag, rWS, voff, WS_FLAGS + hop * HOP_B, SC1); };
  auto wait_flags = [&](int hop, bool active) {
    if (w == 0) {
      for (;;) {
        const u32x2 v = __builtin_amdgcn_raw_buffer_load_b64(rWS, (lane & 15) * 8, WS_FLAGS + hop * HOP_B + me * 128, SC1);
        asm volatile("" ::: "memory");
        if (__all(!active || (v[0] == tag[0] && v[1] == tag[1]))) break;
        if (budget == 0) break;
        --budget;
      }
    }
    bar();
  };
  auto grp_flag = [&](int hop) { int ln = lane; PIN(ln); push_flag(hop, ln < 16 ? (tb * 16 + ln) * 128 + h * 8 : OOB); };
  auto store_x = [&](u32x4 f, unsigned base) {
    int ln = lane; PIN(ln);
    st128<SC1>(f, rWS, ln * 16 + (ln < 48 ? h * 768 : 12288 - 768 + h * 256), base + T * TILE_B);
  };
  auto store_o = [&](u32x4 f, unsigned base) {
    int ln = lane; PIN(ln);
    st128<SC1>(f, rWS, ln < 48 ? ln * 16 : OOB, base + T * TILE_B + h * 768);
    __builtin_amdgcn_raw_buffer_store_b64(lo2(f), rWS, ln < 48 ? OOB : ln * 8, base + T * TILE_B + 12288 - 384 + h * 128, SC1);
  };
  auto clean = [&](int layer) {
    const u32x4 sv = {SENT, SENT, SENT, SENT};
    const unsigned mid = WS_MID + layer * MID_B;
    int ln = lane; PIN(ln);
    if ((layer & 1) == 0) __builtin_amdgcn_raw_buffer_store_b64(lo2(sv), rWS, ln * 8, mid + ((h * 16 + tb) * 8 + w) * 512, SC1);
    else {
      st128<SC1>(sv, rWS, ln * 16, mid + (h * 128 + T) * 2048);
      st128<SC1>(sv, rWS, ln * 16 + 1024, mid + (h * 128 + T) * 2048);
    }
    store_o(sv, WS_XO + (2 * layer) * XO_B);
    store_x(sv, WS_XO + (2 * layer + 1) * XO_B);
  };

  u32x4 xf[14];
  f32x4 acc[4];
  f32x4 xs0, xs1;
  float mean, rstd;
  f32x4 tc[4];
  {
    const lptr stg = lds + L_X0 + w * 8192;
    const lptr wr0 = stg + (lane >> 3) * 128 + (((lane & 7) ^ (lane >> 4)) * 16);
    const lptr wr1 = stg + 1024 + (lane >> 3) * 128 + (((lane & 7) ^ (lane >> 4) ^ 4) * 16);
    const int sw = (r >> 1) & 7;
    auto rd = [&](int c) { return *(LAS u32x4*)(stg + ((c >> 3) & 3) * 2048 + r * 128 + (((c & 7) ^ sw) * 16)); };
    auto chunk = [&](int u) {
      if (u >= 12) return 7 * (4 * (u - 12) + g) + 3;
      const int cs = 4 * u + g, p = (cs * 43) >> 8, m = cs - 6 * p;
      return 7 * p + m + (m > 2);
    };
    auto ia = [](int i) { return i == 0 ? 0 : i + 1; };
    auto ib = [](int i) { return i == 0 ? 1 : i + 7; };
    const int cown = 7 * pair + (g == 3 ? 3 : g + 4 * odd);
    float s1 = 0.f, s2 = 0.f;
#pragma unroll
    for (int i = 0; i < 8; ++i) { *(LAS u32x4*)(((i & 1) ? wr1 : wr0) + (i >> 1) * 2048) = raw[i]; SB(); }
#pragma unroll
    for (int i = 0; i < 7; ++i) xf[ia(i)] = rd(chunk(ord(ia(i))));
    u32x4 xa = rd(cown);
#pragma unroll
    for (int n = 0; n < 4; ++n) { const int i = (n + 6) % 7; patch(pA[i]); put(pA[i], L_WK + wh * SLAB, i); }
#pragma unroll
    for (int i = 0; i < 7; ++i) { _Pragma("unroll") for (int j = 0; j < 4; ++j) { const unsigned u = xf[ia(i)][j]; s1 = dot2(u, ONES, s1); s2 = dot2(u, u, s2); } }
    PIN(s1); PIN(s2); PIN(xa);
    bar();
    STAMP(1);
    u32x4 tw[7];
    {
      const rsrc_t rs = __builtin_amdgcn_make_buffer_rsrc(wh ? (void*)woA : (void*)wqkvA, 0, wh ? DM * DM * 2 : DM * 3 * DM * 2, 0x00020000);
      const int st = wh ? 896 : 2688;
#pragma unroll
      for (int i = 0; i < 7; ++i) { tw[i] = __builtin_amdgcn_raw_buffer_load_b128(rs, krow(i) * st + col, 0, 0); SB(); }
    }
    unsigned e0, e1;
    {
      const rsrc_t rPH = __builtin_amdgcn_make_buffer_rsrc((void*)wphi, 0, 16 * 28 * 28 * 2, 0x00020000);
      const int s = 8 * g + 2 * wq, d = odd ? (s < 24 ? s + 4 : s - 24) : s, f = 16 * wh + r;
      const unsigned off = s >= 28 ? OOB : ((h * 28 + d) * 28 + (f < 28 ? f : f - 4)) * 2;
      e0 = __builtin_amdgcn_raw_buffer_load_b16(rPH, off, 0, 0); e1 = __builtin_amdgcn_raw_buffer_load_b16(rPH, off + 56, 0, 0);
    }
    clean(0);
    SB();
    lptr b0 = trb0 + L_WK, b1 = trb1 + L_WK, bc = ((w & 1) ? trb1 : trb0) + L_WK + ((w >> 1) & 1) * SLAB; PIN(b0); PIN(b1); PIN(bc);
    const u32x4 ones = {ONES, ONES, ONES, ONES};
    f32x4 cacc = Z4;
    u32x4 cw;
#pragma unroll
    for (int i = 0; i < 4; ++i) acc[i] = Z4;
    gemm2<7>(acc, b0, b1, xf, ia, [&](int i) { cw = trfrag(bc + ord(ia(i)) * 2048); },
             [&](int i) { cacc = mfma(cw, ones, cacc); if (i >= 1) *(LAS u32x4*)((((i + 7) & 1) ? wr1 : wr0) + (((i + 7) >> 1) & 3) * 2048) = raw[i + 7]; });
#pragma unroll
    for (int i = 0; i < 7; ++i) xf[ib(i)] = rd(chunk(ord(ib(i))));
    if (pair >= 4) xa = rd(cown);
#pragma unroll
    for (int i = 3; i < 6; ++i) { patch(pA[i]); put(pA[i], L_WK + wh * SLAB, i); }
#pragma unroll
    for (int i = 0; i < 7; ++i) { _Pragma("unroll") for (int j = 0; j < 4; ++j) { const unsigned u = xf[ib(i)][j]; s1 = dot2(u, ONES, s1); s2 = dot2(u, u, s2); } }
    if (g == 3 && odd) { xa[0] = xa[2]; xa[1] = xa[3]; }
    xs0 = (f32x4){bflo(xa[0]), bfhi(xa[0]), bflo(xa[1]), bfhi(xa[1])};
    xs1 = (f32x4){bflo(xa[2]), bfhi(xa[2]), bflo(xa[3]), bfhi(xa[3])};
    red2<false>(s1, s2);
    mean = s1 * (1.0f / DM);
    rstd = __builtin_amdgcn_rsqf(fmaxf(s2 * (1.0f / DM) - mean * mean, 0.f) + 1e-5f);
    PIN(mean); PIN(rstd); PIN(xs0); PIN(xs1);
    bar();
    STAMP(2);
    gemm2<7>(acc, b0, b1, xf, ib, [&](int i) { cw = trfrag(bc + ord(ib(i)) * 2048); }, [&](int i) { cacc = mfma(cw, ones, cacc); });
    put_cs(cacc, 0, 1 + ((w >> 1) & 1), w & 1);
#pragma unroll
    for (int i = 0; i < 7; ++i) { patch(tw[i]); pQ[i] = tw[i]; }
    write_slab(tw, wh ? L_WO : L_WQ);
    *(LAS unsigned*)(lds + L_WPH + wh * 1024 + lane * 16 + wq * 4) = e0 | (e1 << 16);
    drain();
    bar();
    const float c = -mean * rstd;
#pragma unroll
    for (int i = 0; i < 4; ++i) tc[i] = *(LAS f32x4*)(lds + L_CS + 128 + i * 64 + g * 16) * c;
    if (w < 2) colsum(0, 0, w);
    const rsrc_t rWB = __builtin_amdgcn_make_buffer_rsrc((void*)wqkvB, 0, DM * 3 * DM * 2, 0x00020000);
#pragma unroll
    for (int i = 0; i < 7; ++i) { pB[i] = __builtin_amdgcn_raw_buffer_load_b128(rWB, krow(i) * 2688 + col, 896 + wh * 896, 0); SB(); }
  }
  u32x2 idn;
  idn[0] = (4 * g == r ? 0x3f80u : 0u) | (4 * g + 1 == r ? 0x3f800000u : 0u);
  idn[1] = (4 * g + 2 == r ? 0x3f80u : 0u) | (4 * g + 3 == r ? 0x3f800000u : 0u);
  const float wtail = g == 3 ? 0.f : 1.f;

  for (int layer = 0; layer < NL; ++layer) {
    const int type = layer & 1;
    const int pb = 2 + layer * 14;
    if (layer > 0) {
      wait_flags(3 * layer - 1, true);
      STAMP(pb + 0);
      for (;;) {
        const unsigned base = WS_XO + (2 * layer - 1) * XO_B + T * TILE_B;
        int v16 = lane * 16; PIN(v16);
        u32x4 sp[4];
        const int vs = v16 + (v16 & 0x300);
#pragma unroll
        for (int u = 0; u < 4; ++u) { sp[u] = __builtin_amdgcn_raw_buffer_load_b128(rWS, vs + (u >> 1) * 2048 + (u & 1) * 256, base + 12288, SC1); SB(); }
        auto ld = [&](int s) { xf[2 + s] = __builtin_amdgcn_raw_buffer_load_b128(rWS, v16 + (s & 3) * 1024, base + (s >> 2) * 4096, SC1); SB(); };
#pragma unroll
        for (int s = 0; s < LA - 2; ++s) ld(s);
        unsigned m = 0;
        lptr b0 = trb0 + L_WK, b1 = trb1 + L_WK; PIN(b0); PIN(b1);
#pragma unroll
        for (int i = 0; i < 4; ++i) acc[i] = Z4;
        gemm2<14>(acc, b0, b1, xf, [](int i) { return i; },
                  [&](int i) {
                    if (i + LA < 14) ld(i + LA - 2);
                    if (i < 2) { PIN(sp[2 * i]); PIN(sp[2 * i + 1]); xf[i] = cat(lo2(sp[2 * i]), lo2(sp[2 * i + 1])); }
                  },
                  [&](int i) {
                    if (i < 2) m = max4(max4(m, sp[2 * i]), sp[2 * i + 1]); else m = max4(m, xf[i]);
                    PIN(m);
                    if (i == 2) {
                      float s1 = (U2F(sp[0][2]) + U2F(sp[1][2])) + (U2F(sp[2][2]) + U2F(sp[3][2]));
                      float s2 = (U2F(sp[0][3]) + U2F(sp[1][3])) + (U2F(sp[2][3]) + U2F(sp[3][3]));
                      red2<false>(s1, s2);
                      mean = s1 * (1.0f / DM);
                      rstd = __builtin_amdgcn_rsqf(fmaxf(s2 * (1.0f / DM) - mean * mean, 0.f) + 1e-5f);
                      PIN(mean); PIN(rstd);
                    }
                    if (i == 3) {
                      lptr cs = lds + L_CS + type * 384 + 128 + (v16 >> 8) * 16;
#pragma unroll
                      for (int j = 0; j < 4; ++j) tc[j] = *(LAS f32x4*)(cs + j * 64);
                    }
                    if (i >= 5 && i < 9) {
                      const float c = -mean * rstd;
#pragma unroll
                      for (int j = 0; j < 4; ++j) { float t = tc[i - 5][j] * c; PIN(t); tc[i - 5][j] = t; }
                    }
                  });
        if (!__any(m == SENT) || budget == 0) break;
        --budget; RET(layer * 3);
        asm volatile("" ::: "memory");
      }
    }
    if (layer > 0) drain();
    STAMP(pb + 1);
    const unsigned mid = WS_MID + layer * MID_B;
    int ln16 = lane * 16; PIN(ln16);
    const f32x4 v0 = acc[2] * rstd + tc[2];
    f32x4 v1 = acc[3] * rstd + tc[3];
    DUMP(0, v0, v1, 2);
    if (g == 3) v1[0] = 1.0f;
    const u32x2 vh0 = pack4(mfma16(pack4(v0), idn, Z4)), vh1 = pack4(mfma16(pack4(v1), idn, Z4));
    const f32x4 k0 = acc[0] * rstd + tc[0], k1 = acc[1] * rstd + tc[1];
    DUMP(0, k0, k1, 1);
    const u32x4 kf = pack8(k0, k1);
    if (type == 0) {
      const u32x4 wp0 = *(LAS u32x4*)(lds + L_WPH + ln16), wp1 = *(LAS u32x4*)(lds + L_WPH + 1024 + ln16);
      u32x4 fp, fm;
      dual_softmax(mfma(wp0, kf, Z4), mfma(wp1, kf, Z4), wtail, fp, fm);
      const u32x2 idm = r < 12 ? idn : (u32x2){0u, 0u};
      const u32x2 t0 = pack4(mfma16(lo2(fp), idn, Z4)), t1 = pack4(mfma16(hi2(fp), idm, Z4));
      const u32x2 t2 = pack4(mfma16(lo2(fm), idn, Z4)), t3 = pack4(mfma16(hi2(fm), idm, Z4));
      bar();
      const lptr d = lds + L_SCR + (w >> 1) * 1024 + ln16 + (w & 1) * 8;
      *(LAS u32x2*)(d) = t0; *(LAS u32x2*)(d + 4096) = t1; *(LAS u32x2*)(d + 8192) = t2; *(LAS u32x2*)(d + 12288) = t3;
      *(LAS u32x2*)(d + 16384) = vh0; *(LAS u32x2*)(d + 20480) = vh1;
      bar();
      { int ln = lane; PIN(ln); push_flag(3 * layer, ln < 16 ? (ln * 16 + h) * 128 + tb * 8 : OOB); }
      const lptr pa = lds + L_SCR + (w >> 1) * 4096 + ln16, pv = lds + L_SCR + 16384 + (w & 1) * 4096 + ln16;
      u32x4 fa[4], fb[4];
#pragma unroll
      for (int m = 0; m < 4; ++m) { fa[m] = *(LAS u32x4*)(pa + m * 1024); fb[m] = *(LAS u32x4*)(pv + m * 1024); }
      f32x4 kv = Z4;
#pragma unroll
      for (int m = 0; m < 4; ++m) kv = mfma(fa[m], fb[m], kv);
      __builtin_amdgcn_raw_buffer_store_b64(pack4(kv), rWS, ln16 >> 1, mid + ((h * 16 + tb) * 8 + w) * 512, SC1);
    } else {
      bar();
      { int ln = lane; PIN(ln); push_flag(3 * layer, (ln < 2 && tb + 1 + ln < 16) ? ((tb + 1 + ln) * 16 + h) * 128 + ln * 8 : OOB); }
      const unsigned ga = mid + (h * 128 + T) * 2048;
      st128<SC1>(kf, rWS, ln16, ga);
      st128<SC1>(cat(vh0, vh1), rWS, ln16 + 1024, ga);
      *(LAS u32x4*)(lds + L_SCR + (16 + w) * 1024 + ln16) = kf;
      const lptr d = lds + L_SCR + 24576 + (8 + (w >> 1)) * 2048 + ln16 + (w & 1) * 8;
      *(LAS u32x2*)(d) = vh0; *(LAS u32x2*)(d + 1024) = vh1;
    }
    STAMP(pb + 2);
    u32x4 qf;
    {
      const lptr cs = lds + L_CS + type * 384 + (ln16 >> 8) * 16;
      f32x4 q0 = *(LAS f32x4*)(cs) * (-mean), q1 = *(LAS f32x4*)(cs + 64) * (-mean);
      gemm1(q0, q1, trb0 + L_WQ, trb1 + L_WQ, xf, [](int) {}, [](int) {});
      const float sc = type ? rstd * (0.18898223650461361f * 1.4426950408889634f) : rstd;
      q0 *= sc; q1 *= sc;
      DUMP(0, q0, q1, 0);
      if (g == 3) q1[0] = 1.0f;
      qf = pack8(q0, q1);
    }
    f32x4 o0, o1;
    if (type == 0) {
      const u32x4 wp0 = *(LAS u32x4*)(lds + L_WPH + ln16), wp1 = *(LAS u32x4*)(lds + L_WPH + 1024 + ln16);
      u32x4 fp, fm;
      dual_softmax(mfma(wp0, qf, Z4), mfma(wp1, qf, Z4), wtail, fp, fm);
      PIN(fp); PIN(fm);
      STAMP(pb + 3);
      wait_flags(3 * layer, true);
      STAMP(pb + 4);
      f32x4 sum;
      for (;;) {
        u32x2 pr[16];
        const unsigned ga = mid + (h * 128 + w) * 512;
#pragma unroll
        for (int i = 0; i < 16; ++i) { pr[i] = __builtin_amdgcn_raw_buffer_load_b64(rWS, ln16 >> 1, ga + i * 4096, SC1); SB(); }
        sum = Z4;
        unsigned m = 0;
#pragma unroll
        for (int i = 0; i < 16; ++i) { sum = mfma16(idn, pr[i], sum); m = umax3(m, pr[i][0], pr[i][1]); PIN(m); SB(); }
        if (!__any(m == SENT) || budget == 0) break;
        --budget; RET(layer * 3 + 1);
        asm volatile("" ::: "memory");
      }
      *(LAS u32x2*)(lds + L_KVF + wh * 2048 + (w & 1) * 1024 + ln16 + ((w >> 1) & 1) * 8) = pack4(sum);
      STAMP(pb + 5);
      bar();
      const lptr kvf = lds + L_KVF + ln16;
      const u32x4 a0 = *(LAS u32x4*)(kvf), a1 = *(LAS u32x4*)(kvf + 1024), a2 = *(LAS u32x4*)(kvf + 2048), a3 = *(LAS u32x4*)(kvf + 3072);
      grp_flag(3 * layer + 1);
      SB();
      o1 = mfma(a3, fm, mfma(a1, fp, Z4));
      o0 = mfma(a2, fm, mfma(a0, fp, Z4));
      const float rd = __builtin_amdgcn_rcpf(row3_val(o1[0]) + 1e-6f);
      o0 *= rd; o1 *= rd;
    } else {
      PIN(qf);
      STAMP(pb + 3);
      wait_flags(3 * layer, (lane & 15) < 2 && tb - 1 - (lane & 15) >= 0);
      STAMP(pb + 4);
      {
        u32x4 t[4];
        if (tb - 2 + wh >= 0) {
          const unsigned ga = mid + (h * 128 + tb * 8 - 16 + 2 * w) * 2048;
          for (;;) {
#pragma unroll
            for (int i = 0; i < 4; ++i) t[i] = __builtin_amdgcn_raw_buffer_load_b128(rWS, ln16 + i * 1024, ga, SC1);
            const unsigned m = max4(max4(max4(max4(0u, t[0]), t[1]), t[2]), t[3]);
            if (!__any(m == SENT) || budget == 0) break;
            --budget; RET(layer * 3 + 1);
            asm volatile("" ::: "memory");
          }
        } else {
          t[0] = t[2] = (u32x4){0u, 0u, g == 3 ? 0xff80u : 0u, 0u};
          t[1] = t[3] = (u32x4){0u, 0u, 0u, 0u};
        }
        const lptr dk = lds + L_SCR + 2 * w * 1024 + ln16, dv = lds + L_SCR + 24576 + w * 2048 + ln16;
        *(LAS u32x4*)(dk) = t[0]; *(LAS u32x4*)(dk + 1024) = t[2];
        *(LAS u32x4*)(dv) = cat(lo2(t[1]), lo2(t[3])); *(LAS u32x4*)(dv + 1024) = cat(hi2(t[1]), hi2(t[3]));
      }
      STAMP(pb + 5);
      bar();
      if (layer < NL - 1) clean(layer + 1);
      lptr kp = lds + L_SCR + (w & ~1) * 1024 + ln16, vp = lds + L_SCR + 24576 + (w >> 1) * 2048 + ln16; PIN(kp); PIN(vp);
      f32x4 sc[18];
      u32x4 fr[6];
      auto tj = [](int n) { return n < 2 ? 16 + n : n - 2; };
#pragma unroll
      for (int n = 0; n < 5; ++n) fr[n] = *(LAS u32x4*)(kp + tj(n) * 1024);
      SB();
      int lim = (lane & 15) + 16 * (w & 1) - 4 * (lane >> 4); PIN(lim);
      float m0 = -__builtin_inff();
#pragma unroll
      for (int n = 0; n < 22; ++n) {
        if (n + 5 < 18) fr[(n + 5) % 6] = *(LAS u32x4*)(kp + tj(n + 5) * 1024);
        else if (n + 5 < 22) fr[(n + 5) % 6] = *(LAS u32x4*)(vp + 2048 + (n - 13) * 1024);
        SB();
        if (n < 18) sc[tj(n)] = mfma(fr[n % 6], qf, Z4);
        SB();
        if (n == 6 || n == 7) {
#pragma unroll
          for (int i = 0; i < 4; ++i) sc[n - 6][i] = (16 * (n - 6) + i <= lim) ? sc[n + 10][i] : sc[n - 6][i];
        }
        if (n >= 6) { m0 = fmax3(fmax3(m0, sc[n - 6][0], sc[n - 6][1]), sc[n - 6][2], sc[n - 6][3]); PIN(m0); }
        SB();
      }
      const float m = redmax(m0);
      o0 = Z4; o1 = Z4;
#pragma unroll
      for (int j = 1; j < 8; ++j) {
        const int nx = j + 2 < 8 ? j + 2 : (j + 2 == 8 ? 0 : 8);
        fr[(2 * j + 2) % 6] = *(LAS u32x4*)(vp + nx * 2048); fr[(2 * j + 3) % 6] = *(LAS u32x4*)(vp + nx * 2048 + 1024);
        f32x4 a = sc[2 * j] - m, c = sc[2 * j + 1] - m;
#pragma unroll
        for (int i = 0; i < 4; ++i) { a[i] = __builtin_amdgcn_exp2f(a[i]); c[i] = __builtin_amdgcn_exp2f(c[i]); }
        const u32x4 p = pack8(a, c);
        SB();
        o0 = mfma(fr[(2 * j - 2) % 6], p, o0);
        o1 = mfma(fr[(2 * j - 1) % 6], p, o1);
        SB();
      }
      {
        f32x4 a = sc[0] - m, c = sc[1] - m;
#pragma unroll
        for (int i = 0; i < 4; ++i) { a[i] = __builtin_amdgcn_exp2f(a[i]); c[i] = __builtin_amdgcn_exp2f(c[i]); }
        const u32x4 p = pack8(a, c);
        u32x4 mk;
#pragma unroll
        for (int i = 0; i < 4; ++i) { const int k0 = 16 * (i >> 1) + 2 * (i & 1); mk[i] = (k0 <= lim ? 0xffffu : 0u) | (k0 + 1 <= lim ? 0xffff0000u : 0u); }
        bar();
        grp_flag(3 * layer + 1);
        SB();
        o1 = mfma(fr[3], p & ~mk, o1);
        o1 = mfma(fr[5], p & mk, o1);
        o0 = mfma(fr[2], p & ~mk, o0);
        o0 = mfma(fr[4], p & mk, o0);
      }
      const float rd = __builtin_amdgcn_rcpf(row3_val(o1[0]));
      o0 *= rd; o1 *= rd;
    }
    DUMP(1, o0, o1, 0);
    store_o(pack8(o0, o1), WS_XO + (2 * layer) * XO_B);
    STAMP(pb + 6);
    if (layer == 0) {
#pragma unroll
      for (int i = 0; i < 7; ++i) patch(pB[i]);
    }
    if (type == 0) write_slab(pB, L_WK + wh * SLAB); else write_slab(pA, L_WK + wh * SLAB);
    if (layer > 0 && wh != type) write_slab(pQ, L_WQ);
    STAMP(pb + 7);
    wait_flags(3 * layer + 1, true);
    STAMP(pb + 8);
    f32x4 x0n, x1n;
    for (;;) {
      const unsigned base = WS_XO + (2 * layer) * XO_B + T * TILE_B;
      int v16 = lane * 16; PIN(v16);
      u32x2 sp[4];
      const int vs = (v16 >> 1) + ((v16 >> 1) & 0x180);
#pragma unroll
      for (int u = 0; u < 4; ++u) { sp[u] = __builtin_amdgcn_raw_buffer_load_b64(rWS, vs + (u >> 1) * 1024 + (u & 1) * 128, base + 12288, SC1); SB(); }
      auto ld = [&](int s) { xf[2 + s] = __builtin_amdgcn_raw_buffer_load_b128(rWS, v16 + (s & 3) * 1024, base + (s >> 2) * 4096, SC1); SB(); };
#pragma unroll
      for (int s = 0; s < LA - 1; ++s) ld(s);
      xf[0] = cat(sp[0], sp[1]); xf[1] = cat(sp[2], sp[3]);
      x0n = xs0; x1n = xs1;
      unsigned m = 0;
      lptr b0 = trb0 + L_WO + type * SLAB, b1 = trb1 + L_WO + type * SLAB; PIN(b0); PIN(b1);
      gemm1(x0n, x1n, b0, b1, xf, [&](int i) { if (i + LA + 1 < 14) ld(i + LA - 1); }, [&](int i) { m = max4(m, xf[i]); PIN(m); });
      if (!__any(m == SENT) || budget == 0) break;
      --budget; RET(layer * 3 + 2);
      asm volatile("" ::: "memory");
    }
    xs0 = x0n; xs1 = x1n;
    STAMP(pb + 9);
    if (layer == NL - 1) {
      const rsrc_t rOUT = __builtin_amdgcn_make_buffer_rsrc((void*)out, 0, SEQ * DM * 2, 0x00020000);
      const u32x4 f = pack8(xs0, xs1);
      int ln = lane; PIN(ln);
      const int row = (ln & 15) * 896;
      st128<0>(f, rOUT, ln < 48 ? row + (ln & 48) : OOB, T * 16 * 896 + (7 * pair + 4 * odd) * 16);
      __builtin_amdgcn_raw_buffer_store_b64(lo2(f), rOUT, ln < 48 ? OOB : row, T * 16 * 896 + (7 * pair + 3) * 16 + odd * 8, 0);
    } else {
      bar();
      grp_flag(3 * layer + 2);
      SB();
      u32x4 f = pack8(xs0, xs1);
      const unsigned f2 = g == 3 ? 0u : f[2], f3 = g == 3 ? 0u : f[3];
      float s1 = dot2(f[1], ONES, dot2(f[0], ONES, 0.f)) + dot2(f3, ONES, dot2(f2, ONES, 0.f));
      float s2 = dot2(f[1], f[1], dot2(f[0], f[0], 0.f)) + dot2(f3, f3, dot2(f2, f2, 0.f));
      red2<false>(s1, s2);
      if (g == 3) { f[2] = F2U(s1); f[3] = F2U(s2); }
      store_x(f, WS_XO + (2 * layer + 1) * XO_B);
      STAMP(pb + 10);
      if (type == 0) clean(layer + 1);
      if (layer == 0) {
        u32x4 tw[7];
        const rsrc_t rs = __builtin_amdgcn_make_buffer_rsrc(wh ? (void*)wqkvB : (void*)woB, 0, wh ? DM * 3 * DM * 2 : DM * DM * 2, 0x00020000);
        const int st = wh ? 2688 : 896;
        int ln = lane; PIN(ln);
#pragma unroll
        for (int i = 0; i < 7; ++i) { tw[i] = __builtin_amdgcn_raw_buffer_load_b128(rs, krow_l(i, ln) * st + (ln & 3) * 16, h * 56 - odd * 8, 0); SB(); }
        if (w >= 4) colsum(1, 1 + ((w >> 1) & 1), w & 1);
#pragma unroll
        for (int i = 0; i < 7; ++i) { patch(tw[i]); if (wh) pQ[i] = tw[i]; }
        write_slab(tw, wh ? L_WQ : L_WO + SLAB);
        bar();
        if (w >= 6) colsum(1, 0, w & 1);
      }
    }
    asm volatile("" : "=v"(acc[0]), "=v"(acc[1]), "=v"(acc[2]), "=v"(acc[3]), "=v"(tc[0]), "=v"(tc[1]), "=v"(tc[2]), "=v"(tc[3]), "=v"(mean), "=v"(rstd));
  }
  STAMP(61); RSTAMP(63);
  if (b == 0 && w == 0) __builtin_amdgcn_raw_buffer_store_b64((u32x2){(unsigned)(epoch + 1), (unsigned)((epoch + 1) >> 32)}, rWS, WS_EPOCH, 0, SC1);
}
}

extern "C" void kernel_launch(void* const* d_in, const int* in_sizes, int n_in, void* d_out, int out_size, void* d_ws, size_t ws_size, hipStream_t stream) {
  (void)hipFuncSetAttribute((const void*)fused_kernel, hipFuncAttributeMaxDynamicSharedMemorySize, L_END);
  fused_kernel<<<256, 512, L_END, stream>>>((const u16*)d_in[0], (const u16*)d_in[1], (const u16*)d_in[2], (const u16*)d_in[3],
                                            (const u16*)d_in[5], (const u16*)d_in[6], (u16*)d_out, (unsigned char*)d_ws);
}
```

```cpp
#define NL 4
#include <hip/hip_runtime.h>
#include <hip/hip_bf16.h>

typedef unsigned short u16;
typedef unsigned long long u64;
typedef __bf16 bf16x8 __attribute__((ext_vector_type(8)));
typedef __bf16 bf16x4 __attribute__((ext_vector_type(4)));
typedef __bf16 bf16x2 __attribute__((ext_vector_type(2)));
typedef float f32x16 __attribute__((ext_vector_type(16)));
typedef float f32x4 __attribute__((ext_vector_type(4)));
typedef float f32x2 __attribute__((ext_vector_type(2)));
typedef unsigned u32x4 __attribute__((ext_vector_type(4)));
typedef unsigned u32x2 __attribute__((ext_vector_type(2)));
#define LAS __attribute__((address_space(3)))
#define GAS __attribute__((address_space(1)))
#define DI __device__ __forceinline__
#define SB() __builtin_amdgcn_sched_barrier(0)
#define PIN(x) asm volatile("" : "+v"(x))
typedef __amdgpu_buffer_rsrc_t rsrc_t;
typedef LAS unsigned char* lptr;

namespace {
constexpr int SEQ = 2048, DM = 448;
constexpr int SC1 = 16;
constexpr unsigned WS_EPOCH = 0;
constexpr unsigned WS_FLAGS = 4096;
constexpr unsigned HOP_B = 256 * 128;
constexpr unsigned TILE_B = 32768;
constexpr unsigned XO_B = 4 * TILE_B;
constexpr unsigned WS_XO = 1u << 20;
constexpr unsigned MID_B = 64 * 4096;
constexpr unsigned WS_MID = WS_XO + 16 * 8 * XO_B;
constexpr unsigned WS_PROF = WS_MID + 64 * MID_B;
constexpr unsigned WS_END = WS_PROF + 256 * 2048;
constexpr unsigned SLAB = 448 * 64;
constexpr unsigned L_WK = 0, L_WV = SLAB, L_WQ = 2 * SLAB, L_WO = 3 * SLAB;
constexpr unsigned L_SCR = 0;
constexpr unsigned L_X0 = SLAB;
constexpr unsigned L_KVF = 5 * SLAB;
constexpr unsigned L_MK = L_KVF + 4096;
constexpr unsigned L_WPH = L_MK + 2048;
constexpr unsigned L_CS = L_WPH + 2048;
constexpr unsigned L_SYNC = L_CS + 6144;
constexpr unsigned L_PROF = L_SYNC + 64;
constexpr unsigned L_END = L_PROF + 2048;
static_assert(L_END <= 163840 && L_X0 + 4 * 29184 <= L_MK, "lds");
constexpr unsigned SPIN_CAP = 1u << 15;
constexpr unsigned SENT = 0xffffffffu;
constexpr unsigned OOB = 0x7ff00000u;
constexpr unsigned ONES = 0x3f803f80u;
constexpr unsigned CODE_B = 32 * 1024;
constexpr float L2E = 1.4426950408889634f;

DI f32x16 mfma(u32x4 a, u32x4 b, f32x16 c) {
  return __builtin_amdgcn_mfma_f32_32x32x16_bf16(__builtin_bit_cast(bf16x8, a), __builtin_bit_cast(bf16x8, b), c, 0, 0, 0);
}
DI unsigned pk2(float a, float b) { f32x2 t = {a, b}; return __builtin_bit_cast(unsigned, __builtin_convertvector(t, bf16x2)); }
template <int S_> DI u32x4 pack8(const f32x16& c) {
  return (u32x4){pk2(c[8 * S_], c[8 * S_ + 1]), pk2(c[8 * S_ + 2], c[8 * S_ + 3]), pk2(c[8 * S_ + 4], c[8 * S_ + 5]), pk2(c[8 * S_ + 6], c[8 * S_ + 7])};
}
DI u32x4 cat(u32x2 a, u32x2 b) { return (u32x4){a[0], a[1], b[0], b[1]}; }
DI u32x2 lo2(u32x4 v) { return (u32x2){v[0], v[1]}; }
DI unsigned umax3(unsigned a, unsigned b, unsigned c) { return max(max(a, b), c); }
DI unsigned max4(unsigned m, u32x4 v) { return umax3(umax3(m, v[0], v[1]), v[2], v[3]); }
DI unsigned add4(unsigned m, u32x4 v) { return (m + v[0] + v[1]) + v[2] + v[3]; }
DI unsigned xhalf_addu(unsigned v) { auto r = __builtin_amdgcn_permlane32_swap(v, v, false, false); return r[0] + r[1]; }
DI float fmax3(float a, float b, float c) { return fmaxf(fmaxf(a, b), c); }
template <int CTRL> DI float dppadd(float v) { return v + __builtin_bit_cast(float, __builtin_amdgcn_update_dpp(0, __builtin_bit_cast(int, v), CTRL, 0xf, 0xf, true)); }
DI float fmin3(float a, float b, float c) { return fminf(fminf(a, b), c); }
DI f32x16 splat(float v) { f32x16 z; _Pragma("unroll") for (int i = 0; i < 16; ++i) z[i] = v; return z; }
#define Z16 splat(0.f)
DI u32x4 trfrag(lptr p) {
  bf16x4 a0 = __builtin_amdgcn_ds_read_tr16_b64_v4bf16((LAS bf16x4*)p);
  bf16x4 a1 = __builtin_amdgcn_ds_read_tr16_b64_v4bf16((LAS bf16x4*)(p + 256));
  return __builtin_bit_cast(u32x4, __builtin_shufflevector(a0, a1, 0, 1, 2, 3, 4, 5, 6, 7));
}
DI u32x4 ldl(lptr p) { return *(LAS u32x4*)p; }
DI float U2F(unsigned u) { return __uint_as_float(u); }
DI unsigned F2U(float f) { return __float_as_uint(f); }
template <bool MX> DI void red2(float& a, float& b) {
  auto r = __builtin_amdgcn_permlane32_swap(F2U(a), F2U(b), false, false);
  const float t = MX ? fmaxf(U2F(r[0]), U2F(r[1])) : U2F(r[0]) + U2F(r[1]);
  r = __builtin_amdgcn_permlane32_swap(F2U(t), F2U(t), false, false);
  a = U2F(r[0]); b = U2F(r[1]);
}
DI float xhalf_max(float v) { auto r = __builtin_amdgcn_permlane32_swap(F2U(v), F2U(v), false, false); return fmaxf(U2F(r[0]), U2F(r[1])); }
DI float upper_val(float v) { auto r = __builtin_amdgcn_permlane32_swap(F2U(v), F2U(v), false, false); return U2F(r[1]); }
DI float dot2(unsigned u, unsigned v, float c) { return __builtin_amdgcn_fdot2_f32_bf16(__builtin_bit_cast(bf16x2, u), __builtin_bit_cast(bf16x2, v), c, false); }
DI float bflo(unsigned u) { return U2F(u << 16); }
DI float bfhi(unsigned u) { return U2F(u & 0xffff0000u); }
DI void split2(float v, unsigned& hi, unsigned& lo) { hi = pk2(v, v) & 0xffffu; lo = pk2(v - bflo(hi), 0.f) & 0xffffu; }
template <int AUX> DI void st128(u32x4 v, rsrc_t rs, unsigned voff, unsigned soff) {
  SB();
  __builtin_amdgcn_raw_buffer_store_b128(v, rs, voff, soff, AUX);
  SB();
  asm volatile("s_nop 1" :: "v"(v));
  SB();
}
DI void drain() { asm volatile("s_waitcnt vmcnt(0)" ::: "memory"); }
DI void bar() { SB(); asm volatile("s_waitcnt lgkmcnt(0)\n\ts_barrier" ::: "memory"); SB(); }
constexpr int chunk_of(int s, int hh) { return s < 24 ? 7 * (s / 3) + (s % 3 == 0 ? hh : (s % 3 == 1 ? 2 + 2 * hh : 5 + hh)) : 7 * (2 * (s - 24) + hh) + 3; }
constexpr int ord(int i) { return i < 4 ? 24 + i : i - 4; }

#define STAMP(i) do { } while (0)
#define RET(i) do { } while (0)
#define RSTAMP(i) do { } while (0)
#define DUMP(buf, acc, part, sc) do { } while (0)

DI void dual_softmax(const f32x16& p, float c, float wtail, u32x4 (&fp)[2], u32x4 (&fm)[2]) {
  float mx = fmax3(p[0], p[1], p[2]), mn = fmax3(-p[0], -p[1], -p[2]);
#pragma unroll
  for (int i = 3; i < 15; i += 2) { mx = fmax3(mx, p[i], p[i + 1]); mn = fmax3(mn, -p[i], -p[i + 1]); }
  mx = fmax3(mx, p[15], p[15]); mn = fmax3(mn, -p[15], -p[15]);
  red2<true>(mx, mn);
  const float a = -c * mx, b = -c * mn;
  float nc = -c; PIN(nc);
  f32x16 ep = p * c + a, em = p * nc + b;
  SB();
#pragma unroll
  for (int i = 0; i < 16; ++i) ep[i] = __builtin_amdgcn_exp2f(ep[i]);
#pragma unroll
  for (int i = 0; i < 16; ++i) em[i] = __builtin_amdgcn_exp2f(em[i]);
  f32x2 sp = {ep[0], ep[1]}, sm = {em[0], em[1]};
#pragma unroll
  for (int i = 2; i < 12; i += 2) { sp += (f32x2){ep[i], ep[i + 1]}; sm += (f32x2){em[i], em[i + 1]}; }
  sp += wtail * ((f32x2){ep[12], ep[13]} + (f32x2){ep[14], ep[15]});
  sm += wtail * ((f32x2){em[12], em[13]} + (f32x2){em[14], em[15]});
  float u = sp[0] + sp[1], v = sm[0] + sm[1];
  red2<false>(u, v);
  ep *= __builtin_amdgcn_rcpf(u); em *= __builtin_amdgcn_rcpf(v);
  fp[0] = pack8<0>(ep); fp[1] = pack8<1>(ep); fm[0] = pack8<0>(em); fm[1] = pack8<1>(em);
}
DI u32x4 ident_frag(int s, int r, int hh) {
  const unsigned val = 0x3f80u << (16 * (r & 1));
  const int idx = (((r >> 2) ^ hh) & 1) * 8 + (r >> 4) * 4 + ((r >> 2) & 2) + ((r >> 1) & 1) - 4 * s;
  return (u32x4){idx == 0 ? val : 0u, idx == 1 ? val : 0u, idx == 2 ? val : 0u, idx == 3 ? val : 0u};
}
template <int PD, typename F, typename G> DI void gemm1(f32x16& acc, lptr wb, const u32x4 (&xf)[28], F pre, G post) {
  u32x4 wr[PD];
#pragma unroll
  for (int i = 0; i < PD - 1; ++i) wr[i] = trfrag(wb + ord(i) * 1024);
  SB();
#pragma unroll
  for (int i = 0; i < 28; ++i) {
    pre(i);
    if (i + PD - 1 < 28) wr[(i + PD - 1) % PD] = trfrag(wb + ord(i + PD - 1) * 1024);
    SB();
    acc = mfma(wr[i % PD], xf[i], acc);
    SB();
    post(i);
    SB();
  }
}
template <int N, typename I, typename F, typename G>
DI void gemm2(f32x16& ak, f32x16& av, lptr bk, const u32x4 (&xf)[28], I idx, F pre, G post) {
  u32x4 wk[3], wv[3];
  wv[0] = trfrag(bk + SLAB + ord(idx(0)) * 1024); wk[0] = trfrag(bk + ord(idx(0)) * 1024);
  wv[1] = trfrag(bk + SLAB + ord(idx(1)) * 1024); wk[1] = trfrag(bk + ord(idx(1)) * 1024);
  SB();
#pragma unroll
  for (int i = 0; i < N; ++i) {
    pre(i);
    if (i + 2 < N) wv[(i + 2) % 3] = trfrag(bk + SLAB + ord(idx(i + 2)) * 1024);
    SB();
    av = mfma(wv[i % 3], xf[idx(i)], av);
    SB();
    if (i + 2 < N) wk[(i + 2) % 3] = trfrag(bk + ord(idx(i + 2)) * 1024);
    post(i);
    SB();
    ak = mfma(wk[i % 3], xf[idx(i)], ak);
    SB();
  }
}
#define LA 11
#define XH 27
#define VSLEEP 8
#define QSLEEP 28
#define PDELAY 4

__global__ void __launch_bounds__(512, 2) fused_kernel(const u16* __restrict__ x0, const u16* __restrict__ wqkvA, const u16* __restrict__ wphi,
                                                       const u16* __restrict__ woA, const u16* __restrict__ wqkvB, const u16* __restrict__ woB,
                                                       u16* __restrict__ out, unsigned char* ws) {
  extern __shared__ __attribute__((aligned(16))) unsigned char smem[];
  const lptr lds = (lptr)smem;
  const int tid = threadIdx.x, lane = tid & 63, wave = __builtin_amdgcn_readfirstlane(tid >> 6);
  const int w = wave & 3;
  const int r = lane & 31, hh = lane >> 5;
  const int b = blockIdx.x;
  const int tb = ((b >> 1) & 3) * 4 + (b >> 6), h = (b & 1) * 8 + ((b >> 3) & 7);
  const int me = tb * 16 + h;
  const int odd = h & 1, pair = h >> 1;
  const int T = tb * 4 + w;
  const unsigned TOFF = tb * 8 * XO_B + w * TILE_B;
  const rsrc_t rWS = __builtin_amdgcn_make_buffer_rsrc((void*)ws, 0, WS_END, 0x00020000);
  unsigned budget = SPIN_CAP;
  u64 epoch;
  u32x2 tag;
  u32x2 ev;
  auto load_epoch = [&]() { ev = __builtin_amdgcn_raw_buffer_load_b64(rWS, 0, WS_EPOCH, SC1); SB(); };
  auto make_tag = [&]() {
    epoch = (u64)(unsigned)__builtin_amdgcn_readfirstlane(ev[0]) | ((u64)(unsigned)__builtin_amdgcn_readfirstlane(ev[1]) << 32);
    u64 z = epoch + 0x9E3779B97F4A7C15ull;
    z = (z ^ (z >> 30)) * 0xBF58476D1CE4E5B9ull; z = (z ^ (z >> 27)) * 0x94D049BB133111EBull; z ^= z >> 31;
    unsigned t0 = (unsigned)z, t1 = (unsigned)(z >> 32);
    asm volatile("" : "+s"(t0), "+s"(t1));
    tag = (u32x2){t0, t1};
  };
  auto push = [&](int hop, unsigned voff) { __builtin_amdgcn_raw_buffer_store_b64(tag, rWS, voff, WS_FLAGS + hop * HOP_B, SC1); };
  auto poll = [&](int hop, int nact) {
    int ln = lane & 15; PIN(ln);
    const unsigned voff = ln < nact ? ln * 8 : 0;
    for (;;) {
      const u32x2 v = __builtin_amdgcn_raw_buffer_load_b64(rWS, voff, WS_FLAGS + hop * HOP_B + me * 128, SC1);
      asm volatile("" ::: "memory");
      if (__all(v[0] == tag[0] && v[1] == tag[1])) break;
      if (budget == 0) break;
      --budget;
    }
  };
  auto make_trb = [&]() {
    const int rb = (lane >> 4) & 1, q = (lane >> 2) & 3, p = lane & 3;
    const int rp = 4 * rb + p, lam = (rp & 4) | ((rp & 1) << 1) | ((rp >> 1) & 1);
    const int pc = ((odd ? 0x01327654 : 0x76543210) >> (4 * lam)) & 7;
    return lds + (8 * hh + q) * 64 + pc * 8;
  };
  auto store_x = [&](u32x4 f0, u32x4 f1, unsigned base) {
    int ln = lane; PIN(ln);
    st128<SC1>(f0, rWS, ln * 16, base + TOFF + (3 * pair + 2 * odd) * 1024);
    st128<SC1>(f1, rWS, ln * 16 + (ln >> 5) * (24576 - 512 + h * 512 - (3 * pair + 1) * 1024 - odd * 512), base + TOFF + (3 * pair + 1) * 1024 + odd * 512);
  };
  auto store_o = [&](u32x4 f0, u32x4 f1, unsigned base) {
    int ln = lane; PIN(ln);
    st128<SC1>(f0, rWS, ln * 16, base + TOFF + (3 * pair + 2 * odd) * 1024);
    st128<SC1>(f1, rWS, ln * 16 + (ln >> 5) * OOB, base + TOFF + (3 * pair + 1) * 1024 + odd * 512);
    __builtin_amdgcn_raw_buffer_store_b64(lo2(f1), rWS, ln * 8 + (1 - (ln >> 5)) * OOB, base + TOFF + 24576 - 256 + h * 256, SC1);
  };

  if (wave < 4) {
    RSTAMP(62); STAMP(0);
    const rsrc_t rX0 = __builtin_amdgcn_make_buffer_rsrc((void*)x0, 0, SEQ * DM * 2, 0x00020000);
    u32x4 xf[28];
    f32x16 xs, ak, av;
    float rstd;
    u32x4 mf;
    auto stats = [&](float s1, float s2) {
      const float mean = s1 * (1.0f / DM);
      rstd = __builtin_amdgcn_rsqf(fmaxf(s2 * (1.0f / DM) - mean * mean, 0.f) + 1e-5f);
      unsigned hi, lo; split2(-mean, hi, lo);
      mf = (u32x4){hi | (hi << 16), lo, hi | (hi << 16), lo};
      PIN(rstd); PIN(mf);
    };
    auto ldx = [&](int i, int ln) { xf[i] = __builtin_amdgcn_raw_buffer_load_b128(rX0, (ln >> 3) * 896 + (ln & 7) * 16 + (i >> 2) * 128, (T * 32 + (i & 3) * 8) * 896, 0); SB(); };
    u32x4 xa, xc;
    float t1, t2;
#pragma unroll
        for (int i = 0; i < XH; ++i) ldx(i, lane);
        STAMP(54);
    lptr trb = make_trb();
    u32x4 id0 = ident_frag(0, r, hh), id1 = ident_frag(1, r, hh);
    float wtail = hh ? 0.f : 1.f;
    unsigned lomask = hh ? 0u : ~0u;
    PIN(trb); PIN(id0); PIN(id1); PIN(wtail); PIN(lomask);
    asm volatile("" : "=v"(ak), "=v"(av), "=v"(rstd), "=v"(mf), "=v"(xs));
        const lptr stg = lds + L_X0 + w * 29184;
        lptr wr = stg + (lane >> 3) * 912 + (lane & 7) * 16, rd1 = stg + r * 912 + hh * 16, rd2 = rd1 + hh * 16, rd7 = rd1 + hh * 96;
        lptr pab = rd1 + (7 * pair + 5 * odd) * 16, pc = stg + r * 912 + (7 * pair + (hh ? 3 : 2 + 2 * odd)) * 16;
        PIN(wr); PIN(rd1); PIN(rd2); PIN(rd7); PIN(pab); PIN(pc);
        float s1[4] = {0.f, 0.f, 0.f, 0.f}, s2[4] = {0.f, 0.f, 0.f, 0.f};
        SB();
#pragma unroll
        for (int i = 0; i < 28; ++i) {
          if (i + XH < 28) ldx(i + XH, lane);
          if (i + XH == 28) load_epoch();
          *(LAS u32x4*)(wr + (i & 3) * 8 * 912 + (i >> 2) * 128) = xf[i];
#pragma unroll
          for (int j = 0; j < 4; ++j) { const unsigned u = xf[i][j]; s1[i & 3] = dot2(u, ONES, s1[i & 3]); s2[i & 3] = dot2(u, u, s2[i & 3]); }
          PIN(s1[i & 3]); PIN(s2[i & 3]);
          SB();
        }
        STAMP(55);
#pragma unroll
        for (int i = 0; i < 28; ++i) {
          const int c0 = chunk_of(ord(i), 0), c1 = chunk_of(ord(i), 1);
          xf[i] = ldl((c1 - c0 == 1 ? rd1 : c1 - c0 == 2 ? rd2 : rd7) + c0 * 16);
        }
        xa = ldl(pab); xc = ldl(pc);
        SB();
#pragma unroll
        for (int i = 0; i < 8; ++i) { float& v = i < 4 ? s1[i] : s2[i - 4]; v = dppadd<0xb1>(v); v = dppadd<0x4e>(v); v = dppadd<0x141>(v); }
        const int src = (8 * (r & 7) + (r >> 3)) * 4;
        unsigned m1 = 0u - (lane & 1), m2 = 0u - ((lane >> 1) & 1); PIN(m1); PIN(m2);
        auto pick = [&](const float (&v)[4]) {
          const unsigned a = (F2U(v[1]) & m1) | (F2U(v[0]) & ~m1), c = (F2U(v[3]) & m1) | (F2U(v[2]) & ~m1);
          return (c & m2) | (a & ~m2);
        };
        t1 = U2F(__builtin_amdgcn_ds_bpermute(src, pick(s1))); t2 = U2F(__builtin_amdgcn_ds_bpermute(src, pick(s2)));
    make_tag();
    auto release = [&](int hop) { if (w == 0) poll(hop, 16); bar(); };
    auto announce = [&](int hop) { if (w == 0) { int ln = lane; PIN(ln); push(hop, ln < 16 ? (tb * 16 + ln) * 128 + h * 8 : OOB); } SB(); };
    for (int layer = 0; layer < NL; ++layer) {
      const int type = layer & 1;
      const int pb = 2 + layer * 14;
      if (layer > 0) {
        release(3 * layer - 1);
        STAMP(pb + 0);
        for (;;) {
          const unsigned base = WS_XO + (2 * layer - 1) * XO_B + TOFF;
          int v16 = lane * 16; PIN(v16);
          u32x4 sp[8];
          const int vs = v16 + (v16 & 512);
#pragma unroll
          for (int u = 0; u < 8; ++u) { sp[u] = __builtin_amdgcn_raw_buffer_load_b128(rWS, vs + ((u >> 1) & 1) * 2048 + (u & 1) * 512, base + 24576 + (u >> 2) * 4096, SC1); SB(); }
          auto ld = [&](int s) { xf[4 + s] = __builtin_amdgcn_raw_buffer_load_b128(rWS, v16 + (s & 3) * 1024, base + (s >> 2) * 4096, SC1); SB(); };
#pragma unroll
          for (int s = 0; s < LA - 8; ++s) ld(s);
          unsigned m = 0;
          lptr bk = trb + L_WK; PIN(bk);
          ak = Z16; av = Z16;
          float s1 = 0.f, s2 = 0.f;
          u32x4 ck, cv;
          gemm2<28>(ak, av, bk, xf, [](int i) { return i; },
                    [&](int i) {
                      if (i < 4) { ld(LA - 8 + 2 * i); ld(LA - 7 + 2 * i); } else if (i + LA - 4 < 24) ld(i + LA - 4);
                      if (i < 4) { PIN(sp[2 * i]); PIN(sp[2 * i + 1]); xf[i] = cat(lo2(sp[2 * i]), lo2(sp[2 * i + 1])); }
                      if (i == 8) { av = mfma(cv, mf, av); ak = mfma(ck, mf, ak); SB(); }
                    },
                    [&](int i) {
                      if (i < 4) {
                        m = max4(max4(m, sp[2 * i]), sp[2 * i + 1]);
                        s1 += U2F(sp[2 * i][2]); s2 += U2F(sp[2 * i][3]); PIN(s1); PIN(s2);
                        s1 += U2F(sp[2 * i + 1][2]); s2 += U2F(sp[2 * i + 1][3]); PIN(s1); PIN(s2);
                      } else m = max4(m, xf[i]);
                      PIN(m);
                      if (i == 5) {
                        red2<false>(s1, s2);
                        stats(s1, s2);
                        const lptr cs = lds + L_CS + type * 3072 + v16;
                        ck = ldl(cs); cv = ldl(cs + 1024);
                      }
                    });
          if (!__any(m == SENT) || budget == 0) break;
          --budget; RET(layer * 3);
          asm volatile("" ::: "memory");
        }
      }
      int first = layer; asm volatile("" : "+s"(first));
      if (first == 0) {
        int ln = lane; PIN(ln);
        const int hh = ln >> 5;
        STAMP(56);
        bar();
        STAMP(1);
        u32x4 cc = ldl(lds + L_CS + ln * 16);
        ak = Z16;
        lptr bk = trb + L_WK; PIN(bk);
        gemm1<5>(ak, bk, xf, [&](int i) { if (i == 12) { ak = mfma(cc, mf, ak); SB(); } }, [&](int i) {
          if (i == 1) stats(t1, t2);
          if (i == 4) {
            const unsigned sel = hh && odd ? 0u : ~0u;
            xc[0] = (xc[0] & sel) | (xc[2] & ~sel); xc[1] = (xc[1] & sel) | (xc[3] & ~sel); PIN(xc);
          }
          if (i >= 16 && i < 24) { const unsigned u = i < 20 ? xa[i & 3] : xc[i & 3]; xs[2 * (i - 16)] = bflo(u); xs[2 * (i - 16) + 1] = bfhi(u); PIN(xs); }
        });
        asm volatile("" :: "v"(ak));
        STAMP(57);
        bar();
        STAMP(2);
        cc = ldl(lds + L_CS + 1024 + ln * 16);
        av = Z16;
        gemm1<5>(av, bk + SLAB, xf, [&](int i) { if (i == 8) { av = mfma(cc, mf, av); SB(); } }, [](int) {});
      }
      STAMP(pb + 1);
      const unsigned mid = WS_MID + (h * 4 + layer) * MID_B;
      int ln16 = lane * 16; PIN(ln16);
      u32x4 wph[2], kf0, kf1;
      if (type == 0) {
        wph[0] = ldl(lds + L_WPH + ln16); wph[1] = ldl(lds + L_WPH + 1024 + ln16);
        DUMP(0, ak, 1, rstd); DUMP(0, av, 2, rstd);
        kf0 = pack8<0>(ak); kf1 = pack8<1>(ak);
        f32x16 p = mfma(wph[0], kf0, Z16);
        p = mfma(wph[1], kf1, p);
        SB();
#pragma unroll
        for (int i = 0; i < 16; ++i) { float t = av[i] * rstd; PIN(t); av[i] = t; }
        u32x4 vb0 = pack8<0>(av), vb1 = pack8<1>(av);
        vb1[2] |= ~lomask & 0x3f80u;
        PIN(vb0); PIN(vb1);
        SB();
        u32x4 fp[2], fm[2];
        dual_softmax(p, rstd * L2E, wtail, fp, fm);
        fp[1][2] &= lomask; fp[1][3] &= lomask; fm[1][2] &= lomask; fm[1][3] &= lomask;
        PIN(fp[0]); PIN(fp[1]); PIN(fm[0]); PIN(fm[1]);
        bar();
        const int rr = (ln16 >> 4) & 31, pc = (ln16 >> 9) ^ ((rr >> 1) & 3);
        const lptr d0 = lds + L_SCR + w * 2048 + rr * 64 + pc * 16, d1 = lds + L_SCR + w * 2048 + rr * 64 + (pc ^ 2) * 16;
        *(LAS u32x4*)(d0) = fp[0]; *(LAS u32x4*)(d1) = fp[1];
        *(LAS u32x4*)(d0 + 8192) = fm[0]; *(LAS u32x4*)(d1 + 8192) = fm[1];
        *(LAS u32x4*)(d0 + 16384) = vb0; *(LAS u32x4*)(d1 + 16384) = vb1;
        bar();
      } else {
        {
          int ln = lane; PIN(ln);
          push(3 * layer, (ln < 2 && tb + 1 + ln < 16) ? ((tb + 1 + ln) * 16 + h) * 128 + (ln * 4 + w) * 8 : OOB);
        }
        av *= rstd;
        DUMP(0, av, 2, 1.f);
        u32x4 vb1 = pack8<1>(av);
        vb1[2] |= ~lomask & 0x3f80u;
        f32x16 vt = mfma(pack8<0>(av), id0, Z16);
        vt = mfma(vb1, id1, vt);
        SB();
#pragma unroll
        for (int i = 0; i < 16; ++i) { float t = ak[i] * rstd; PIN(t); ak[i] = t; }
        DUMP(0, ak, 1, 1.f);
        kf0 = pack8<0>(ak); kf1 = pack8<1>(ak);
        const unsigned g = mid + T * 4096;
        const lptr d = lds + L_SCR + (8 + w) * 4096 + ln16;
        st128<SC1>(kf0, rWS, ln16, g);
        const u32x4 vf0 = pack8<0>(vt), vf1 = pack8<1>(vt);
        st128<SC1>(vf0, rWS, ln16 + 2048, g);
        st128<SC1>(vf1, rWS, ln16 + 3072, g);
        {
          u32x4 kc = kf1;
          kc[3] |= ~lomask & (tag[0] + layer - xhalf_addu(add4(add4(0u, kf0), kf1) + add4(add4(0u, vf0), vf1)));
          st128<SC1>(kc, rWS, ln16 + 1024, g);
        }
        bar();
        *(LAS u32x4*)(d) = kf0; *(LAS u32x4*)(d + 1024) = kf1; *(LAS u32x4*)(d + 2048) = vf0; *(LAS u32x4*)(d + 3072) = vf1;
      }
      STAMP(pb + 2);
      u32x4 qf0, qf1;
      {
        f32x16 aq = mfma(ldl(lds + L_CS + type * 3072 + 2048 + ln16), mf, Z16);
        lptr bq = trb + L_WQ; PIN(bq);
        gemm1<5>(aq, bq, xf, [](int) {}, [](int) {});
        aq *= type ? rstd * (0.18898223650461361f * L2E) : 1.0f;
        DUMP(0, aq, 0, type ? 1.f : rstd);
        qf0 = pack8<0>(aq); qf1 = pack8<1>(aq);
        qf1[2] |= ~lomask & 0x3f80u;
      }
      f32x16 o;
      if (type == 0) {
        u32x4 fp[2], fm[2];
        {
          f32x16 p = mfma(wph[0], qf0, Z16);
          p = mfma(wph[1], qf1, p);
          dual_softmax(p, rstd * L2E, wtail, fp, fm);
        }
        PIN(fp[0]); PIN(fp[1]); PIN(fm[0]); PIN(fm[1]);
        STAMP(pb + 3);
        bar();
        STAMP(pb + 4);
        const lptr kvf = lds + L_KVF + ln16;
        const u32x4 a0 = ldl(kvf), a1 = ldl(kvf + 1024), a2 = ldl(kvf + 2048), a3 = ldl(kvf + 3072);
        SB();
        announce(3 * layer + 1);
        o = mfma(a0, fp[0], Z16);
        o = mfma(a1, fp[1], o);
        o = mfma(a2, fm[0], o);
        o = mfma(a3, fm[1], o);
        o *= __builtin_amdgcn_rcpf(upper_val(o[12]) + 1e-6f);
      } else {
        lptr kv = lds + L_SCR + w * 4096 + ln16; PIN(kv);
        const u32x4 mk0 = ldl(lds + L_MK + ln16), mk1 = ldl(lds + L_MK + 1024 + ln16);
        f32x16 sd = mfma(kf0, qf0, Z16);
        sd = mfma(kf1, qf1, sd);
        PIN(sd);
        STAMP(pb + 3);
        bar();
        STAMP(pb + 4);
        u32x4 kk[8][2], vv[3][2];
        f32x16 st[3];
#pragma unroll
        for (int n = 0; n < 3; ++n) { kk[n][0] = ldl(kv + n * 4096); kk[n][1] = ldl(kv + n * 4096 + 1024); }
        SB();
        float m0 = -__builtin_inff(), m1 = m0;
#pragma unroll
        for (int n = 0; n < 10; ++n) {
          if (n + 3 < 8) { kk[n + 3][0] = ldl(kv + (n + 3) * 4096); kk[n + 3][1] = ldl(kv + (n + 3) * 4096 + 1024); }
          else if (n < 8) { const int t = n == 5 ? 0 : (n == 6 ? 8 : 1); vv[n - 5][0] = ldl(kv + t * 4096 + 2048); vv[n - 5][1] = ldl(kv + t * 4096 + 3072); }
          SB();
          if (n < 8) st[n % 3] = mfma(kk[n][0], qf0, Z16);
          SB();
          if (n == 2) {
#pragma unroll
            for (int i = 0; i < 8; ++i) sd[i] = ((i & 3) + 8 * (i >> 2) + 4 * hh) > r ? st[0][i] : sd[i];
          }
          if (n >= 2) {
            const f32x16& s = n == 2 ? sd : st[(n - 2) % 3];
#pragma unroll
            for (int i = 0; i < 8; i += 2) m0 = fmax3(m0, s[i], s[i + 1]);
            PIN(m0);
          }
          SB();
          if (n < 8) st[n % 3] = mfma(kk[n][1], qf1, st[n % 3]);
          SB();
          if (n == 2) {
#pragma unroll
            for (int i = 8; i < 16; ++i) sd[i] = ((i & 3) + 8 * (i >> 2) + 4 * hh) > r ? st[0][i] : sd[i];
          }
          if (n >= 2) {
            const f32x16& s = n == 2 ? sd : st[(n - 2) % 3];
#pragma unroll
            for (int i = 8; i < 16; i += 2) m1 = fmax3(m1, s[i], s[i + 1]);
            PIN(m1);
          }
          SB();
        }
        const float m = xhalf_max(fmax3(m0, m1, m1));
        f32x16 negm = splat(-m);
        PIN(negm);
        SB();
        auto ex4 = [&](f32x16& s, int i0) {
#pragma unroll
          for (int i = i0; i < i0 + 4; ++i) s[i] = __builtin_amdgcn_exp2f(s[i]);
          unsigned a = pk2(s[i0], s[i0 + 1]), c = pk2(s[i0 + 2], s[i0 + 3]); PIN(a); PIN(c);
          SB();
          return (u32x2){a, c};
        };
        u32x4 pa, pb2;
        {
          st[1] = mfma(kk[1][0], qf0, negm);
          SB();
#pragma unroll
          for (int i = 0; i < 8; ++i) { float t = sd[i] - m; PIN(t); sd[i] = t; }
          SB();
          st[1] = mfma(kk[1][1], qf1, st[1]);
          SB();
#pragma unroll
          for (int i = 8; i < 16; ++i) { float t = sd[i] - m; PIN(t); sd[i] = t; }
          SB();
          const u32x2 a = ex4(sd, 0), c = ex4(sd, 4);
          const u32x4 p0 = cat(a, c);
          u32x4 t0 = p0 & mk0; pa = p0 & ~mk0; PIN(t0); PIN(pa);
          SB();
          o = mfma(vv[0][0], t0, Z16);
          SB();
          const u32x2 e = ex4(sd, 8), f = ex4(sd, 12);
          const u32x4 p1 = cat(e, f);
          u32x4 t1 = p1 & mk1; pb2 = p1 & ~mk1; PIN(t1); PIN(pb2);
          SB();
          o = mfma(vv[0][1], t1, o);
          SB();
        }
#pragma unroll
        for (int j = 1; j < 8; ++j) {
          f32x16& s = st[j & 1];
          if (j < 7) { vv[(j + 2) % 3][0] = ldl(kv + (j + 1) * 4096 + 2048); vv[(j + 2) % 3][1] = ldl(kv + (j + 1) * 4096 + 3072); }
          if (j == 7) { bar(); announce(3 * layer + 1); }
          SB();
          const u32x2 a = ex4(s, 0);
          o = mfma(vv[j % 3][0], pa, o);
          SB();
          const u32x2 c = ex4(s, 4);
          if (j < 7) st[(j + 1) & 1] = mfma(kk[j + 1][0], qf0, negm); else o = mfma(vv[j % 3][1], pb2, o);
          SB();
          const u32x2 e = ex4(s, 8);
          if (j < 7) st[(j + 1) & 1] = mfma(kk[j + 1][1], qf1, st[(j + 1) & 1]); else o = mfma(vv[(j + 1) % 3][0], cat(a, c), o);
          SB();
          const u32x2 f = ex4(s, 12);
          if (j < 7) o = mfma(vv[j % 3][1], pb2, o); else o = mfma(vv[(j + 1) % 3][1], cat(e, f), o);
          SB();
          pa = cat(a, c); pb2 = cat(e, f);
        }
        o *= __builtin_amdgcn_rcpf(upper_val(o[12]));
      }
      DUMP(1, o, 0, 1.f);
      store_o(pack8<0>(o), pack8<1>(o), WS_XO + (2 * layer) * XO_B);
      STAMP(pb + 6);
      release(3 * layer + 1);
      STAMP(pb + 7);
      f32x16 xn;
      for (;;) {
        const unsigned base = WS_XO + (2 * layer) * XO_B + TOFF;
        int v16 = lane * 16; PIN(v16);
        u32x2 sp[8];
        const int vs = (v16 >> 1) + ((v16 >> 1) & 256);
#pragma unroll
        for (int u = 0; u < 8; ++u) { sp[u] = __builtin_amdgcn_raw_buffer_load_b64(rWS, vs + (u >> 1) * 1024 + (u & 1) * 256, base + 24576, SC1); SB(); }
        auto ld = [&](int s) { xf[4 + s] = __builtin_amdgcn_raw_buffer_load_b128(rWS, v16 + (s & 3) * 1024, base + (s >> 2) * 4096, SC1); SB(); };
#pragma unroll
        for (int s = 0; s < LA - 4; ++s) ld(s);
#pragma unroll
        for (int u = 0; u < 4; ++u) xf[u] = cat(sp[2 * u], sp[2 * u + 1]);
        xn = xs;
        unsigned m = 0;
        lptr bo = trb + L_WO + type * SLAB; PIN(bo);
        gemm1<4>(xn, bo, xf, [&](int i) { if (i + LA - 4 < 24) ld(i + LA - 4); }, [&](int i) { m = max4(m, xf[i]); PIN(m); });
        if (!__any(m == SENT) || budget == 0) break;
        --budget; RET(layer * 3 + 2);
        asm volatile("" ::: "memory");
      }
      xs = xn;
      STAMP(pb + 8);
      if (layer == NL - 1) {
        const rsrc_t rOUT = __builtin_amdgcn_make_buffer_rsrc((void*)out, 0, SEQ * DM * 2, 0x00020000);
        const u32x4 f0 = pack8<0>(xs), f1 = pack8<1>(xs);
        int ln = lane; PIN(ln);
        const int row = (ln & 31) * 896, up = ln >> 5;
        st128<0>(f0, rOUT, row + up * 16, T * 32 * 896 + (7 * pair + 5 * odd) * 16);
        st128<0>(f1, rOUT, row + up * OOB, T * 32 * 896 + (7 * pair + 2 + 2 * odd) * 16);
        __builtin_amdgcn_raw_buffer_store_b64(lo2(f1), rOUT, row + (1 - up) * OOB, T * 32 * 896 + (7 * pair + 3) * 16 + odd * 8, 0);
      } else {
        bar();
        announce(3 * layer + 2);
        const u32x4 f0 = pack8<0>(xs);
        u32x4 f1 = pack8<1>(xs);
        f1[2] &= lomask; f1[3] &= lomask;
        float s1 = 0.f, s2 = 0.f, t1 = 0.f, t2 = 0.f;
#pragma unroll
        for (int j = 0; j < 4; ++j) { const unsigned u = f0[j], v = f1[j]; s1 = dot2(u, ONES, s1); s2 = dot2(u, u, s2); t1 = dot2(v, ONES, t1); t2 = dot2(v, v, t2); }
        s1 += t1; s2 += t2;
        red2<false>(s1, s2);
        f1[2] |= ~lomask & F2U(s1); f1[3] |= ~lomask & F2U(s2);
        store_x(f0, f1, WS_XO + (2 * layer + 1) * XO_B);
        STAMP(pb + 9);
      }
      asm volatile("" : "=v"(ak), "=v"(av), "=v"(rstd), "=v"(mf), "=v"(xa), "=v"(xc), "=v"(t1), "=v"(t2));
    }
    STAMP(61); RSTAMP(63);
    if (b == 0 && w == 0) __builtin_amdgcn_raw_buffer_store_b64((u32x2){(unsigned)(epoch + 1), (unsigned)((epoch + 1) >> 32)}, rWS, WS_EPOCH, 0, SC1);
  } else {
    __builtin_amdgcn_s_setprio(3);
    STAMP(53);
    const int ht = tid & 255;
    const rsrc_t rWA = __builtin_amdgcn_make_buffer_rsrc((void*)wqkvA, 0, DM * 3 * DM * 2, 0x00020000);
    const int sub = lane & 3;
    const int col = h * 56 - odd * 8;
    int krow[7];
    auto mkrow = [&](int i) {
      const int s = 4 * i + w;
      const int p = (s * 43) >> 7, m = s - 3 * p;
      const int c = i < 6 ? 7 * p + ((0x520 >> (4 * m)) & 15) + hh * (1 + (m & 1)) : 7 * (2 * w + hh) + 3;
      krow[i] = 8 * c + ((lane >> 2) & 7);
    };
    auto ldw = [&](rsrc_t rs, int i, int stride, int off) { int k = krow[i]; PIN(k); const u32x4 v = __builtin_amdgcn_raw_buffer_load_b128(rs, k * stride + sub * 16, col + off, 0); SB(); return v; };
    u32x4 pA[21], pB[21];
    unsigned tch0, tch1;
    asm volatile("s_load_dword %0, %2, 0x0\n\ts_load_dword %1, %3, 0x0" : "=&s"(tch0), "=&s"(tch1)
                 : "s"(w == 0 ? (const void*)ws : w == 1 ? (const void*)wqkvB : w == 2 ? (const void*)woA : (const void*)wphi), "s"(w < 2 ? (const void*)woB : (const void*)out));
    unsigned warm0;
    {
      u64 pc; asm volatile("s_getpc_b64 %0" : "=s"(pc));
      const rsrc_t rs = __builtin_amdgcn_make_buffer_rsrc(w == 3 ? (void*)(pc & ~127ull) : (void*)wqkvA, 0, w == 3 ? CODE_B : DM * 3 * DM * 2, 0x00020000);
      const int n = w == 3 ? 8 : 13;
      warm0 = __builtin_amdgcn_raw_buffer_load_b32(rs, lane < n ? (lane + (w == 3 ? (b >> 3) : b * 3 + w) * n) * 128 : OOB, 0, 0);
      SB();
    }
#pragma unroll
    for (int i = 0; i < 7; ++i) { mkrow(i); pA[7 + i] = ldw(rWA, i, 2688, 896); }
    STAMP(54);
    asm volatile("s_waitcnt lgkmcnt(0)" :: "s"(tch0), "s"(tch1));
    const lptr trb = make_trb();
    const unsigned keep01 = (odd && sub == 0) ? 0u : ~0u, keep23 = (!odd && sub == 3) ? 0u : ~0u;
    auto patch = [&](u32x4& v) { v[0] &= keep01; v[1] &= keep01; v[2] &= keep23; v[3] &= keep23; };
    auto write_slab = [&](const u32x4* p, unsigned off) {
      lptr d = lds + off + ht * 16; PIN(d);
#pragma unroll
      for (int i = 0; i < 7; ++i) *(LAS u32x4*)(d + i * 4096) = p[i];
    };
    const u32x4 ones = {ONES, ONES, ONES, ONES};
    auto first_slab = [&](u32x4* p, int type, int part) {
#pragma unroll
      for (int i = 0; i < 7; ++i) patch(p[i]);
      write_slab(p, part * SLAB);
      lptr wb = trb + part * SLAB + w * 1024; PIN(wb);
      u32x4 wr[7];
#pragma unroll
      for (int i = 0; i < 7; ++i) wr[i] = trfrag(wb + i * 4096);
      f32x16 a = Z16;
#pragma unroll
      for (int i = 0; i < 7; ++i) a = mfma(ones, wr[i], a);
      unsigned hi, lo; split2(a[0], hi, lo);
      if (hh == (w >> 1)) *(LAS u32x2*)(lds + L_CS + type * 3072 + part * 1024 + lane * 16 + (w & 1) * 8) = (u32x2){hi | (lo << 16), hi};
    };
    const u32x4 sv = {SENT, SENT, SENT, SENT};
    auto clean_kv = [&](int layer) { if (layer < NL) st128<SC1>(sv, rWS, lane * 16, WS_MID + (h * 4 + layer) * MID_B + (tb * 4 + w) * 1024); };
    auto clean_o = [&](int layer) { if (layer < NL) store_o(sv, sv, WS_XO + (2 * layer) * XO_B); };
    auto clean_x = [&](int layer) { if (layer < NL - 1) store_x(sv, sv, WS_XO + (2 * layer + 1) * XO_B); };
    volatile LAS unsigned* const sy = (volatile LAS unsigned*)(lds + L_SYNC);
    auto wait_h = [&](int hop, int nact) {
      if (w == 0) { if (nact > 0) poll(hop, nact); sy[0] = hop + 1; }
      else while (__builtin_amdgcn_readfirstlane(sy[0]) != hop + 1) __builtin_amdgcn_s_sleep(1);
      asm volatile("" ::: "memory");
    };
    if (w == 0) sy[0] = 0u;
    const rsrc_t rWB = __builtin_amdgcn_make_buffer_rsrc((void*)wqkvB, 0, DM * 3 * DM * 2, 0x00020000);
    const rsrc_t rOA = __builtin_amdgcn_make_buffer_rsrc((void*)woA, 0, DM * DM * 2, 0x00020000);
    const rsrc_t rOB = __builtin_amdgcn_make_buffer_rsrc((void*)woB, 0, DM * DM * 2, 0x00020000);
    u32x4 id0 = ident_frag(0, r, hh); PIN(id0);
    if (w >= 2) {
      u32x4 mk;
#pragma unroll
      for (int i = 0; i < 4; ++i) { const int k0 = ((2 * i) & 3) + 8 * (i >> 1) + 4 * hh + 16 * (w & 1); mk[i] = (k0 > r ? 0xffffu : 0u) | (k0 + 1 > r ? 0xffff0000u : 0u); }
      *(LAS u32x4*)(lds + L_MK + (w & 1) * 1024 + lane * 16) = mk;
    }
    u32x4 tw[7];
    SB();
    first_slab(pA + 7, 0, 0);
    asm volatile("" :: "v"(warm0));
    STAMP(55);
    __builtin_amdgcn_s_setprio(0);
    if (VSLEEP) __builtin_amdgcn_s_sleep(VSLEEP);
#pragma unroll
    for (int i = 0; i < 7; ++i) pA[14 + i] = ldw(rWA, i, 2688, 1792);
    STAMP(56);
    unsigned e[4];
    {
      const rsrc_t rPH = __builtin_amdgcn_make_buffer_rsrc((void*)wphi, 0, 16 * 28 * 28 * 2, 0x00020000);
      const int lam = 4 * (w >> 1) + 2 * hh + (w & 1);
      const int pc = ((odd ? 0x01327654 : 0x76543210) >> (4 * lam)) & 7;
      const unsigned off = lam == 7 ? OOB : ((h * 28 + 4 * pc - 4 * odd) * 28 + (r < 28 ? r : r - 8)) * 2;
#pragma unroll
      for (int j = 0; j < 4; ++j) e[j] = __builtin_amdgcn_raw_buffer_load_b16(rPH, off + j * 56, 0, 0);
      SB();
    }
    load_epoch();
    unsigned warm;
    {
      const void* p = w < 2 ? (const void*)wqkvB : w == 2 ? (const void*)woA : (const void*)woB;
      const int n = w < 2 ? 19 : 13;
      const rsrc_t rs = __builtin_amdgcn_make_buffer_rsrc((void*)p, 0, w < 2 ? DM * 3 * DM * 2 : DM * DM * 2, 0x00020000);
      warm = __builtin_amdgcn_raw_buffer_load_b32(rs, lane < n ? (lane + (w < 2 ? 2 * b + w : b) * n) * 128 : OOB, 0, 0);
      SB();
    }
    clean_kv(0);
#pragma unroll
    for (int j = 0; j < 7; ++j) pA[j] = ldw(rWA, j, 2688, 0);
    __builtin_amdgcn_s_setprio(3);
    STAMP(57);
    bar();
    first_slab(pA + 14, 0, 1);
    *(LAS u32x2*)(lds + L_WPH + (w >> 1) * 1024 + lane * 16 + (w & 1) * 8) = (u32x2){e[0] | (e[1] << 16), e[2] | (e[3] << 16)};
#pragma unroll
    for (int i = 0; i < 3; ++i) tw[i] = ldw(rOA, i, 896, 0);
    STAMP(58);
    bar();
    __builtin_amdgcn_s_setprio(0);
    clean_o(0); clean_x(0);
#pragma unroll
    for (int i = 3; i < 7; ++i) tw[i] = ldw(rOA, i, 896, 0);
#pragma unroll
    for (int i = 0; i < 7; ++i) pB[7 + i] = ldw(rWB, i, 2688, 896);
    STAMP(59);
    first_slab(pA, 0, 2);
    make_tag();
    asm volatile("" :: "v"(warm));
    __builtin_amdgcn_s_setprio(3);
    STAMP(60);
#pragma unroll
    for (int layer = 0; layer < NL; ++layer) {
      const int type = layer & 1;
      const int pb = 2 + layer * 14;
      const unsigned mid = WS_MID + (h * 4 + layer) * MID_B;
      if (type == 0) {
        lptr ta0, ta1;
        {
          const int rb = (lane >> 4) & 1, q = (lane >> 2) & 3, p = lane & 3;
          const int rp = 4 * rb + p, lam = (rp & 4) | ((rp & 1) << 1) | ((rp >> 1) & 1);
          ta0 = lds + L_SCR + (8 * hh + q) * 64 + (((lam >> 1) ^ (q >> 1)) * 16) + (lam & 1) * 8;
          ta1 = lds + L_SCR + (8 * hh + q + 4) * 64 + (((lam >> 1) ^ (q >> 1) ^ 2) * 16) + (lam & 1) * 8;
          PIN(ta0); PIN(ta1);
        }
        bar();
        bar();
        if (w == 0) push(3 * layer, lane < 16 ? (lane * 16 + h) * 128 + tb * 8 : OOB);
        STAMP(pb + 0);
        {
          auto tr2 = [&](unsigned off) {
            bf16x4 a0 = __builtin_amdgcn_ds_read_tr16_b64_v4bf16((LAS bf16x4*)(ta0 + off));
            bf16x4 a1 = __builtin_amdgcn_ds_read_tr16_b64_v4bf16((LAS bf16x4*)(ta1 + off));
            return __builtin_bit_cast(u32x4, __builtin_shufflevector(a0, a1, 0, 1, 2, 3, 4, 5, 6, 7));
          };
          const unsigned so = (w >> 1) * 8192;
          u32x4 fa[4], fb[4];
          f32x16 kv = Z16;
#pragma unroll
          for (int i = 0; i < 3; ++i) { fa[i] = tr2(so + i * 1024); fb[i] = tr2(16384 + i * 1024); }
          SB();
#pragma unroll
          for (int i = 0; i < 8; ++i) {
            if (i + 3 < 8) { fa[(i + 3) & 3] = tr2(so + (i + 3) * 1024); fb[(i + 3) & 3] = tr2(16384 + (i + 3) * 1024); }
            SB();
            kv = mfma(fa[i & 3], fb[i & 3], kv);
            SB();
          }
          st128<SC1>((w & 1) ? pack8<1>(kv) : pack8<0>(kv), rWS, lane * 16, mid + (tb * 4 + w) * 1024);
        }
        STAMP(pb + 1);
        if (layer == 0) {
          if (w > 0) {
#pragma unroll
            for (int i = 0; i < 7; ++i) pB[14 + i] = ldw(rWB, i, 2688, 1792);
          }
          write_slab(tw, L_WO);
        }
        wait_h(3 * layer, 16);
        STAMP(pb + 2);
        f32x16 sum;
        for (;;) {
          sum = Z16;
          unsigned m = 0;
          u32x4 pr[16];
          const unsigned g = mid + w * 1024;
#pragma unroll
          for (int i = 0; i < 16; ++i) { pr[i] = __builtin_amdgcn_raw_buffer_load_b128(rWS, lane * 16, g + i * 4096, SC1); SB(); }
#pragma unroll
          for (int i = 0; i < 16; ++i) { sum = mfma(id0, pr[i], sum); m = max4(m, pr[i]); PIN(m); SB(); }
          if (!__any(m == SENT) || budget == 0) break;
          --budget; RET(layer * 3 + 1);
          asm volatile("" ::: "memory");
        }
        *(LAS u32x4*)(lds + L_KVF + w * 1024 + lane * 16) = pack8<0>(sum);
        STAMP(pb + 3);
        bar();
        __builtin_amdgcn_s_sleep(8);
      } else {
        bar();
        STAMP(pb + 0);
        if (w == 0) __builtin_amdgcn_s_sleep(PDELAY);
        wait_h(3 * layer, tb >= 2 ? 8 : 4 * tb);
        STAMP(pb + 2);
        u32x4 t[8];
        if (tb - 2 + (w >> 1) >= 0) {
          const unsigned g = mid + (tb * 4 - 8 + 2 * w) * 4096;
          for (;;) {
            unsigned c[2] = {0u, 0u};
#pragma unroll
            for (int i = 0; i < 8; ++i) { t[i] = __builtin_amdgcn_raw_buffer_load_b128(rWS, lane * 16 + (i & 3) * 1024, g + (i >> 2) * 4096, SC1); SB(); }
#pragma unroll
            for (int i = 0; i < 8; ++i) {
              c[i >> 2] = add4(c[i >> 2], t[i]); PIN(c[i >> 2]);
              if ((i & 3) == 1) t[i][3] &= (unsigned)hh - 1u;
              *(LAS u32x4*)(lds + L_SCR + 2 * w * 4096 + i * 1024 + lane * 16) = t[i];
              SB();
            }
            const auto cs = __builtin_amdgcn_permlane32_swap(c[0], c[1], false, false);
            if (__all(cs[0] + cs[1] == tag[0] + layer) || budget == 0) break;
            --budget; RET(layer * 3 + 1);
            asm volatile("" ::: "memory");
          }
        } else {
#pragma unroll
          for (int i = 0; i < 8; ++i)
            *(LAS u32x4*)(lds + L_SCR + 2 * w * 4096 + i * 1024 + lane * 16) = (u32x4){0u, 0u, ((i & 3) == 1 && hh) ? 0xff80u : 0u, 0u};
        }
        STAMP(pb + 3);
        bar();
        clean_o(layer);
        if (layer == 1) {
          clean_x(1); clean_kv(2);
#pragma unroll
          for (int i = 0; i < 7; ++i) tw[i] = ldw(rOB, i, 896, 0);
        }
        asm volatile("s_waitcnt vmcnt(%0)" :: "n"(layer == 1 ? 7 : 0) : "memory");
        bar();
      }
      __builtin_amdgcn_s_setprio(0);
      if (layer == 0) {
        if (w == 0) {
#pragma unroll
          for (int i = 0; i < 7; ++i) pB[14 + i] = ldw(rWB, i, 2688, 1792);
        }
      } else {
        if (layer == 1) write_slab(tw, L_WO + SLAB);
        if (layer < NL - 1) { if (type == 0) { write_slab(pB + 7, L_WK); write_slab(pB + 14, L_WV); } else write_slab(pA + 7, L_WK); }
      }
      STAMP(pb + 4);
      bar();
      STAMP(pb + 5);
      if (layer < NL - 1) {
        if (layer == 0) {
          first_slab(pB + 7, 1, 0); first_slab(pB + 14, 1, 1);
        } else {
          if (type == 0) write_slab(pB, L_WQ); else { write_slab(pA + 14, L_WV); write_slab(pA, L_WQ); }
        }
        bar();
        if (layer == 0) {
          __builtin_amdgcn_s_sleep(5);
#pragma unroll
          for (int i = 0; i < 7; ++i) pB[i] = ldw(rWB, i, 2688, 0);
        }
        bar();
        if (layer == 0) first_slab(pB, 1, 2);
        if (layer == 1) { __builtin_amdgcn_s_sleep(52); clean_o(2); clean_x(2); }
        __builtin_amdgcn_s_setprio(3);
      }
    }
  }
}
}

extern "C" void kernel_launch(void* const* d_in, const int* in_sizes, int n_in, void* d_out, int out_size, void* d_ws, size_t ws_size, hipStream_t stream) {
  (void)hipFuncSetAttribute((const void*)fused_kernel, hipFuncAttributeMaxDynamicSharedMemorySize, L_END);
  fused_kernel<<<256, 512, L_END, stream>>>((const u16*)d_in[0], (const u16*)d_in[1], (const u16*)d_in[2], (const u16*)d_in[3],
                                            (const u16*)d_in[5], (const u16*)d_in[6], (u16*)d_out, (unsigned char*)d_ws);
}
```

```cpp
#define NL 4
#include <hip/hip_runtime.h>
#include <hip/hip_bf16.h>

typedef unsigned short u16;
typedef unsigned long long u64;
typedef __bf16 bf16x8 __attribute__((ext_vector_type(8)));
typedef __bf16 bf16x4 __attribute__((ext_vector_type(4)));
typedef __bf16 bf16x2 __attribute__((ext_vector_type(2)));
typedef float f32x16 __attribute__((ext_vector_type(16)));
typedef float f32x4 __attribute__((ext_vector_type(4)));
typedef float f32x2 __attribute__((ext_vector_type(2)));
typedef unsigned u32x4 __attribute__((ext_vector_type(4)));
typedef unsigned u32x2 __attribute__((ext_vector_type(2)));
#define LAS __attribute__((address_space(3)))
#define GAS __attribute__((address_space(1)))
#define DI __device__ __forceinline__
#define SB() __builtin_amdgcn_sched_barrier(0)
#define PIN(x) asm volatile("" : "+v"(x))
typedef __amdgpu_buffer_rsrc_t rsrc_t;
typedef LAS unsigned char* lptr;

namespace {
constexpr int SEQ = 2048, DM = 448;
constexpr int SC1 = 16;
constexpr unsigned WS_EPOCH = 0;
constexpr unsigned WS_FLAGS = 4096;
constexpr unsigned HOP_B = 256 * 128;
constexpr unsigned TILE_B = 32768;
constexpr unsigned XO_B = 4 * TILE_B;
constexpr unsigned WS_XO = 1u << 20;
constexpr unsigned MID_B = 64 * 4096;
constexpr unsigned WS_MID = WS_XO + 16 * 8 * XO_B;
constexpr unsigned WS_PK = WS_MID + 64 * MID_B;
constexpr unsigned WS_PROF = WS_PK + 16 * 262144;
constexpr unsigned WS_END = WS_PROF + 256 * 2048;
constexpr unsigned SLAB = 448 * 64;
constexpr unsigned L_WK = 0, L_WV = SLAB, L_WQ = 2 * SLAB, L_WO = 3 * SLAB;
constexpr unsigned L_SCR = 0;
constexpr unsigned L_X0 = SLAB;
constexpr unsigned L_KVF = 5 * SLAB;
constexpr unsigned L_MK = L_KVF + 4096;
constexpr unsigned L_WPH = L_MK + 2048;
constexpr unsigned L_CS = L_WPH + 2048;
constexpr unsigned L_SYNC = L_CS + 6144;
constexpr unsigned L_PROF = L_SYNC + 64;
constexpr unsigned L_END = L_PROF + 2048;
static_assert(L_END <= 163840 && L_X0 + 4 * SLAB <= L_KVF, "lds");
constexpr unsigned SPIN_CAP = 1u << 15;
constexpr unsigned SENT = 0xffffffffu;
constexpr unsigned OOB = 0x7ff00000u;
constexpr unsigned ONES = 0x3f803f80u;
constexpr unsigned CODE_B = 31 * 1024;
constexpr float L2E = 1.4426950408889634f;

DI f32x16 mfma(u32x4 a, u32x4 b, f32x16 c) {
  return __builtin_amdgcn_mfma_f32_32x32x16_bf16(__builtin_bit_cast(bf16x8, a), __builtin_bit_cast(bf16x8, b), c, 0, 0, 0);
}
DI unsigned pk2(float a, float b) { f32x2 t = {a, b}; return __builtin_bit_cast(unsigned, __builtin_convertvector(t, bf16x2)); }
template <int S_> DI u32x4 pack8(const f32x16& c) {
  return (u32x4){pk2(c[8 * S_], c[8 * S_ + 1]), pk2(c[8 * S_ + 2], c[8 * S_ + 3]), pk2(c[8 * S_ + 4], c[8 * S_ + 5]), pk2(c[8 * S_ + 6], c[8 * S_ + 7])};
}
DI u32x4 cat(u32x2 a, u32x2 b) { return (u32x4){a[0], a[1], b[0], b[1]}; }
DI u32x2 lo2(u32x4 v) { return (u32x2){v[0], v[1]}; }
DI unsigned umax3(unsigned a, unsigned b, unsigned c) { return max(max(a, b), c); }
DI unsigned max4(unsigned m, u32x4 v) { return umax3(umax3(m, v[0], v[1]), v[2], v[3]); }
DI unsigned add4(unsigned m, u32x4 v) { return (m + v[0] + v[1]) + v[2] + v[3]; }
DI unsigned xhalf_addu(unsigned v) { auto r = __builtin_amdgcn_permlane32_swap(v, v, false, false); return r[0] + r[1]; }
DI float fmax3(float a, float b, float c) { return fmaxf(fmaxf(a, b), c); }
template <int CTRL> DI float dppadd(float v) { return v + __builtin_bit_cast(float, __builtin_amdgcn_update_dpp(0, __builtin_bit_cast(int, v), CTRL, 0xf, 0xf, true)); }
DI float fmin3(float a, float b, float c) { return fminf(fminf(a, b), c); }
DI f32x16 splat(float v) { f32x16 z; _Pragma("unroll") for (int i = 0; i < 16; ++i) z[i] = v; return z; }
#define Z16 splat(0.f)
DI u32x4 trfrag(lptr p) {
  bf16x4 a0 = __builtin_amdgcn_ds_read_tr16_b64_v4bf16((LAS bf16x4*)p);
  bf16x4 a1 = __builtin_amdgcn_ds_read_tr16_b64_v4bf16((LAS bf16x4*)(p + 256));
  return __builtin_bit_cast(u32x4, __builtin_shufflevector(a0, a1, 0, 1, 2, 3, 4, 5, 6, 7));
}
DI u32x4 ldl(lptr p) { return *(LAS u32x4*)p; }
DI float U2F(unsigned u) { return __uint_as_float(u); }
DI unsigned F2U(float f) { return __float_as_uint(f); }
template <bool MX> DI void red2(float& a, float& b) {
  auto r = __builtin_amdgcn_permlane32_swap(F2U(a), F2U(b), false, false);
  const float t = MX ? fmaxf(U2F(r[0]), U2F(r[1])) : U2F(r[0]) + U2F(r[1]);
  r = __builtin_amdgcn_permlane32_swap(F2U(t), F2U(t), false, false);
  a = U2F(r[0]); b = U2F(r[1]);
}
DI float xhalf_max(float v) { auto r = __builtin_amdgcn_permlane32_swap(F2U(v), F2U(v), false, false); return fmaxf(U2F(r[0]), U2F(r[1])); }
DI float upper_val(float v) { auto r = __builtin_amdgcn_permlane32_swap(F2U(v), F2U(v), false, false); return U2F(r[1]); }
DI float dot2(unsigned u, unsigned v, float c) { return __builtin_amdgcn_fdot2_f32_bf16(__builtin_bit_cast(bf16x2, u), __builtin_bit_cast(bf16x2, v), c, false); }
DI float bflo(unsigned u) { return U2F(u << 16); }
DI float bfhi(unsigned u) { return U2F(u & 0xffff0000u); }
DI void split2(float v, unsigned& hi, unsigned& lo) { hi = pk2(v, v) & 0xffffu; lo = pk2(v - bflo(hi), 0.f) & 0xffffu; }
template <int AUX> DI void st128(u32x4 v, rsrc_t rs, unsigned voff, unsigned soff) {
  SB();
  __builtin_amdgcn_raw_buffer_store_b128(v, rs, voff, soff, AUX);
  SB();
  asm volatile("s_nop 1" :: "v"(v));
  SB();
}
DI void drain() { asm volatile("s_waitcnt vmcnt(0)" ::: "memory"); }
DI void bar() { SB(); asm volatile("s_waitcnt lgkmcnt(0)\n\ts_barrier" ::: "memory"); SB(); }
constexpr int chunk_of(int s, int hh) { return s < 24 ? 7 * (s / 3) + (s % 3 == 0 ? hh : (s % 3 == 1 ? 2 + 2 * hh : 5 + hh)) : 7 * (2 * (s - 24) + hh) + 3; }
constexpr int ord(int i) { return i < 4 ? 24 + i : i - 4; }

#define STAMP(i) do { } while (0)
#define RET(i) do { } while (0)
#define RSTAMP(i) do { } while (0)
#define STAMPX(i) STAMP(i)
#define CALS(hop) do { } while (0)
#define CALA(hop) do { } while (0)
#define TOT0
#define TOT1
#define DUMP(buf, acc, part, sc) do { } while (0)

DI void dual_softmax(const f32x16& p, float c, float wtail, u32x4 (&fp)[2], u32x4 (&fm)[2]) {
  float mx = fmax3(p[0], p[1], p[2]), mn = fmax3(-p[0], -p[1], -p[2]);
#pragma unroll
  for (int i = 3; i < 15; i += 2) { mx = fmax3(mx, p[i], p[i + 1]); mn = fmax3(mn, -p[i], -p[i + 1]); }
  mx = fmax3(mx, p[15], p[15]); mn = fmax3(mn, -p[15], -p[15]);
  red2<true>(mx, mn);
  const float a = -c * mx, b = -c * mn;
  float nc = -c; PIN(nc);
  f32x16 ep = p * c + a, em = p * nc + b;
  SB();
#pragma unroll
  for (int i = 0; i < 16; ++i) ep[i] = __builtin_amdgcn_exp2f(ep[i]);
#pragma unroll
  for (int i = 0; i < 16; ++i) em[i] = __builtin_amdgcn_exp2f(em[i]);
  f32x2 sp = {ep[0], ep[1]}, sm = {em[0], em[1]};
#pragma unroll
  for (int i = 2; i < 12; i += 2) { sp += (f32x2){ep[i], ep[i + 1]}; sm += (f32x2){em[i], em[i + 1]}; }
  sp += wtail * ((f32x2){ep[12], ep[13]} + (f32x2){ep[14], ep[15]});
  sm += wtail * ((f32x2){em[12], em[13]} + (f32x2){em[14], em[15]});
  float u = sp[0] + sp[1], v = sm[0] + sm[1];
  red2<false>(u, v);
  ep *= __builtin_amdgcn_rcpf(u); em *= __builtin_amdgcn_rcpf(v);
  fp[0] = pack8<0>(ep); fp[1] = pack8<1>(ep); fm[0] = pack8<0>(em); fm[1] = pack8<1>(em);
}
DI u32x4 ident_frag(int s, int r, int hh) {
  const unsigned val = 0x3f80u << (16 * (r & 1));
  const int idx = (((r >> 2) ^ hh) & 1) * 8 + (r >> 4) * 4 + ((r >> 2) & 2) + ((r >> 1) & 1) - 4 * s;
  return (u32x4){idx == 0 ? val : 0u, idx == 1 ? val : 0u, idx == 2 ? val : 0u, idx == 3 ? val : 0u};
}
#define CHK1 3
#define CHK2 12
template <typename S, typename A> DI void steps28(S step, A abort) {
#pragma unroll
  for (int i = 0; i <= CHK1; ++i) step(i);
  if (abort()) return;
#pragma unroll
  for (int i = CHK1 + 1; i <= CHK2; ++i) step(i);
  if (abort()) return;
#pragma unroll
  for (int i = CHK2 + 1; i < 28; ++i) step(i);
}
template <int PD, typename F, typename G, typename A> DI void gemm1(f32x16& acc, lptr wb, const u32x4 (&xf)[28], F pre, G post, A abort) {
  u32x4 wr[PD];
#pragma unroll
  for (int i = 0; i < PD - 1; ++i) wr[i] = trfrag(wb + ord(i) * 1024);
  SB();
  steps28([&](int i) __attribute__((always_inline)) {
    pre(i);
    if (i + PD - 1 < 28) wr[(i + PD - 1) % PD] = trfrag(wb + ord(i + PD - 1) * 1024);
    SB();
    acc = mfma(wr[i % PD], xf[i], acc);
    SB();
    post(i);
    SB();
  }, abort);
}
template <typename F, typename G, typename A>
DI void gemm2(f32x16& ak, f32x16& av, lptr bk, const u32x4 (&xf)[28], F pre, G post, A abort) {
  u32x4 wk[3], wv[3];
  wv[0] = trfrag(bk + SLAB + ord(0) * 1024); wk[0] = trfrag(bk + ord(0) * 1024);
  wv[1] = trfrag(bk + SLAB + ord(1) * 1024); wk[1] = trfrag(bk + ord(1) * 1024);
  SB();
  steps28([&](int i) __attribute__((always_inline)) {
    pre(i);
    if (i + 2 < 28) wv[(i + 2) % 3] = trfrag(bk + SLAB + ord(i + 2) * 1024);
    SB();
    av = mfma(wv[i % 3], xf[i], av);
    SB();
    if (i + 2 < 28) wk[(i + 2) % 3] = trfrag(bk + ord(i + 2) * 1024);
    post(i);
    SB();
    ak = mfma(wk[i % 3], xf[i], ak);
    SB();
  }, abort);
}
#define NOAB [] { return false; }
#define EARLYAB [&] { return __any(m == SENT) && budget != 0; }
#define LA 11
#define XH 27
#define VSLEEP 8
#define QSLEEP 28
#define LAO 14
#define XBAR 26
#define DOA 3
#define DOB 6
#define DX 3
#define KVI 6
#define KVB 3
#define NKB 5
#define XM 4
#ifndef NV0
#define NV0 7
#endif
#define NK2 8
#define NK3 9
#define PACE 8
#define LAG 8
#define PACED() do { if (PACE) asm volatile("s_nop %0" :: "n"(PACE)); SB(); } while (0)
#define PSLP 4
#define RDLY 0
#define KDLY 0
#define LAND 20
#define TKA0 4300
#define TKA 3720
#define TKB1 1730
#define TKB 1830
#define TOA0 1900
#define TOA 1800
#define TOB1 4100
#define TOB 3170
#define TX0 3950
#define TX 3230
#define BACK 8
#define NQ0 4
#define NQB 0
#define TX2 3600
#define ND0 10
#define NPO 14
#define SWS 6
#define PDELAY 14

__global__ void __launch_bounds__(512, 2) fused_kernel(const u16* __restrict__ x0, const u16* __restrict__ wqkvA, const u16* __restrict__ wphi,
                                                       const u16* __restrict__ woA, const u16* __restrict__ wqkvB, const u16* __restrict__ woB,
                                                       u16* __restrict__ out, unsigned char* ws) {
  extern __shared__ __attribute__((aligned(16))) unsigned char smem[];
  const lptr lds = (lptr)smem;
  const int tid = threadIdx.x, lane = tid & 63, wave = __builtin_amdgcn_readfirstlane(tid >> 6);
  TOT0
  const int w = wave & 3;
  const int r = lane & 31, hh = lane >> 5;
  const int b = blockIdx.x;
  const int tb = ((b >> 1) & 3) * 4 + (b >> 6), h = (b & 1) * 8 + ((b >> 3) & 7);
  const int me = tb * 16 + h;
  const int odd = h & 1, pair = h >> 1;
  const int T = tb * 4 + w;
  const unsigned TOFF = tb * 8 * XO_B + w * TILE_B;
  const int kh = w & 1, tx = 2 * odd + (w >> 1);
  const unsigned XOFF = tb * 8 * XO_B + tx * TILE_B;
  const rsrc_t rWS = __builtin_amdgcn_make_buffer_rsrc((void*)ws, 0, WS_END, 0x00020000);
  unsigned budget = SPIN_CAP;
  u64 epoch;
  u32x2 tag;
  u32x2 ev;
  auto load_epoch = [&]() { ev = __builtin_amdgcn_raw_buffer_load_b64(rWS, 0, WS_EPOCH, SC1); SB(); };
  auto make_tag = [&]() {
    epoch = (u64)(unsigned)__builtin_amdgcn_readfirstlane(ev[0]) | ((u64)(unsigned)__builtin_amdgcn_readfirstlane(ev[1]) << 32);
    u64 z = epoch + 0x9E3779B97F4A7C15ull;
    z = (z ^ (z >> 30)) * 0xBF58476D1CE4E5B9ull; z = (z ^ (z >> 27)) * 0x94D049BB133111EBull; z ^= z >> 31;
    unsigned t0 = (unsigned)z, t1 = (unsigned)(z >> 32);
    asm volatile("" : "+s"(t0), "+s"(t1));
    tag = (u32x2){t0, t1};
  };
  volatile LAS unsigned* const cnt = (volatile LAS unsigned*)(lds + L_SYNC);
  auto arrive = [&](int k) { if (lane == 0) asm volatile("ds_add_u32 %0, %1" :: "v"(lds + L_SYNC + 4 * k), "v"(1u) : "memory"); };
  auto gather_all = [&](int layer, bool nap) {
    while (__builtin_amdgcn_readfirstlane(*cnt) < 8u * layer + 4u) { if (nap) __builtin_amdgcn_s_sleep(2); }
    asm volatile("" ::: "memory");
  };
  auto poll = [&](int hop, int mask) {
    int ln = lane & mask; PIN(ln);
    u32x2 v;
    for (;;) {
      v = __builtin_amdgcn_raw_buffer_load_b64(rWS, ln * 8, WS_FLAGS + hop * HOP_B + me * 128, SC1);
      asm volatile("" ::: "memory");
      if (__all(v[0] == tag[0] && ((v[1] ^ tag[1]) >> 20) == 0)) break;
      if (budget == 0) break;
      --budget;
      if (PSLP) __builtin_amdgcn_s_sleep(PSLP);
    }
    const unsigned t = v[1] << 12;
    for (int n = 0; n < 64; ++n) {
      const unsigned now = (unsigned)__builtin_amdgcn_s_memrealtime() << 12;
      if (!__any((int)(t - now) > 0)) break;
    }
    asm volatile("" ::: "memory");
  };
  auto make_trb = [&]() {
    const int rb = (lane >> 4) & 1, q = (lane >> 2) & 3, p = lane & 3;
    const int rp = 4 * rb + p, lam = (rp & 4) | ((rp & 1) << 1) | ((rp >> 1) & 1);
    const int pc = ((odd ? 0x01327654 : 0x76543210) >> (4 * lam)) & 7;
    return lds + (8 * hh + q) * 64 + pc * 8;
  };
  auto store_x0 = [&](u32x4 f0, unsigned base) { int ln = lane; PIN(ln); st128<SC1>(f0, rWS, ln * 16, base + XOFF + (3 * pair + 2 * kh) * 1024); };
  auto store_x1 = [&](u32x4 f1, unsigned base) {
    int ln = lane; PIN(ln);
    st128<SC1>(f1, rWS, ln * 16 + __umul24(ln >> 5, 24576 - 512 - 1024 - pair * 2048), base + XOFF + (3 * pair + 1) * 1024 + kh * 512);
  };
  auto store_o = [&](u32x4 f0, u32x4 f1, unsigned base) {
    int ln = lane; PIN(ln);
    st128<SC1>(f0, rWS, ln * 16, base + TOFF + (3 * pair + 2 * odd) * 1024);
    st128<SC1>(f1, rWS, ln * 16 + (ln >> 5) * OOB, base + TOFF + (3 * pair + 1) * 1024 + odd * 512);
    __builtin_amdgcn_raw_buffer_store_b64(lo2(f1), rWS, ln * 8 + (1 - (ln >> 5)) * OOB, base + TOFF + 24576 - 256 + h * 256, SC1);
  };
  const int ht = tid & 255, sub = lane & 3;
  const int col = h * 56 - odd * 8;
  int krow[7];
  auto mkrow = [&](int i) {
    const int s = 4 * i + w;
    const int p = (s * 43) >> 7, m = s - 3 * p;
    const int c = i < 6 ? 7 * p + ((0x520 >> (4 * m)) & 15) + hh * (1 + (m & 1)) : 7 * (2 * w + hh) + 3;
    krow[i] = 8 * c + ((lane >> 2) & 7);
  };
  auto ldw = [&](rsrc_t rs, int i, int stride, int off) { int k = krow[i]; PIN(k); const u32x4 v = __builtin_amdgcn_raw_buffer_load_b128(rs, k * stride + sub * 16, col + off, 0); SB(); return v; };
  auto write_slab = [&](const u32x4* p, unsigned off) {
    lptr d = lds + off + ht * 16; PIN(d);
#pragma unroll
    for (int i = 0; i < 7; ++i) *(LAS u32x4*)(d + i * 4096) = p[i];
  };
  auto put_slab = [&](u32x4* p, int type, int part) {
    const unsigned keep01 = (odd && sub == 0) ? 0u : ~0u, keep23 = (!odd && sub == 3) ? 0u : ~0u;
#pragma unroll
    for (int i = 0; i < 7; ++i) {
      p[i][0] &= keep01; p[i][1] &= keep01; p[i][2] &= keep23; p[i][3] &= keep23;
      if (type == 0 && part == 0) { p[i][0] |= ~keep01 & 0x3f80u; p[i][2] |= ~keep23 & 0x3f80u; }
    }
    write_slab(p, part * SLAB);
  };
  auto sum_slab = [&](int type, int part, lptr trb) {
    lptr wb = trb + part * SLAB + w * 1024; PIN(wb);
    u32x4 wr[7];
#pragma unroll
    for (int i = 0; i < 7; ++i) wr[i] = trfrag(wb + i * 4096);
    f32x16 a = Z16;
#pragma unroll
    for (int i = 0; i < 7; ++i) a = mfma((u32x4){ONES, ONES, ONES, ONES}, wr[i], a);
    unsigned hi, lo; split2(a[0], hi, lo);
    if (hh == (w >> 1)) *(LAS u32x2*)(lds + L_CS + type * 3072 + part * 1024 + lane * 16 + (w & 1) * 8) = (u32x2){hi | (lo << 16), hi};
  };
  auto first_slab = [&](u32x4* p, int type, int part, lptr trb) { put_slab(p, type, part); sum_slab(type, part, trb); };
  const rsrc_t rWA = __builtin_amdgcn_make_buffer_rsrc((void*)wqkvA, 0, DM * 3 * DM * 2, 0x00020000);
  auto dma_x0 = [&](int i0, int i1) __attribute__((always_inline)) {
    const rsrc_t rX0 = __builtin_amdgcn_make_buffer_rsrc((void*)x0, 0, SEQ * DM * 2, 0x00020000);
    const int o0 = (lane >> 3) * 896 + (((lane & 7) ^ (lane >> 4)) * 16), o1 = o0 ^ 64;
#pragma unroll
    for (int i = i0; i < i1; ++i) {
      __builtin_amdgcn_raw_ptr_buffer_load_lds(rX0, (LAS void*)(lds + L_X0 + w * SLAB + i * 1024), 16, (i & 1) ? o1 : o0, (T * 32 + (i & 3) * 8) * 896 + (i >> 2) * 128, 0, 0);
      SB();
    }
  };

  if (wave < 4) {
    RSTAMP(62); STAMP(0);
    u32x4 xf[28];
    f32x16 xs, ak, av;
    float rstd;
    u32x4 mf;
    auto stats = [&](float s1, float s2) {
      const float mean = s1 * (1.0f / DM);
      rstd = __builtin_amdgcn_rsqf(fmaxf(s2 * (1.0f / DM) - mean * mean, 0.f) + 1e-5f);
      unsigned hi, lo; split2(-mean, hi, lo);
      mf = (u32x4){hi | (hi << 16), lo, hi | (hi << 16), lo};
      PIN(rstd); PIN(mf);
    };
    lptr trb;
    u32x4 id0, id1;
    float wtail;
    unsigned lomask, phoff;
    {
      __builtin_amdgcn_s_setprio(3);
#pragma unroll
      for (int i = 0; i < 7; ++i) { mkrow(i); xf[21 + i] = ldw(rWA, i, 2688, 896); }
      STAMPX(54);
      trb = make_trb();
      id0 = ident_frag(0, r, hh); id1 = ident_frag(1, r, hh);
      wtail = hh ? 0.f : 1.f; lomask = hh ? 0u : ~0u;
      PIN(trb); PIN(id0); PIN(id1); PIN(wtail); PIN(lomask);
      if (w >= 2) {
        u32x4 mk;
#pragma unroll
        for (int i = 0; i < 4; ++i) { const int k0 = ((2 * i) & 3) + 8 * (i >> 1) + 4 * hh + 16 * (w & 1); mk[i] = (k0 > r ? 0xffffu : 0u) | (k0 + 1 > r ? 0xffff0000u : 0u); }
        *(LAS u32x4*)(lds + L_MK + (w & 1) * 1024 + lane * 16) = mk;
      }
      if (w == 0) { cnt[0] = 0u; cnt[1] = 0u; cnt[2] = 0u; cnt[3] = 0u; cnt[4] = 0u; cnt[5] = 0u; cnt[6] = 0u; }
      const int lam = 4 * (w >> 1) + 2 * hh + (w & 1);
      const int pc = ((odd ? 0x01327654 : 0x76543210) >> (4 * lam)) & 7;
      unsigned off = lam == 7 ? OOB : ((h * 28 + 4 * pc - 4 * odd) * 28 + (r < 28 ? r : r - 8)) * 2;
      phoff = off; PIN(phoff);
      __builtin_amdgcn_s_setprio(0);
    }
    tag = (u32x2){0u, 0u}; epoch = 0;
    asm volatile("" : "=v"(ak), "=v"(av), "=v"(rstd), "=v"(mf), "=v"(xs));
#pragma unroll
    for (int i = 0; i < 21; ++i) asm volatile("" : "=v"(xf[i]));
    auto release = [&](int hop) { if (w == 0) poll(hop, 15); bar(); };
    for (int layer = 0; layer < NL; ++layer) {
      const int type = layer & 1;
      const int pb = 2 + layer * 14;
      if (layer > 0) {
        release(3 * layer - 1);
        STAMP(pb + 0);
        for (int pass = 0;;) {
          const unsigned base = WS_XO + (2 * layer - 1) * XO_B + TOFF;
          int v16 = lane * 16; PIN(v16);
          u32x4 sp[8];
          const int vs = v16 + (v16 & 512);
#pragma unroll
          for (int u = 0; u < 8; ++u) { sp[u] = __builtin_amdgcn_raw_buffer_load_b128(rWS, vs + ((u >> 1) & 1) * 2048 + (u & 1) * 512, base + 24576 + (u >> 2) * 4096, SC1); SB(); PACED(); }
          auto ld = [&](int s) { xf[4 + s] = __builtin_amdgcn_raw_buffer_load_b128(rWS, v16 + (s & 3) * 1024, base + (s >> 2) * 4096, SC1); SB(); };
#pragma unroll
          for (int s = 0; s < LA - 8; ++s) { ld(s); PACED(); }
          unsigned m = 0;
          lptr bk = trb + L_WK; PIN(bk);
          ak = Z16; av = Z16;
          float s1 = 0.f, s2 = 0.f;
          u32x4 ck, cv;
          gemm2(ak, av, bk, xf,
                    [&](int i) {
                      if (i < 4) { ld(LA - 8 + 2 * i); ld(LA - 7 + 2 * i); } else if (i + LA - 4 < 24) ld(i + LA - 4);
                      if (i < 4) { PIN(sp[2 * i]); PIN(sp[2 * i + 1]); xf[i] = cat(lo2(sp[2 * i]), lo2(sp[2 * i + 1])); }
                      if (i == 8) { av = mfma(cv, mf, av); ak = mfma(ck, mf, ak); SB(); }
                    },
                    [&](int i) {
                      if (i < 4) {
                        m = max4(max4(m, sp[2 * i]), sp[2 * i + 1]);
                        s1 += U2F(sp[2 * i][2]); s2 += U2F(sp[2 * i][3]); PIN(s1); PIN(s2);
                        s1 += U2F(sp[2 * i + 1][2]); s2 += U2F(sp[2 * i + 1][3]); PIN(s1); PIN(s2);
                      } else m = max4(m, xf[i]);
                      PIN(m);
                      if (i == CHK2 + 1) { if (pass == 0) arrive(2); pass = 1; }
                      if (i == 5) {
                        red2<false>(s1, s2);
                        stats(s1, s2);
                        const lptr cs = lds + L_CS + type * 3072 + v16;
                        ck = ldl(cs); cv = ldl(cs + 1024);
                      }
                    }, EARLYAB);
          if (!__any(m == SENT) || budget == 0) break;
          --budget; RET(layer * 3);
          __builtin_amdgcn_s_sleep(BACK);
          asm volatile("" ::: "memory");
        }
      }
      int first = layer; asm volatile("" : "+s"(first));
      if (first == 0) {
        int ln = lane; PIN(ln);
        const int hh = ln >> 5;
        const unsigned ra = (ln & 31) * 128 + ((ln >> 1) & 7) * 16;
        const lptr stg = lds + L_X0 + w * SLAB;
        auto kof = [](int c) { return (c >> 3) * 4096 + (c & 7) * 16; };
        lptr ad[28];
#pragma unroll
        for (int i = 0; i < 28; ++i) {
          const int k0 = kof(chunk_of(ord(i), 0)), k1 = kof(chunk_of(ord(i), 1));
          ad[i] = stg + (ra ^ k0 ^ (~lomask & (k0 ^ k1))); PIN(ad[i]);
        }
        lptr pab = lds + L_X0 + tx * SLAB + (ra ^ kof(7 * pair + 5 * kh + hh)), pc = lds + L_X0 + tx * SLAB + (ra ^ kof(7 * pair + (hh ? 3 : 2 + 2 * kh))); PIN(pab); PIN(pc);
        SB();
        first_slab(xf + 21, 0, 0, trb);
        STAMPX(53);
        bar();
        STAMPX(55);
#pragma unroll
        for (int i = 0; i < 28; ++i) xf[i] = ldl(ad[i]);
        u32x4 xa = ldl(pab), xc = ldl(pc);
        arrive(5);
        SB();
        float s2a = 0.f, s2b = 0.f;
#pragma unroll
        for (int i = 0; i < 28; ++i) {
          const u32x4 u = xf[i]; s2a = dot2(u[1], u[1], dot2(u[0], u[0], s2a)); s2b = dot2(u[3], u[3], dot2(u[2], u[2], s2b));
          PIN(s2a); PIN(s2b);
          SB();
        }
        const rsrc_t rPH = __builtin_amdgcn_make_buffer_rsrc((void*)wphi, 0, 16 * 28 * 28 * 2, 0x00020000);
        unsigned e[4];
#pragma unroll
        for (int j = 0; j < 4; ++j) e[j] = __builtin_amdgcn_raw_buffer_load_b16(rPH, phoff + j * 56, 0, 0);
        load_epoch();
        STAMP(1);
        u32x4 cc = ldl(lds + L_CS + ln * 16);
        ak = Z16;
        lptr bk = trb + L_WK; PIN(bk);
        gemm1<5>(ak, bk, xf, [](int) {}, [&](int i) {
          if (i == 4) {
            const unsigned sel = hh && kh ? 0u : ~0u;
            xc[0] = (xc[0] & sel) | (xc[2] & ~sel); xc[1] = (xc[1] & sel) | (xc[3] & ~sel); PIN(xc);
          }
          if (i >= 16 && i < 24) { const unsigned u = i < 20 ? xa[i & 3] : xc[i & 3]; xs[2 * (i - 16)] = bflo(u); xs[2 * (i - 16) + 1] = bfhi(u); PIN(xs); }
        }, NOAB);
        asm volatile("" :: "v"(ak));
        *(LAS u32x2*)(lds + L_WPH + (w >> 1) * 1024 + ln * 16 + (w & 1) * 8) = (u32x2){e[0] | (e[1] << 16), e[2] | (e[3] << 16)};
        make_tag();
        STAMPX(57);
        bar();
        STAMP(2);
        const u32x4 cv = ldl(lds + L_CS + 1024 + ln * 16);
        av = Z16;
        gemm1<5>(av, bk + SLAB, xf, [&](int i) { if (i == 8) { ak = mfma(cc, mf, ak); SB(); } if (i == 10) { av = mfma(cv, mf, av); SB(); } }, [&](int i) {
          if (i == 2) {
            float s1 = U2F(F2U(ak[12]) & ~lomask), s2 = s2a + s2b;
            red2<false>(s1, s2);
            stats(s1, s2);
          }
        }, NOAB);
      }
      asm volatile("" ::: "memory");
      arrive(0);
      STAMPX(pb + 1);
      const unsigned mid = WS_MID + (h * 4 + layer) * MID_B;
      int ln16 = lane * 16; PIN(ln16);
      u32x4 wph[2], kf0, kf1;
      if (type == 0) {
        wph[0] = ldl(lds + L_WPH + ln16); wph[1] = ldl(lds + L_WPH + 1024 + ln16);
        DUMP(0, ak, 1, rstd); DUMP(0, av, 2, rstd);
        kf0 = pack8<0>(ak); kf1 = pack8<1>(ak);
        f32x16 p = mfma(wph[0], kf0, Z16);
        p = mfma(wph[1], kf1, p);
        SB();
#pragma unroll
        for (int i = 0; i < 16; ++i) { float t = av[i] * rstd; PIN(t); av[i] = t; }
        u32x4 vb0 = pack8<0>(av), vb1 = pack8<1>(av);
        vb1[2] |= ~lomask & 0x3f80u;
        PIN(vb0); PIN(vb1);
        SB();
        u32x4 fp[2], fm[2];
        dual_softmax(p, rstd * L2E, wtail, fp, fm);
        fp[1][2] &= lomask; fp[1][3] &= lomask; fm[1][2] &= lomask; fm[1][3] &= lomask;
        PIN(fp[0]); PIN(fp[1]); PIN(fm[0]); PIN(fm[1]);
        gather_all(layer, false);
        const int rr = (ln16 >> 4) & 31, pc = (ln16 >> 9) ^ ((rr >> 1) & 3);
        const lptr d0 = lds + L_SCR + w * 2048 + rr * 64 + pc * 16, d1 = lds + L_SCR + w * 2048 + rr * 64 + (pc ^ 2) * 16;
        *(LAS u32x4*)(d0) = fp[0]; *(LAS u32x4*)(d1) = fp[1];
        *(LAS u32x4*)(d0 + 8192) = fm[0]; *(LAS u32x4*)(d1 + 8192) = fm[1];
        *(LAS u32x4*)(d0 + 16384) = vb0; *(LAS u32x4*)(d1 + 16384) = vb1;
        bar();
      } else {
        av *= rstd;
        DUMP(0, av, 2, 1.f);
        u32x4 vb1 = pack8<1>(av);
        vb1[2] |= ~lomask & 0x3f80u;
        f32x16 vt = mfma(pack8<0>(av), id0, Z16);
        vt = mfma(vb1, id1, vt);
        SB();
#pragma unroll
        for (int i = 0; i < 16; ++i) { float t = ak[i] * rstd; PIN(t); ak[i] = t; }
        DUMP(0, ak, 1, 1.f);
        kf0 = pack8<0>(ak); kf1 = pack8<1>(ak);
        const unsigned g = mid + T * 4096;
        const lptr d = lds + L_SCR + (8 + w) * 4096 + ln16;
        st128<SC1>(kf0, rWS, ln16, g);
        const u32x4 vf0 = pack8<0>(vt), vf1 = pack8<1>(vt);
        st128<SC1>(vf0, rWS, ln16 + 2048, g);
        st128<SC1>(vf1, rWS, ln16 + 3072, g);
        {
          u32x4 kc = kf1;
          kc[3] |= ~lomask & (tag[0] + layer - xhalf_addu(add4(add4(0u, kf0), kf1) + add4(add4(0u, vf0), vf1)));
          st128<SC1>(kc, rWS, ln16 + 1024, g);
          CALS(3 * layer);
        }
        gather_all(layer, false);
        *(LAS u32x4*)(d) = kf0; *(LAS u32x4*)(d + 1024) = kf1; *(LAS u32x4*)(d + 2048) = vf0; *(LAS u32x4*)(d + 3072) = vf1;
      }
      STAMPX(pb + 2);
      u32x4 qf0, qf1;
      {
        f32x16 aq = mfma(ldl(lds + L_CS + type * 3072 + 2048 + ln16), mf, Z16);
        lptr bq = trb + L_WQ; PIN(bq);
        gemm1<5>(aq, bq, xf, [](int) {}, [](int) {}, NOAB);
        aq *= type ? rstd * (0.18898223650461361f * L2E) : 1.0f;
        DUMP(0, aq, 0, type ? 1.f : rstd);
        qf0 = pack8<0>(aq); qf1 = pack8<1>(aq);
        qf1[2] |= ~lomask & 0x3f80u;
      }
      f32x16 o;
      if (type == 0) {
        u32x4 fp[2], fm[2];
        {
          f32x16 p = mfma(wph[0], qf0, Z16);
          p = mfma(wph[1], qf1, p);
          dual_softmax(p, rstd * L2E, wtail, fp, fm);
        }
        PIN(fp[0]); PIN(fp[1]); PIN(fm[0]); PIN(fm[1]);
        STAMPX(pb + 3);
        bar();
        STAMP(pb + 4);
        const lptr kvf = lds + L_KVF + ln16;
        const u32x4 a0 = ldl(kvf), a1 = ldl(kvf + 1024), a2 = ldl(kvf + 2048), a3 = ldl(kvf + 3072);
        SB();
        o = mfma(a0, fp[0], Z16);
        o = mfma(a1, fp[1], o);
        o = mfma(a2, fm[0], o);
        o = mfma(a3, fm[1], o);
        o *= __builtin_amdgcn_rcpf(upper_val(o[12]) + 1e-6f);
      } else {
        lptr kv = lds + L_SCR + w * 4096 + ln16; PIN(kv);
        const u32x4 mk0 = ldl(lds + L_MK + ln16), mk1 = ldl(lds + L_MK + 1024 + ln16);
        f32x16 sd = mfma(kf0, qf0, Z16);
        sd = mfma(kf1, qf1, sd);
        PIN(sd);
        STAMPX(pb + 3);
        bar();
        STAMP(pb + 4);
        u32x4 kk[8][2], vv[3][2];
        f32x16 st[3];
#pragma unroll
        for (int n = 0; n < 3; ++n) { kk[n][0] = ldl(kv + n * 4096); kk[n][1] = ldl(kv + n * 4096 + 1024); }
        SB();
        float m0 = -__builtin_inff(), m1 = m0;
#pragma unroll
        for (int n = 0; n < 10; ++n) {
          if (n + 3 < 8) { kk[n + 3][0] = ldl(kv + (n + 3) * 4096); kk[n + 3][1] = ldl(kv + (n + 3) * 4096 + 1024); }
          else if (n < 8) { const int t = n == 5 ? 0 : (n == 6 ? 8 : 1); vv[n - 5][0] = ldl(kv + t * 4096 + 2048); vv[n - 5][1] = ldl(kv + t * 4096 + 3072); }
          SB();
          if (n < 8) st[n % 3] = mfma(kk[n][0], qf0, Z16);
          SB();
          if (n == 2) {
#pragma unroll
            for (int i = 0; i < 8; ++i) sd[i] = ((i & 3) + 8 * (i >> 2) + 4 * hh) > r ? st[0][i] : sd[i];
          }
          if (n >= 2) {
            const f32x16& s = n == 2 ? sd : st[(n - 2) % 3];
#pragma unroll
            for (int i = 0; i < 8; i += 2) m0 = fmax3(m0, s[i], s[i + 1]);
            PIN(m0);
          }
          SB();
          if (n < 8) st[n % 3] = mfma(kk[n][1], qf1, st[n % 3]);
          SB();
          if (n == 2) {
#pragma unroll
            for (int i = 8; i < 16; ++i) sd[i] = ((i & 3) + 8 * (i >> 2) + 4 * hh) > r ? st[0][i] : sd[i];
          }
          if (n >= 2) {
            const f32x16& s = n == 2 ? sd : st[(n - 2) % 3];
#pragma unroll
            for (int i = 8; i < 16; i += 2) m1 = fmax3(m1, s[i], s[i + 1]);
            PIN(m1);
          }
          SB();
        }
        const float m = xhalf_max(fmax3(m0, m1, m1));
        f32x16 negm = splat(-m);
        PIN(negm);
        SB();
        auto ex4 = [&](f32x16& s, int i0) {
#pragma unroll
          for (int i = i0; i < i0 + 4; ++i) s[i] = __builtin_amdgcn_exp2f(s[i]);
          unsigned a = pk2(s[i0], s[i0 + 1]), c = pk2(s[i0 + 2], s[i0 + 3]); PIN(a); PIN(c);
          SB();
          return (u32x2){a, c};
        };
        u32x4 pa, pb2;
        {
          st[1] = mfma(kk[1][0], qf0, negm);
          SB();
#pragma unroll
          for (int i = 0; i < 8; ++i) { float t = sd[i] - m; PIN(t); sd[i] = t; }
          SB();
          st[1] = mfma(kk[1][1], qf1, st[1]);
          SB();
#pragma unroll
          for (int i = 8; i < 16; ++i) { float t = sd[i] - m; PIN(t); sd[i] = t; }
          SB();
          const u32x2 a = ex4(sd, 0), c = ex4(sd, 4);
          const u32x4 p0 = cat(a, c);
          u32x4 t0 = p0 & mk0; pa = p0 & ~mk0; PIN(t0); PIN(pa);
          SB();
          o = mfma(vv[0][0], t0, Z16);
          SB();
          const u32x2 e = ex4(sd, 8), f = ex4(sd, 12);
          const u32x4 p1 = cat(e, f);
          u32x4 t1 = p1 & mk1; pb2 = p1 & ~mk1; PIN(t1); PIN(pb2);
          SB();
          o = mfma(vv[0][1], t1, o);
          SB();
        }
#pragma unroll
        for (int j = 1; j < 8; ++j) {
          f32x16& s = st[j & 1];
          if (j < 7) { vv[(j + 2) % 3][0] = ldl(kv + (j + 1) * 4096 + 2048); vv[(j + 2) % 3][1] = ldl(kv + (j + 1) * 4096 + 3072); }
          if (j == 7) arrive(4);
          SB();
          const u32x2 a = ex4(s, 0);
          o = mfma(vv[j % 3][0], pa, o);
          SB();
          const u32x2 c = ex4(s, 4);
          if (j < 7) st[(j + 1) & 1] = mfma(kk[j + 1][0], qf0, negm); else o = mfma(vv[j % 3][1], pb2, o);
          SB();
          const u32x2 e = ex4(s, 8);
          if (j < 7) st[(j + 1) & 1] = mfma(kk[j + 1][1], qf1, st[(j + 1) & 1]); else o = mfma(vv[(j + 1) % 3][0], cat(a, c), o);
          SB();
          const u32x2 f = ex4(s, 12);
          if (j < 7) o = mfma(vv[j % 3][1], pb2, o); else o = mfma(vv[(j + 1) % 3][1], cat(e, f), o);
          SB();
          pa = cat(a, c); pb2 = cat(e, f);
        }
        o *= __builtin_amdgcn_rcpf(upper_val(o[12]));
      }
      DUMP(1, o, 0, 1.f);
      store_o(pack8<0>(o), pack8<1>(o), WS_XO + (2 * layer) * XO_B);
      CALS(3 * layer + 1);
      STAMPX(pb + 6);
      lptr bown, both;
      {
        int ln = lane; PIN(ln);
        const int q = (ln >> 2) & 3, rp = ((ln >> 2) & 4) + (ln & 3), lam = (rp & 4) | ((rp & 1) << 1) | ((rp >> 1) & 1);
        const int pe = lam, po = 6 + ((0x01327654 >> (4 * lam)) & 7);
        const lptr rb = lds + L_WO + kh * SLAB + (8 * (ln >> 5) + q) * 128;
        const lptr ae = rb + ((pe * 8) ^ ((q >> 1) * 64)), ao = rb + ((po * 8) ^ ((q >> 1) * 64));
        bown = kh ? ao : ae; both = kh ? ae : ao;
        PIN(bown); PIN(both);
      }
      release(3 * layer + 1);
      STAMP(pb + 7);
      f32x16 xn, xo;
      u32x4 wa[4];
        auto tr = [](lptr p) {
          bf16x4 a0 = __builtin_amdgcn_ds_read_tr16_b64_v4bf16((LAS bf16x4*)p), a1 = __builtin_amdgcn_ds_read_tr16_b64_v4bf16((LAS bf16x4*)(p + 512));
          return __builtin_bit_cast(u32x4, __builtin_shufflevector(a0, a1, 0, 1, 2, 3, 4, 5, 6, 7));
        };
      for (;;) {
        const unsigned base = WS_XO + (2 * layer) * XO_B + XOFF;
        int v16 = lane * 16; PIN(v16);
        u32x2 sp[4];
        const int vs = (v16 >> 1) + ((v16 >> 1) & 256);
#pragma unroll
        for (int u = 0; u < 4; ++u) { sp[u] = __builtin_amdgcn_raw_buffer_load_b64(rWS, vs + (u >> 1) * 1024 + (u & 1) * 256, base + 24576 + kh * 2048, SC1); SB(); if (u & 1) PACED(); }
#pragma unroll
        for (int s = 0; s < 12; ++s) { xf[2 + s] = __builtin_amdgcn_raw_buffer_load_b128(rWS, v16 + (s & 3) * 1024, base + kh * 12288 + (s >> 2) * 4096, SC1); SB(); PACED(); }
        xf[0] = cat(sp[0], sp[1]); xf[1] = cat(sp[2], sp[3]);
        u32x4 wb[4];
        wb[0] = tr(both); wb[1] = tr(both + 2048); wb[2] = tr(both + 4096); wa[0] = tr(bown); wa[1] = tr(bown + 2048); wa[2] = tr(bown + 4096);
        SB();
        xn = xs; xo = Z16;
        unsigned m = 0;
#pragma unroll
        for (int i = 0; i < 14; ++i) {
          if (i + 3 < 14) wb[(i + 3) & 3] = tr(both + (i + 3) * 2048);
          SB();
          xo = mfma(wb[i & 3], xf[i], xo);
          SB();
          m = max4(m, xf[i]); PIN(m);
          if (i >= LAG) {
            wa[(i - LAG + 3) & 3] = tr(bown + (i - LAG + 3) * 2048);
            SB();
            xn = mfma(wa[(i - LAG) & 3], xf[i - LAG], xn);
          }
          SB();
        }
        if (!__any(m == SENT) || budget == 0) break;
        --budget; RET(layer * 3 + 2);
        __builtin_amdgcn_s_sleep(BACK);
        asm volatile("" ::: "memory");
      }
      {
        const lptr ex = lds + L_WQ + ln16;
        f32x4 t[4];
#pragma unroll
        for (int i = 14 - LAG; i < 14; ++i) {
          if (i + 3 < 14) wa[(i + 3) & 3] = tr(bown + (i + 3) * 2048);
          SB();
          xn = mfma(wa[i & 3], xf[i], xn);
          SB();
          if (i == 15 - LAG) {
#pragma unroll
            for (int g = 0; g < 4; ++g) *(LAS f32x4*)(ex + w * 4096 + g * 1024) = (f32x4){xo[4 * g], xo[4 * g + 1], xo[4 * g + 2], xo[4 * g + 3]};
          }
          if (i == 11) {
            bar();
#pragma unroll
            for (int g = 0; g < 4; ++g) t[g] = *(LAS f32x4*)(ex + (w ^ 1) * 4096 + g * 1024);
          }
          SB();
        }
#pragma unroll
        for (int g = 0; g < 4; ++g) {
#pragma unroll
          for (int j = 0; j < 4; ++j) xs[4 * g + j] = xn[4 * g + j] + t[g][j];
        }
      }
      STAMPX(pb + 8);
      if (layer == NL - 1) {
        const rsrc_t rOUT = __builtin_amdgcn_make_buffer_rsrc((void*)out, 0, SEQ * DM * 2, 0x00020000);
        const u32x4 f0 = pack8<0>(xs), f1 = pack8<1>(xs);
        int ln = lane; PIN(ln);
        const int row = (ln & 31) * 896, up = ln >> 5;
        const int ob = (tb * 4 + tx) * 32 * 896 + 7 * pair * 16;
        st128<0>(f0, rOUT, row + up * 16, ob + 5 * kh * 16);
        st128<0>(f1, rOUT, row + up * OOB, ob + (2 + 2 * kh) * 16);
        __builtin_amdgcn_raw_buffer_store_b64(lo2(f1), rOUT, row + (1 - up) * OOB, ob + 3 * 16 + kh * 8, 0);
      } else {
        const u32x4 f0 = pack8<0>(xs);
        store_x0(f0, WS_XO + (2 * layer + 1) * XO_B);
        u32x4 f1 = pack8<1>(xs);
        f1[2] &= lomask; f1[3] &= lomask;
        float s1 = 0.f, s2 = 0.f, t1 = 0.f, t2 = 0.f;
#pragma unroll
        for (int j = 0; j < 4; ++j) { const unsigned u = f0[j], v = f1[j]; s1 = dot2(u, ONES, s1); s2 = dot2(u, u, s2); t1 = dot2(v, ONES, t1); t2 = dot2(v, v, t2); }
        s1 += t1; s2 += t2;
        red2<false>(s1, s2);
        f1[2] |= ~lomask & F2U(s1); f1[3] |= ~lomask & F2U(s2);
        store_x1(f1, WS_XO + (2 * layer + 1) * XO_B);
        CALS(3 * layer + 2);
        STAMPX(pb + 9);
      }
      asm volatile("" : "=v"(ak), "=v"(av), "=v"(rstd), "=v"(mf), "=v"(phoff));
#pragma unroll
      for (int i = 14; i < 28; ++i) asm volatile("" : "=v"(xf[i]));
    }
    STAMP(61); RSTAMP(63);
    TOT1
    if (b == 0 && w == 0) __builtin_amdgcn_raw_buffer_store_b64((u32x2){(unsigned)(epoch + 1), (unsigned)((epoch + 1) >> 32)}, rWS, WS_EPOCH, 0, SC1);
  } else {
    STAMPX(53);
    u32x4 pA[21], pB[21];
    unsigned tch0, tch1;
    asm volatile("s_load_dword %0, %2, 0x0\n\ts_load_dword %1, %3, 0x0" : "=&s"(tch0), "=&s"(tch1)
                 : "s"(w == 0 ? (const void*)ws : w == 1 ? (const void*)wqkvB : w == 2 ? (const void*)woA : (const void*)wphi), "s"(w < 2 ? (const void*)woB : (const void*)out));
    dma_x0(0, 4);
    unsigned warm0;
    {
      u64 pc; asm volatile("s_getpc_b64 %0" : "=s"(pc));
      const rsrc_t rs = __builtin_amdgcn_make_buffer_rsrc(w == 3 ? (void*)(pc & ~127ull) : (void*)wqkvA, 0, w == 3 ? CODE_B : DM * 3 * DM * 2, 0x00020000);
      const int n = w == 3 ? 8 : 13;
      warm0 = __builtin_amdgcn_raw_buffer_load_b32(rs, lane < n ? (lane + (w == 3 ? (b >> 3) : b * 3 + w) * n) * 128 : OOB, 0, 0);
      SB();
    }
#pragma unroll
    for (int i = 0; i < 7; ++i) { mkrow(i); PIN(krow[i]); }
    lptr trb = make_trb(); PIN(trb);
    u32x4 id0 = ident_frag(0, r, hh); PIN(id0);
    SB();
    dma_x0(4, 28);
#pragma unroll
    for (int i = 0; i < NV0; ++i) pA[14 + i] = ldw(rWA, i, 2688, 1792);
    STAMPX(54);
    asm volatile("s_waitcnt vmcnt(%2) lgkmcnt(0)" :: "s"(tch0), "s"(tch1), "n"(NV0) : "memory");
    const u32x4 sv = {SENT, SENT, SENT, SENT};
    auto clean_kv = [&](int layer) { if (layer < NL) st128<SC1>(sv, rWS, lane * 16, WS_MID + (h * 4 + layer) * MID_B + (tb * 4 + w) * 1024); };
    auto clean_o = [&](int layer) { if (layer < NL) store_o(sv, sv, WS_XO + (2 * layer) * XO_B); };
    auto clean_x = [&](int layer) { if (layer < NL - 1) { store_x0(sv, WS_XO + (2 * layer + 1) * XO_B); store_x1(sv, WS_XO + (2 * layer + 1) * XO_B); } };
    unsigned r0 = (unsigned)__builtin_amdgcn_s_memrealtime(), c0 = (unsigned)__builtin_amdgcn_s_memtime(), tann = 0;
    auto mark = [&](int cyc) {
      const unsigned r1 = (unsigned)__builtin_amdgcn_s_memrealtime(), c1 = (unsigned)__builtin_amdgcn_s_memtime();
      tann = r1 + LAND + (unsigned)((float)cyc * (float)(r1 - r0) * __builtin_amdgcn_rcpf((float)(c1 - c0)));
      r0 = r1; c0 = c1;
    };
    auto push = [&](int hop, unsigned voff) {
      CALA(hop);
      __builtin_amdgcn_raw_buffer_store_b64((u32x2){tag[0], (tag[1] & 0xfff00000u) | (tann & 0xfffffu)}, rWS, voff, WS_FLAGS + hop * HOP_B, SC1);
    };
    auto push_grp = [&](int hop) { push(hop, lane < 16 ? (tb * 16 + lane) * 128 + h * 8 : OOB); };
    auto push_mid = [&](int layer) {
      if (layer & 1) push(3 * layer, (lane < 2 && tb + 1 + lane < 16) ? ((tb + 1 + lane) * 16 + h) * 128 + lane * 8 : OOB);
      else push(3 * layer, lane < 16 ? (lane * 16 + h) * 128 + tb * 8 : OOB);
    };
    const rsrc_t rWB = __builtin_amdgcn_make_buffer_rsrc((void*)wqkvB, 0, DM * 3 * DM * 2, 0x00020000);
    const rsrc_t rOA = __builtin_amdgcn_make_buffer_rsrc((void*)woA, 0, DM * DM * 2, 0x00020000);
    const rsrc_t rOB = __builtin_amdgcn_make_buffer_rsrc((void*)woB, 0, DM * DM * 2, 0x00020000);
    auto spin = [&](int k, unsigned n) { while (__builtin_amdgcn_readfirstlane(cnt[k]) < n) __builtin_amdgcn_s_sleep(1); asm volatile("" ::: "memory"); };
    auto wait_h = [&](int hop, int mask) {
      if (w == 0) { if (mask >= 0) poll(hop, mask); cnt[1] = hop + 1; }
      else while (__builtin_amdgcn_readfirstlane(cnt[1]) != hop + 1) { }
      asm volatile("" ::: "memory");
    };
    const unsigned pkb = WS_PK + h * 262144;
    auto ldp = [&](unsigned off) { const u32x4 v = __builtin_amdgcn_raw_buffer_load_b128(rWS, lane * 16, pkb + off + w * 1024, SC1); SB(); return v; };
    STAMPX(55);
    bar();
#pragma unroll
    for (int i = NV0; i < 7; ++i) pA[14 + i] = ldw(rWA, i, 2688, 1792);
    __builtin_amdgcn_s_setprio(3);
    STAMPX(57);
    spin(5, 4);
    put_slab(pA + 14, 0, 1);
    load_epoch();
    u32x4 pw[4];
    {
      const unsigned act = tb < 14 ? 0u : OOB;
#pragma unroll
      for (int j = 0; j < 2; ++j) {
        const int st = 2 * tb + j, p = (st * 43) >> 7, m = st - 3 * p;
        const int c = st < 24 ? 7 * p + ((0x520 >> (4 * m)) & 15) + hh * (1 + (m & 1)) : 7 * (2 * (st - 24) + hh) + 3;
        pw[j] = __builtin_amdgcn_raw_buffer_load_b128(rWB, ((8 * c + ((lane >> 2) & 7)) * 2688 + sub * 16) | (w < 3 ? act : OOB), col + (w == 2 ? 0 : 896 + 896 * w), 0);
        SB();
      }
      {
        const int n = 4 * tb + w, k2 = n >= 28, i = (n - 28 * k2) >> 1, st = i - 2, p = (st * 43) >> 7, m = st - 3 * p;
        const int c = 28 * k2 + (i < 2 ? 14 * i + 3 + 7 * (n & 1) : 7 * p + ((0x520 >> (4 * m)) & 15) + (n & 1) * (1 + (m & 1)));
        const unsigned vo = (c * 7168 + (lane >> 3) * 896 + 112 * pair + (((lane & 7) ^ ((lane >> 2) & 4)) * 16)) | act;
        pw[2] = __builtin_amdgcn_raw_buffer_load_b128(rOA, vo, 0, 0); SB();
        pw[3] = __builtin_amdgcn_raw_buffer_load_b128(rOB, vo, 0, 0); SB();
      }
    }
    clean_kv(0);
    sum_slab(0, 1, trb);
#pragma unroll
    for (int j = 0; j < NQ0; ++j) pA[j] = ldw(rWA, j, 2688, 0);
#pragma unroll
    for (int i = 0; i < 7; ++i) pA[7 + i] = ldl(lds + L_WK + i * 4096 + ht * 16);
    STAMPX(58);
    bar();
    if (w == 3) mark(TKA0);
    __builtin_amdgcn_s_setprio(0);
    {
      const unsigned keep01 = (odd && sub == 0) ? 0u : ~0u, keep23 = (!odd && sub == 3) ? 0u : ~0u, act = (tb < 14 ? 0u : OOB) | (lane * 16);
#pragma unroll
      for (int j = 0; j < 2; ++j) st128<SC1>(pw[j] & (u32x4){keep01, keep01, keep23, keep23}, rWS, w < 3 ? act : OOB, pkb + w * SLAB + (2 * tb + j) * 1024);
      st128<SC1>(pw[2], rWS, act, pkb + 3 * SLAB + (4 * tb + w) * 1024);
      st128<SC1>(pw[3], rWS, act, pkb + 5 * SLAB + (4 * tb + w) * 1024);
    }
    clean_o(0);
#pragma unroll
    for (int j = NQ0; j < 7; ++j) pA[j] = ldw(rWA, j, 2688, 0);
    make_tag();
    STAMPX(59);
    put_slab(pA, 0, 2);
    drain();
    arrive(3);
    sum_slab(0, 2, trb);
    if (w == 3) { spin(3, 4); push_mid(0); }
    asm volatile("" :: "v"(warm0));
    __builtin_amdgcn_s_setprio(3);
    u32x4 po[14];
    auto swap_wo = [&](bool keep) {
      lptr d = lds + L_WO + ht * 16; PIN(d);
#pragma unroll
      for (int i = 0; i < 14; ++i) { u32x4 t; if (keep) t = ldl(d + i * 4096); *(LAS u32x4*)(d + i * 4096) = po[i]; if (keep) po[i] = t; SB(); }
    };
    STAMPX(60);
#pragma unroll
    for (int layer = 0; layer < NL; ++layer) {
      const int type = layer & 1;
      const int pb = 2 + layer * 14;
      const unsigned mid = WS_MID + (h * 4 + layer) * MID_B;
      if (type == 0) {
        lptr ta0, ta1;
        {
          const int rb = (lane >> 4) & 1, q = (lane >> 2) & 3, p = lane & 3;
          const int rp = 4 * rb + p, lam = (rp & 4) | ((rp & 1) << 1) | ((rp >> 1) & 1);
          ta0 = lds + L_SCR + (8 * hh + q) * 64 + (((lam >> 1) ^ (q >> 1)) * 16) + (lam & 1) * 8;
          ta1 = lds + L_SCR + (8 * hh + q + 4) * 64 + (((lam >> 1) ^ (q >> 1) ^ 2) * 16) + (lam & 1) * 8;
          PIN(ta0); PIN(ta1);
        }
        bar();
        STAMPX(pb + 0);
        {
          auto tr2 = [&](unsigned off) {
            bf16x4 a0 = __builtin_amdgcn_ds_read_tr16_b64_v4bf16((LAS bf16x4*)(ta0 + off));
            bf16x4 a1 = __builtin_amdgcn_ds_read_tr16_b64_v4bf16((LAS bf16x4*)(ta1 + off));
            return __builtin_bit_cast(u32x4, __builtin_shufflevector(a0, a1, 0, 1, 2, 3, 4, 5, 6, 7));
          };
          const unsigned so = (w >> 1) * 8192;
          u32x4 fa[4], fb[4];
          f32x16 kv = Z16;
#pragma unroll
          for (int i = 0; i < 3; ++i) { fa[i] = tr2(so + i * 1024); fb[i] = tr2(16384 + i * 1024); }
          SB();
#pragma unroll
          for (int i = 0; i < 8; ++i) {
            if (i + 3 < 8) { fa[(i + 3) & 3] = tr2(so + (i + 3) * 1024); fb[(i + 3) & 3] = tr2(16384 + (i + 3) * 1024); }
            SB();
            kv = mfma(fa[i & 3], fb[i & 3], kv);
            SB();
          }
          st128<SC1>((w & 1) ? pack8<1>(kv) : pack8<0>(kv), rWS, lane * 16, mid + (tb * 4 + w) * 1024);
          CALS(3 * layer);
        }
        STAMPX(pb + 1);
        if (layer == 0) clean_x(0);
        wait_h(3 * layer, 15);
        STAMPX(pb + 2);
        f32x16 sum;
        for (;;) {
          sum = Z16;
          unsigned m = 0;
          u32x4 pr[16];
          const unsigned g = mid + w * 1024;
#pragma unroll
          for (int i = 0; i < 16; ++i) {
            pr[i] = __builtin_amdgcn_raw_buffer_load_b128(rWS, lane * 16, g + i * 4096, SC1); SB();
            if (i < 15) PACED();
          }
          if (w == 3) { mark(layer ? TOA : TOA0); push_grp(3 * layer + 1); SB(); }
#pragma unroll
          for (int i = 0; i < 16; ++i) {
            if (layer == 0 && i < NPO) po[i] = ldp(3 * SLAB + i * 4096);
            sum = mfma(id0, pr[i], sum); m = max4(m, pr[i]); PIN(m); SB();
          }
          if (!__any(m == SENT) || budget == 0) break;
          --budget; RET(layer * 3 + 1);
          asm volatile("" ::: "memory");
        }
        *(LAS u32x4*)(lds + L_KVF + w * 1024 + lane * 16) = pack8<0>(sum);
        STAMPX(pb + 3);
        bar();
        if (layer == 0) {
#pragma unroll
          for (int i = NPO; i < 14; ++i) po[i] = ldp(3 * SLAB + i * 4096);
        } else __builtin_amdgcn_s_sleep(8);
      } else {
        gather_all(layer, true);
        STAMPX(pb + 0);
        u32x4 t[8];
        wait_h(3 * layer, tb >= 2 ? 1 : tb - 1);
        STAMPX(pb + 2);
        if (tb - 2 + (w >> 1) >= 0) {
          const unsigned g = mid + (tb * 4 - 8 + 2 * w) * 4096;
          for (;;) {
            unsigned c[2] = {0u, 0u};
#pragma unroll
            for (int i = 0; i < 8; ++i) { t[i] = __builtin_amdgcn_raw_buffer_load_b128(rWS, lane * 16 + (i & 3) * 1024, g + (i >> 2) * 4096, SC1); SB(); if (i < 7) PACED(); }
#pragma unroll
            for (int i = 0; i < 8; ++i) {
              c[i >> 2] = add4(c[i >> 2], t[i]); PIN(c[i >> 2]);
              if ((i & 3) == 1) t[i][3] &= (unsigned)hh - 1u;
              *(LAS u32x4*)(lds + L_SCR + 2 * w * 4096 + i * 1024 + lane * 16) = t[i];
              SB();
            }
            const auto cs = __builtin_amdgcn_permlane32_swap(c[0], c[1], false, false);
            if (__all(cs[0] + cs[1] == tag[0] + layer) || budget == 0) break;
            --budget; RET(layer * 3 + 1);
            __builtin_amdgcn_s_sleep(8);
            asm volatile("" ::: "memory");
          }
        } else {
#pragma unroll
          for (int i = 0; i < 8; ++i)
            *(LAS u32x4*)(lds + L_SCR + 2 * w * 4096 + i * 1024 + lane * 16) = (u32x4){0u, 0u, ((i & 3) == 1 && hh) ? 0xff80u : 0u, 0u};
        }
        STAMPX(pb + 3);
        bar();
        if (w == 3) mark(layer == 1 ? TOB1 : TOB);
        clean_o(layer);
        drain();
        arrive(3);
        if (w == 3) { while (__builtin_amdgcn_readfirstlane(cnt[3]) < 2u * layer + 6u) { } asm volatile("" ::: "memory"); }
        if (w == 3) push_grp(3 * layer + 1);
        if (layer == 1) {
          clean_x(1); clean_kv(2);
#pragma unroll
          for (int i = 0; i < 14; ++i) po[i] = ldp(5 * SLAB + i * 4096);
          swap_wo(true);
          spin(4, 4);
        }
      }
      __builtin_amdgcn_s_setprio(0);
      if (layer == 0) {
        swap_wo(false);
      } else if (layer < NL - 1) {
        write_slab((type == 0 ? pB : pA) + 7, L_WK); write_slab((type == 0 ? pB : pA) + 14, L_WV);
        drain();
      }
      STAMPX(pb + 4);
      bar();
      if (w == 3 && layer < NL - 1) { mark(layer == 2 ? TX2 : layer ? TX : TX0); push_grp(3 * layer + 2); }
      STAMPX(pb + 5);
      if (layer == 0) {
        __builtin_amdgcn_s_sleep(12);
#pragma unroll
        for (int i = 0; i < 21; ++i) pB[(i + 7) % 21] = ldp(i * 4096);
      }
      bar();
      if (layer < NL - 1) {
        __builtin_amdgcn_s_sleep(SWS);
        if (layer == 0) { write_slab(pB + 7, L_WK); write_slab(pB + 14, L_WV); sum_slab(1, 0, trb); sum_slab(1, 1, trb); }
        else swap_wo(layer == 1);
        bar();
        write_slab(type == 0 ? pB : pA, L_WQ);
        if (layer == 0) sum_slab(1, 2, trb);
        arrive(0);
        spin(2, 4u * layer + 4u);
        if (w == 3) { mark(layer == 0 ? TKB1 : layer == 1 ? TKA : TKB); push_mid(layer + 1); }
        if (layer == 1) { __builtin_amdgcn_s_sleep(6); clean_o(2); clean_x(2); drain(); }
        __builtin_amdgcn_s_setprio(3);
      }
    }
  }
}
}

extern "C" void kernel_launch(void* const* d_in, const int* in_sizes, int n_in, void* d_out, int out_size, void* d_ws, size_t ws_size, hipStream_t stream) {
  (void)hipFuncSetAttribute((const void*)fused_kernel, hipFuncAttributeMaxDynamicSharedMemorySize, L_END);
  fused_kernel<<<256, 512, L_END, stream>>>((const u16*)d_in[0], (const u16*)d_in[1], (const u16*)d_in[2], (const u16*)d_in[3],
                                            (const u16*)d_in[5], (const u16*)d_in[6], (u16*)d_out, (unsigned char*)d_ws);
}
```

```cpp
#define NL 4
#include <hip/hip_runtime.h>
#include <hip/hip_bf16.h>

typedef unsigned short u16;
typedef unsigned long long u64;
typedef __bf16 bf16x8 __attribute__((ext_vector_type(8)));
typedef __bf16 bf16x4 __attribute__((ext_vector_type(4)));
typedef __bf16 bf16x2 __attribute__((ext_vector_type(2)));
typedef float f32x16 __attribute__((ext_vector_type(16)));
typedef float f32x4 __attribute__((ext_vector_type(4)));
typedef float f32x2 __attribute__((ext_vector_type(2)));
typedef unsigned u32x4 __attribute__((ext_vector_type(4)));
typedef unsigned u32x2 __attribute__((ext_vector_type(2)));
#define LAS __attribute__((address_space(3)))
#define GAS __attribute__((address_space(1)))
#define DI __device__ __forceinline__
#define SB() __builtin_amdgcn_sched_barrier(0)
#define PIN(x) asm volatile("" : "+v"(x))
typedef __amdgpu_buffer_rsrc_t rsrc_t;
typedef LAS unsigned char* lptr;

namespace {
constexpr int SEQ = 2048, DM = 448;
constexpr int SC1 = 16;
constexpr unsigned WS_EPOCH = 0;
constexpr unsigned WS_FLAGS = 4096;
constexpr unsigned HOP_B = 256 * 128;
constexpr unsigned TILE_B = 32768;
constexpr unsigned XO_B = 4 * TILE_B;
constexpr unsigned WS_XO = 1u << 20;
constexpr unsigned MID_B = 64 * 4096;
constexpr unsigned WS_MID = WS_XO + 16 * 8 * XO_B;
constexpr unsigned WS_PK = WS_MID + 64 * MID_B;
constexpr unsigned WS_PROF = WS_PK + 16 * 262144;
constexpr unsigned WS_END = WS_PROF + 256 * 2048;
constexpr unsigned SLAB = 448 * 64;
constexpr unsigned L_WK = 0, L_WV = SLAB, L_WQ = 2 * SLAB, L_WO = 3 * SLAB;
constexpr unsigned L_SCR = 0;
constexpr unsigned L_X0 = SLAB;
constexpr unsigned L_KVF = 5 * SLAB;
constexpr unsigned L_MK = L_KVF + 4096;
constexpr unsigned L_WPH = L_MK + 2048;
constexpr unsigned L_CS = L_WPH + 2048;
constexpr unsigned L_SYNC = L_CS + 6144;
constexpr unsigned L_PROF = L_SYNC + 64;
constexpr unsigned L_END = L_PROF + 2048;
static_assert(L_END <= 163840 && L_X0 + 4 * SLAB <= L_KVF, "lds");
constexpr unsigned SPIN_CAP = 1u << 15;
constexpr unsigned SENT = 0xffffffffu;
constexpr unsigned OOB = 0x7ff00000u;
constexpr unsigned ONES = 0x3f803f80u;
constexpr unsigned CODE_B = 36 * 1024;
constexpr float L2E = 1.4426950408889634f;

DI f32x16 mfma(u32x4 a, u32x4 b, f32x16 c) {
  return __builtin_amdgcn_mfma_f32_32x32x16_bf16(__builtin_bit_cast(bf16x8, a), __builtin_bit_cast(bf16x8, b), c, 0, 0, 0);
}
DI unsigned pk2(float a, float b) { f32x2 t = {a, b}; return __builtin_bit_cast(unsigned, __builtin_convertvector(t, bf16x2)); }
template <int S_> DI u32x4 pack8(const f32x16& c) {
  return (u32x4){pk2(c[8 * S_], c[8 * S_ + 1]), pk2(c[8 * S_ + 2], c[8 * S_ + 3]), pk2(c[8 * S_ + 4], c[8 * S_ + 5]), pk2(c[8 * S_ + 6], c[8 * S_ + 7])};
}
DI u32x4 cat(u32x2 a, u32x2 b) { return (u32x4){a[0], a[1], b[0], b[1]}; }
DI u32x2 lo2(u32x4 v) { return (u32x2){v[0], v[1]}; }
DI unsigned umax3(unsigned a, unsigned b, unsigned c) { return max(max(a, b), c); }
DI unsigned max4(unsigned m, u32x4 v) { return umax3(umax3(m, v[0], v[1]), v[2], v[3]); }
DI unsigned add4(unsigned m, u32x4 v) { return (m + v[0] + v[1]) + v[2] + v[3]; }
DI unsigned xhalf_addu(unsigned v) { auto r = __builtin_amdgcn_permlane32_swap(v, v, false, false); return r[0] + r[1]; }
DI float fmax3(float a, float b, float c) { return fmaxf(fmaxf(a, b), c); }
template <int CTRL> DI float dppadd(float v) { return v + __builtin_bit_cast(float, __builtin_amdgcn_update_dpp(0, __builtin_bit_cast(int, v), CTRL, 0xf, 0xf, true)); }
DI float fmin3(float a, float b, float c) { return fminf(fminf(a, b), c); }
DI f32x16 splat(float v) { f32x16 z; _Pragma("unroll") for (int i = 0; i < 16; ++i) z[i] = v; return z; }
#define Z16 splat(0.f)
DI u32x4 trfrag(lptr p) {
  bf16x4 a0 = __builtin_amdgcn_ds_read_tr16_b64_v4bf16((LAS bf16x4*)p);
  bf16x4 a1 = __builtin_amdgcn_ds_read_tr16_b64_v4bf16((LAS bf16x4*)(p + 256));
  return __builtin_bit_cast(u32x4, __builtin_shufflevector(a0, a1, 0, 1, 2, 3, 4, 5, 6, 7));
}
DI u32x4 ldl(lptr p) { return *(LAS u32x4*)p; }
DI float U2F(unsigned u) { return __uint_as_float(u); }
DI unsigned F2U(float f) { return __float_as_uint(f); }
template <bool MX> DI void red2(float& a, float& b) {
  auto r = __builtin_amdgcn_permlane32_swap(F2U(a), F2U(b), false, false);
  const float t = MX ? fmaxf(U2F(r[0]), U2F(r[1])) : U2F(r[0]) + U2F(r[1]);
  r = __builtin_amdgcn_permlane32_swap(F2U(t), F2U(t), false, false);
  a = U2F(r[0]); b = U2F(r[1]);
}
DI float xhalf_max(float v) { auto r = __builtin_amdgcn_permlane32_swap(F2U(v), F2U(v), false, false); return fmaxf(U2F(r[0]), U2F(r[1])); }
DI float upper_val(float v) { auto r = __builtin_amdgcn_permlane32_swap(F2U(v), F2U(v), false, false); return U2F(r[1]); }
DI float dot2(unsigned u, unsigned v, float c) { return __builtin_amdgcn_fdot2_f32_bf16(__builtin_bit_cast(bf16x2, u), __builtin_bit_cast(bf16x2, v), c, false); }
DI float bflo(unsigned u) { return U2F(u << 16); }
DI float bfhi(unsigned u) { return U2F(u & 0xffff0000u); }
DI void split2(float v, unsigned& hi, unsigned& lo) { hi = pk2(v, v) & 0xffffu; lo = pk2(v - bflo(hi), 0.f) & 0xffffu; }
template <int AUX> DI void st128(u32x4 v, rsrc_t rs, unsigned voff, unsigned soff) {
  SB();
  __builtin_amdgcn_raw_buffer_store_b128(v, rs, voff, soff, AUX);
  SB();
  asm volatile("s_nop 1" :: "v"(v));
  SB();
}
DI void drain() { asm volatile("s_waitcnt vmcnt(0)" ::: "memory"); }
DI void bar() { SB(); asm volatile("s_waitcnt lgkmcnt(0)\n\ts_barrier" ::: "memory"); SB(); }
constexpr int chunk_of(int s, int hh) { return s < 24 ? 7 * (s / 3) + (s % 3 == 0 ? hh : (s % 3 == 1 ? 2 + 2 * hh : 5 + hh)) : 7 * (2 * (s - 24) + hh) + 3; }
constexpr int ord(int i) { return i < 4 ? 24 + i : i - 4; }

#define STAMP(i) do { } while (0)
#define RET(i) do { } while (0)
#define RSTAMP(i) do { } while (0)
#define STAMPX(i) STAMP(i)
#define CALS(hop) do { } while (0)
#define CALA(hop) do { } while (0)
#define TOT0
#define TOT1
#define DUMP(buf, acc, part, sc) do { } while (0)

DI void dual_softmax(const f32x16& p, float c, float wtail, u32x4 (&fp)[2], u32x4 (&fm)[2]) {
  float mx = fmax3(p[0], p[1], p[2]), mn = fmax3(-p[0], -p[1], -p[2]);
#pragma unroll
  for (int i = 3; i < 15; i += 2) { mx = fmax3(mx, p[i], p[i + 1]); mn = fmax3(mn, -p[i], -p[i + 1]); }
  mx = fmax3(mx, p[15], p[15]); mn = fmax3(mn, -p[15], -p[15]);
  red2<true>(mx, mn);
  const float a = -c * mx, b = -c * mn;
  float nc = -c; PIN(nc);
  f32x16 ep = p * c + a, em = p * nc + b;
  SB();
#pragma unroll
  for (int i = 0; i < 16; ++i) ep[i] = __builtin_amdgcn_exp2f(ep[i]);
#pragma unroll
  for (int i = 0; i < 16; ++i) em[i] = __builtin_amdgcn_exp2f(em[i]);
  f32x2 sp = {ep[0], ep[1]}, sm = {em[0], em[1]};
#pragma unroll
  for (int i = 2; i < 12; i += 2) { sp += (f32x2){ep[i], ep[i + 1]}; sm += (f32x2){em[i], em[i + 1]}; }
  sp += wtail * ((f32x2){ep[12], ep[13]} + (f32x2){ep[14], ep[15]});
  sm += wtail * ((f32x2){em[12], em[13]} + (f32x2){em[14], em[15]});
  float u = sp[0] + sp[1], v = sm[0] + sm[1];
  red2<false>(u, v);
  ep *= __builtin_amdgcn_rcpf(u); em *= __builtin_amdgcn_rcpf(v);
  fp[0] = pack8<0>(ep); fp[1] = pack8<1>(ep); fm[0] = pack8<0>(em); fm[1] = pack8<1>(em);
}
DI u32x4 ident_frag(int s, int r, int hh) {
  const unsigned val = 0x3f80u << (16 * (r & 1));
  const int idx = (((r >> 2) ^ hh) & 1) * 8 + (r >> 4) * 4 + ((r >> 2) & 2) + ((r >> 1) & 1) - 4 * s;
  return (u32x4){idx == 0 ? val : 0u, idx == 1 ? val : 0u, idx == 2 ? val : 0u, idx == 3 ? val : 0u};
}
#define CHK1 3
#define CHK2 12
template <typename S, typename A> DI void steps28(S step, A abort) {
#pragma unroll
  for (int i = 0; i <= CHK1; ++i) step(i);
  if (abort()) return;
#pragma unroll
  for (int i = CHK1 + 1; i < 28; ++i) step(i);
}
template <int PD, typename F, typename G, typename A> DI void gemm1(f32x16& acc, lptr wb, const u32x4 (&xf)[28], F pre, G post, A abort) {
  u32x4 wr[PD];
#pragma unroll
  for (int i = 0; i < PD - 1; ++i) wr[i] = trfrag(wb + ord(i) * 1024);
  SB();
  steps28([&](int i) __attribute__((always_inline)) {
    pre(i);
    if (i + PD - 1 < 28) wr[(i + PD - 1) % PD] = trfrag(wb + ord(i + PD - 1) * 1024);
    SB();
    acc = mfma(wr[i % PD], xf[i], acc);
    SB();
    post(i);
    SB();
  }, abort);
}
template <typename H, typename F, typename G, typename A>
DI void gemm2(f32x16& ak, f32x16& av, lptr bk, const u32x4 (&xf)[28], H head, F pre, G post, A abort) {
  u32x4 wk[3], wv[3];
  head([&](int u) {
    if (u == 0) wv[0] = trfrag(bk + SLAB + ord(0) * 1024);
    if (u == 1) wk[0] = trfrag(bk + ord(0) * 1024);
    if (u == 2) wv[1] = trfrag(bk + SLAB + ord(1) * 1024);
    if (u == 3) wk[1] = trfrag(bk + ord(1) * 1024);
    SB();
  });
  steps28([&](int i) __attribute__((always_inline)) {
    pre(i);
    if (i + 2 < 28) wv[(i + 2) % 3] = trfrag(bk + SLAB + ord(i + 2) * 1024);
    SB();
    av = mfma(wv[i % 3], xf[i], av);
    SB();
    if (i + 2 < 28) wk[(i + 2) % 3] = trfrag(bk + ord(i + 2) * 1024);
    post(i, 0);
    SB();
    ak = mfma(wk[i % 3], xf[i], ak);
    SB();
    post(i, 1);
    SB();
  }, abort);
}
#define NOAB [] { return false; }
#define EARLYAB [&] { return __builtin_expect(__any(m == sent), 0); }
#define LA 12
#define XH 27
#define VSLEEP 8
#define QSLEEP 28
#define LAO 8
#define XBAR 26
#define DOA 3
#define DOB 6
#define DX 3
#define KVI 6
#define KVB 3
#define NKB 5
#define XM 4
#ifndef NV0
#define NV0 7
#endif
#define NK2 8
#define NK3 9
#define PACE 8
#define LAG 6
#define PACED() do { if (PACE) asm volatile("s_nop %0" :: "n"(PACE)); SB(); } while (0)
#define PSLP 4
#define RDLY 0
#define KDLY 0
#define LAND 10
#define TKA0 4100
#define TKA 3600
#define TKB1 1950
#define TKB 2050
#define TOA0 2050
#define TOA 1850
#define TOB1 2750
#define TOB 2520
#define TX0 3800
#define TX 2850
#define BACK 8
#define NQ0 7
#define NQB 0
#define TX2 3200
#define ND0 10
#define NPO 14
#define SWS 6
#define LMAX 1.8446744e19f
#define CSL 2
#define WSL 4
#define PACE2() do { asm volatile("s_nop %0" :: "n"(PACE - 2)); SB(); } while (0)
#define NQ1 4
#define PDELAY 14

__global__ void __launch_bounds__(512, 2) fused_kernel(const u16* __restrict__ x0, const u16* __restrict__ wqkvA, const u16* __restrict__ wphi,
                                                       const u16* __restrict__ woA, const u16* __restrict__ wqkvB, const u16* __restrict__ woB,
                                                       u16* __restrict__ out, unsigned char* ws) {
  extern __shared__ __attribute__((aligned(16))) unsigned char smem[];
  u64 tstart; asm volatile("s_memrealtime %0\n\ts_waitcnt lgkmcnt(0)" : "=s"(tstart) :: "memory"); SB();
  unsigned probe = 0;
  const lptr lds = (lptr)smem;
  const int tid = threadIdx.x, lane = tid & 63, wave = __builtin_amdgcn_readfirstlane(tid >> 6);
  TOT0
  const int w = wave & 3;
  const int r = lane & 31, hh = lane >> 5;
  const int b = blockIdx.x;
  const int tb = ((b >> 1) & 3) * 4 + (b >> 6), h = (b & 1) * 8 + ((b >> 3) & 7);
  const int me = tb * 16 + h;
  const int odd = h & 1, pair = h >> 1;
  const int T = tb * 4 + w;
  const unsigned TOFF = tb * 8 * XO_B + w * TILE_B;
  const int kh = w & 1, tx = 2 * odd + (w >> 1);
  const unsigned XOFF = tb * 8 * XO_B + tx * TILE_B;
  const rsrc_t rWS = __builtin_amdgcn_make_buffer_rsrc((void*)ws, 0, WS_END, 0x00020000);
  unsigned budget = SPIN_CAP;
  u64 epoch;
  u32x2 tag;
  u32x2 ev;
  auto load_epoch = [&]() { ev = __builtin_amdgcn_raw_buffer_load_b64(rWS, 0, WS_EPOCH, SC1); SB(); };
  auto make_tag = [&]() {
    epoch = (u64)(unsigned)__builtin_amdgcn_readfirstlane(ev[0]) | ((u64)(unsigned)__builtin_amdgcn_readfirstlane(ev[1]) << 32);
    u64 z = epoch + 0x9E3779B97F4A7C15ull;
    z = (z ^ (z >> 30)) * 0xBF58476D1CE4E5B9ull; z = (z ^ (z >> 27)) * 0x94D049BB133111EBull; z ^= z >> 31;
    unsigned t0 = (unsigned)z, t1 = (unsigned)(z >> 32);
    asm volatile("" : "+s"(t0), "+s"(t1));
    tag = (u32x2){t0, t1};
  };
  volatile LAS unsigned* const cnt = (volatile LAS unsigned*)(lds + L_SYNC);
  auto arrive = [&](int k) { if (lane == 0) asm volatile("ds_add_u32 %0, %1" :: "v"(lds + L_SYNC + 4 * k), "v"(1u) : "memory"); };
  auto gather_all = [&](int layer, bool nap) {
    while (__builtin_amdgcn_readfirstlane(*cnt) < 8u * layer + 4u) { if (nap) __builtin_amdgcn_s_sleep(2); }
    asm volatile("" ::: "memory");
  };
  auto poll = [&](int hop, int mask) {
    int ln = lane & mask; PIN(ln);
    u32x2 v;
    for (;;) {
      v = __builtin_amdgcn_raw_buffer_load_b64(rWS, ln * 8, WS_FLAGS + hop * HOP_B + me * 128, SC1);
      asm volatile("" ::: "memory");
      if (__all(v[0] == tag[0] && ((v[1] ^ tag[1]) >> 20) == 0)) break;
      if (budget <= 1) break;
      --budget;
      if (PSLP) __builtin_amdgcn_s_sleep(PSLP);
    }
    const unsigned t = v[1] << 12;
    for (int n = 0; n < 64; ++n) {
      const unsigned now = (unsigned)__builtin_amdgcn_s_memrealtime() << 12;
      if (!__any((int)(t - now) > 0)) break;
    }
    asm volatile("" ::: "memory");
  };
  auto make_trb = [&]() {
    const int rb = (lane >> 4) & 1, q = (lane >> 2) & 3, p = lane & 3;
    const int rp = 4 * rb + p, lam = (rp & 4) | ((rp & 1) << 1) | ((rp >> 1) & 1);
    const int pc = ((odd ? 0x01327654 : 0x76543210) >> (4 * lam)) & 7;
    return lds + (8 * hh + q) * 64 + pc * 8;
  };
  auto store_x0 = [&](u32x4 f0, unsigned base) { int ln = lane; PIN(ln); st128<SC1>(f0, rWS, ln * 16, base + XOFF + (3 * pair + 2 * kh) * 1024); };
  auto store_x1 = [&](u32x4 f1, unsigned base) {
    int ln = lane; PIN(ln);
    st128<SC1>(f1, rWS, ln * 16 + __umul24(ln >> 5, 24576 - 512 - 1024 - pair * 2048), base + XOFF + (3 * pair + 1) * 1024 + kh * 512);
  };
  auto store_o = [&](u32x4 f0, u32x4 f1, unsigned base) {
    int ln = lane; PIN(ln);
    st128<SC1>(f0, rWS, ln * 16, base + TOFF + (3 * pair + 2 * odd) * 1024);
    st128<SC1>(f1, rWS, ln * 16 + (ln >> 5) * OOB, base + TOFF + (3 * pair + 1) * 1024 + odd * 512);
    __builtin_amdgcn_raw_buffer_store_b64(lo2(f1), rWS, ln * 8 + (1 - (ln >> 5)) * OOB, base + TOFF + 24576 - 256 + h * 256, SC1);
  };
  const int ht = tid & 255, sub = lane & 3;
  const int col = h * 56 - odd * 8;
  int krow[7];
  auto mkrow = [&](int i) {
    const int s = 4 * i + w;
    const int p = (s * 43) >> 7, m = s - 3 * p;
    const int c = i < 6 ? 7 * p + ((0x520 >> (4 * m)) & 15) + hh * (1 + (m & 1)) : 7 * (2 * w + hh) + 3;
    krow[i] = 8 * c + ((lane >> 2) & 7);
  };
  auto ldw = [&](rsrc_t rs, int i, int stride, int off) { int k = krow[i]; PIN(k); const u32x4 v = __builtin_amdgcn_raw_buffer_load_b128(rs, k * stride + sub * 16, col + off, 0); SB(); return v; };
  auto write_slab = [&](const u32x4* p, unsigned off) {
    lptr d = lds + off + ht * 16; PIN(d);
#pragma unroll
    for (int i = 0; i < 7; ++i) *(LAS u32x4*)(d + i * 4096) = p[i];
  };
  auto put_slab = [&](u32x4* p, int type, int part) {
    const unsigned keep01 = (odd && sub == 0) ? 0u : ~0u, keep23 = (!odd && sub == 3) ? 0u : ~0u;
#pragma unroll
    for (int i = 0; i < 7; ++i) {
      p[i][0] &= keep01; p[i][1] &= keep01; p[i][2] &= keep23; p[i][3] &= keep23;
      if (type == 0 && part == 0) { p[i][0] |= ~keep01 & 0x3f80u; p[i][2] |= ~keep23 & 0x3f80u; }
    }
    write_slab(p, part * SLAB);
  };
  auto sum_slab = [&](int type, int part, lptr trb) {
    lptr wb = trb + part * SLAB + w * 1024; PIN(wb);
    u32x4 wr[7];
#pragma unroll
    for (int i = 0; i < 7; ++i) wr[i] = trfrag(wb + i * 4096);
    f32x16 a = Z16;
#pragma unroll
    for (int i = 0; i < 7; ++i) a = mfma((u32x4){ONES, ONES, ONES, ONES}, wr[i], a);
    unsigned hi, lo; split2(a[0], hi, lo);
    if (hh == (w >> 1)) *(LAS u32x2*)(lds + L_CS + type * 3072 + part * 1024 + lane * 16 + (w & 1) * 8) = (u32x2){hi | (lo << 16), hi};
  };
  auto first_slab = [&](u32x4* p, int type, int part, lptr trb) { put_slab(p, type, part); sum_slab(type, part, trb); };
  const rsrc_t rWA = __builtin_amdgcn_make_buffer_rsrc((void*)wqkvA, 0, DM * 3 * DM * 2, 0x00020000);
  auto dma_x0 = [&](int i0, int i1) __attribute__((always_inline)) {
    const rsrc_t rX0 = __builtin_amdgcn_make_buffer_rsrc((void*)x0, 0, SEQ * DM * 2, 0x00020000);
    const int o0 = (lane >> 3) * 896 + (((lane & 7) ^ (lane >> 4)) * 16), o1 = o0 ^ 64;
#pragma unroll
    for (int i = i0; i < i1; ++i) {
      __builtin_amdgcn_raw_ptr_buffer_load_lds(rX0, (LAS void*)(lds + L_X0 + w * SLAB + i * 1024), 16, (i & 1) ? o1 : o0, (T * 32 + (i & 3) * 8) * 896 + (i >> 2) * 128, 0, 0);
      SB();
    }
  };

  if (wave < 4) {
    RSTAMP(62); STAMP(0);
    u32x4 xf[28];
    f32x16 xs, ak, av;
    float rstd;
    u32x4 mf;
    auto stats = [&](float s1, float s2) {
      const float mean = s1 * (1.0f / DM);
      rstd = __builtin_amdgcn_rsqf(fmaxf(s2 * (1.0f / DM) - mean * mean, 0.f) + 1e-5f);
      unsigned hi, lo; split2(-mean, hi, lo);
      mf = (u32x4){hi | (hi << 16), lo, hi | (hi << 16), lo};
      PIN(rstd); PIN(mf);
    };
    lptr trb;
    u32x4 id0, id1;
    float wtail;
    unsigned lomask, phoff;
    {
      __builtin_amdgcn_s_setprio(3);
#pragma unroll
      for (int i = 0; i < 7; ++i) { mkrow(i); xf[21 + i] = ldw(rWA, i, 2688, 896); }
      STAMPX(54);
      trb = make_trb();
      id0 = ident_frag(0, r, hh); id1 = ident_frag(1, r, hh);
      wtail = hh ? 0.f : 1.f; lomask = hh ? 0u : ~0u;
      PIN(trb); PIN(id0); PIN(id1); PIN(wtail); PIN(lomask);
      if (w >= 2) {
        u32x4 mk;
#pragma unroll
        for (int i = 0; i < 4; ++i) { const int k0 = ((2 * i) & 3) + 8 * (i >> 1) + 4 * hh + 16 * (w & 1); mk[i] = (k0 > r ? 0xffffu : 0u) | (k0 + 1 > r ? 0xffff0000u : 0u); }
        *(LAS u32x4*)(lds + L_MK + (w & 1) * 1024 + lane * 16) = mk;
      }
      if (w == 0) { cnt[0] = 0u; cnt[1] = 0u; cnt[2] = 0u; cnt[3] = 0u; cnt[4] = 0u; cnt[5] = 0u; cnt[6] = 0u; }
      cnt[8 + w] = 0u;
      const int lam = 4 * (w >> 1) + 2 * hh + (w & 1);
      const int pc = ((odd ? 0x01327654 : 0x76543210) >> (4 * lam)) & 7;
      unsigned off = lam == 7 ? OOB : ((h * 28 + 4 * pc - 4 * odd) * 28 + (r < 28 ? r : r - 8)) * 2;
      phoff = off; PIN(phoff);
      __builtin_amdgcn_s_setprio(0);
    }
    tag = (u32x2){0u, 0u}; epoch = 0;
    asm volatile("" : "=v"(ak), "=v"(av), "=v"(rstd), "=v"(mf), "=v"(xs));
#pragma unroll
    for (int i = 0; i < 21; ++i) asm volatile("" : "=v"(xf[i]));
    unsigned sent = SENT;
    auto release = [&](int hop) { if (w == 0) poll(hop, 15); bar(); };
    for (int layer = 0; layer < NL; ++layer) {
      const int type = layer & 1;
      const int pb = 2 + layer * 14;
      if (layer > 0) {
        unsigned base = WS_XO + (2 * layer - 1) * XO_B + TOFF;
        int v16 = lane * 16, vs = v16 + (v16 & 512);
        lptr bk = trb + L_WK;
        PIN(vs);
        release(3 * layer - 1);
        STAMP(pb + 0);
        for (;;) {
          PIN(v16); PIN(vs); PIN(bk); asm volatile("" : "+s"(base));
          u32x4 sp[8];
          auto ld = [&](int s) { xf[4 + s] = __builtin_amdgcn_raw_buffer_load_b128(rWS, v16 + (s & 3) * 1024, base + (s >> 2) * 4096, SC1); SB(); };
          unsigned m = 1;
          ak = Z16; av = Z16;
          float s1 = 0.f, s2 = 0.f, mean;
          unsigned hi;
          u32x4 ck, cv;
          gemm2(ak, av, bk, xf,
                    [&](auto gap) {
#pragma unroll
                      for (int u = 0; u < 8; ++u) { sp[u] = __builtin_amdgcn_raw_buffer_load_b128(rWS, vs + ((u >> 1) & 1) * 2048 + (u & 1) * 512, base + 24576 + (u >> 2) * 4096, SC1); SB(); gap(u); if (u < 4) PACE2(); else PACED(); }
#pragma unroll
                      for (int s = 0; s < LA - 8; ++s) { ld(s); PACED(); }
                    },
                    [&](int i) {
                      if (i + LA - 8 < 24) ld(i + LA - 8);
                      if (i < 4) { PIN(sp[2 * i]); PIN(sp[2 * i + 1]); xf[i] = cat(lo2(sp[2 * i]), lo2(sp[2 * i + 1])); }
                      if (i == 22) { av = mfma(cv, mf, av); SB(); }
                      if (i == 24) { ak = mfma(ck, mf, ak); SB(); }
                    },
                    [&](int i, int hf) {
                      if (i < 4) m = max4(m, sp[2 * i + hf]); else m = umax3(m, xf[i][2 * hf], xf[i][2 * hf + 1]);
                      PIN(m);
                      if (i < 8) { if (hf) { s2 += U2F(sp[i][3]); PIN(s2); } else { s1 += U2F(sp[i][2]); PIN(s1); } }
                      if (hf) return;
                      if (i == 13) asm volatile("ds_write_b32 %0, %1" :: "v"(lds + L_SYNC + 32 + 4 * w), "v"(layer) : "memory");
                      if (i == 9) { auto t = __builtin_amdgcn_permlane32_swap(F2U(s1), F2U(s2), false, false); s1 = U2F(t[0]) + U2F(t[1]); PIN(s1); }
                      if (i == 10) { auto t = __builtin_amdgcn_permlane32_swap(F2U(s1), F2U(s1), false, false); s1 = U2F(t[0]); s2 = U2F(t[1]); PIN(s1); PIN(s2); }
                      if (i == 14) { mean = s1 * (-1.0f / DM); s2 = s2 * (1.0f / DM); PIN(mean); PIN(s2); }
                      if (i == 15) { s2 = fmaxf(fmaf(-mean, mean, s2), 0.f); PIN(s2); }
                      if (i == 16) { rstd = __builtin_amdgcn_rsqf(s2 + 1e-5f); PIN(rstd); }
                      if (i == 17) { hi = pk2(mean, mean); s1 = mean - bfhi(hi); PIN(hi); PIN(s1); }
                      if (i == 18) { const unsigned lo = pk2(s1, 0.f); mf = (u32x4){hi, lo, hi, lo}; PIN(mf); }
                      if (i == 19) { const lptr cs = lds + L_CS + type * 3072 + v16; ck = ldl(cs); cv = ldl(cs + 1024); }
                    }, EARLYAB);
          if (__builtin_expect(!__any(m == sent), 1)) break;
          if (--budget == 0) sent = 0u;
          RET(layer * 3);
          __builtin_amdgcn_s_sleep(BACK);
          asm volatile("" ::: "memory");
        }
      }
      int first = layer; asm volatile("" : "+s"(first));
      if (first == 0) {
        int ln = lane; PIN(ln);
        const int hh = ln >> 5;
        const unsigned ra = (ln & 31) * 128 + ((ln >> 1) & 7) * 16;
        const lptr stg = lds + L_X0 + w * SLAB;
        auto kof = [](int c) { return (c >> 3) * 4096 + (c & 7) * 16; };
        lptr ad[28];
#pragma unroll
        for (int i = 0; i < 28; ++i) {
          const int k0 = kof(chunk_of(ord(i), 0)), k1 = kof(chunk_of(ord(i), 1));
          ad[i] = stg + (ra ^ k0 ^ (~lomask & (k0 ^ k1))); PIN(ad[i]);
        }
        lptr pab = lds + L_X0 + tx * SLAB + (ra ^ kof(7 * pair + 5 * kh + hh)), pc = lds + L_X0 + tx * SLAB + (ra ^ kof(7 * pair + (hh ? 3 : 2 + 2 * kh))); PIN(pab); PIN(pc);
        SB();
        first_slab(xf + 21, 0, 0, trb);
        STAMPX(53);
        bar();
        probe = (unsigned)(__builtin_amdgcn_s_memrealtime() - tstart);
#pragma unroll
        for (int i = 0; i < 28; ++i) xf[i] = ldl(ad[i]);
        u32x4 xa = ldl(pab), xc = ldl(pc);
        arrive(5);
        SB();
        float s2a = 0.f, s2b = 0.f;
#pragma unroll
        for (int i = 0; i < 28; ++i) {
          const u32x4 u = xf[i]; s2a = dot2(u[1], u[1], dot2(u[0], u[0], s2a)); s2b = dot2(u[3], u[3], dot2(u[2], u[2], s2b));
          PIN(s2a); PIN(s2b);
          SB();
        }
        const rsrc_t rPH = __builtin_amdgcn_make_buffer_rsrc((void*)wphi, 0, 16 * 28 * 28 * 2, 0x00020000);
        unsigned e[4];
#pragma unroll
        for (int j = 0; j < 4; ++j) e[j] = __builtin_amdgcn_raw_buffer_load_b16(rPH, phoff + j * 56, 0, 0);
        load_epoch();
        STAMP(1);
        u32x4 cc = ldl(lds + L_CS + ln * 16);
        ak = Z16;
        lptr bk = trb + L_WK; PIN(bk);
        gemm1<5>(ak, bk, xf, [](int) {}, [&](int i) {
          if (i == 4) {
            const unsigned sel = hh && kh ? 0u : ~0u;
            xc[0] = (xc[0] & sel) | (xc[2] & ~sel); xc[1] = (xc[1] & sel) | (xc[3] & ~sel); PIN(xc);
          }
          if (i >= 16 && i < 24) { const unsigned u = i < 20 ? xa[i & 3] : xc[i & 3]; xs[2 * (i - 16)] = bflo(u); xs[2 * (i - 16) + 1] = bfhi(u); PIN(xs); }
        }, NOAB);
        asm volatile("" :: "v"(ak));
        *(LAS u32x2*)(lds + L_WPH + (w >> 1) * 1024 + ln * 16 + (w & 1) * 8) = (u32x2){e[0] | (e[1] << 16), e[2] | (e[3] << 16)};
        make_tag();
        STAMPX(57);
        bar();
        STAMP(2);
        const u32x4 cv = ldl(lds + L_CS + 1024 + ln * 16);
        av = Z16;
        gemm1<5>(av, bk + SLAB, xf, [&](int i) { if (i == 8) { ak = mfma(cc, mf, ak); SB(); } if (i == 10) { av = mfma(cv, mf, av); SB(); } }, [&](int i) {
          if (i == 2) {
            float s1 = U2F(F2U(ak[12]) & ~lomask), s2 = s2a + s2b;
            red2<false>(s1, s2);
            stats(s1, s2);
          }
        }, NOAB);
      }
      asm volatile("" ::: "memory");
      arrive(0);
      STAMPX(pb + 1);
      const unsigned mid = WS_MID + (h * 4 + layer) * MID_B;
      int ln16 = lane * 16; PIN(ln16);
      u32x4 wph[2], kf0, kf1;
      if (type == 0) {
        wph[0] = ldl(lds + L_WPH + ln16); wph[1] = ldl(lds + L_WPH + 1024 + ln16);
        DUMP(0, ak, 1, rstd); DUMP(0, av, 2, rstd);
        kf0 = pack8<0>(ak); kf1 = pack8<1>(ak);
        f32x16 p = mfma(wph[0], kf0, Z16);
        p = mfma(wph[1], kf1, p);
        SB();
#pragma unroll
        for (int i = 0; i < 16; ++i) { float t = av[i] * rstd; PIN(t); av[i] = t; }
        u32x4 vb0 = pack8<0>(av), vb1 = pack8<1>(av);
        vb1[2] |= ~lomask & 0x3f80u;
        PIN(vb0); PIN(vb1);
        SB();
        u32x4 fp[2], fm[2];
        dual_softmax(p, rstd * L2E, wtail, fp, fm);
        fp[1][2] &= lomask; fp[1][3] &= lomask; fm[1][2] &= lomask; fm[1][3] &= lomask;
        PIN(fp[0]); PIN(fp[1]); PIN(fm[0]); PIN(fm[1]);
        gather_all(layer, false);
        const int rr = (ln16 >> 4) & 31, pc = (ln16 >> 9) ^ ((rr >> 1) & 3);
        const lptr d0 = lds + L_SCR + w * 2048 + rr * 64 + pc * 16, d1 = lds + L_SCR + w * 2048 + rr * 64 + (pc ^ 2) * 16;
        *(LAS u32x4*)(d0) = fp[0]; *(LAS u32x4*)(d1) = fp[1];
        *(LAS u32x4*)(d0 + 8192) = fm[0]; *(LAS u32x4*)(d1 + 8192) = fm[1];
        *(LAS u32x4*)(d0 + 16384) = vb0; *(LAS u32x4*)(d1 + 16384) = vb1;
        bar();
      } else {
        av *= rstd;
        DUMP(0, av, 2, 1.f);
        u32x4 vb1 = pack8<1>(av);
        vb1[2] |= ~lomask & 0x3f80u;
        f32x16 vt = mfma(pack8<0>(av), id0, Z16);
        vt = mfma(vb1, id1, vt);
        SB();
#pragma unroll
        for (int i = 0; i < 16; ++i) { float t = ak[i] * rstd; PIN(t); ak[i] = t; }
        DUMP(0, ak, 1, 1.f);
        kf0 = pack8<0>(ak); kf1 = pack8<1>(ak);
        const unsigned g = mid + T * 4096;
        const lptr d = lds + L_SCR + (8 + w) * 4096 + ln16;
        st128<SC1>(kf0, rWS, ln16, g);
        const u32x4 vf0 = pack8<0>(vt), vf1 = pack8<1>(vt);
        st128<SC1>(vf0, rWS, ln16 + 2048, g);
        st128<SC1>(vf1, rWS, ln16 + 3072, g);
        {
          u32x4 kc = kf1;
          kc[3] |= ~lomask & (tag[0] + layer - xhalf_addu(add4(add4(0u, kf0), kf1) + add4(add4(0u, vf0), vf1)));
          st128<SC1>(kc, rWS, ln16 + 1024, g);
          CALS(3 * layer);
        }
        gather_all(layer, false);
        *(LAS u32x4*)(d) = kf0; *(LAS u32x4*)(d + 1024) = kf1; *(LAS u32x4*)(d + 2048) = vf0; *(LAS u32x4*)(d + 3072) = vf1;
      }
      STAMPX(pb + 2);
      u32x4 qf0, qf1;
      {
        f32x16 aq = mfma(ldl(lds + L_CS + type * 3072 + 2048 + ln16), mf, Z16);
        lptr bq = trb + L_WQ; PIN(bq);
        gemm1<5>(aq, bq, xf, [](int) {}, [](int) {}, NOAB);
        aq *= type ? rstd * (0.18898223650461361f * L2E) : 1.0f;
        DUMP(0, aq, 0, type ? 1.f : rstd);
        qf0 = pack8<0>(aq); qf1 = pack8<1>(aq);
        qf1[2] |= ~lomask & 0x3f80u;
      }
      f32x16 o;
      if (type == 0) {
        u32x4 fp[2], fm[2];
        {
          f32x16 p = mfma(wph[0], qf0, Z16);
          p = mfma(wph[1], qf1, p);
          dual_softmax(p, rstd * L2E, wtail, fp, fm);
        }
        PIN(fp[0]); PIN(fp[1]); PIN(fm[0]); PIN(fm[1]);
        STAMPX(pb + 3);
        bar();
        STAMP(pb + 4);
        const lptr kvf = lds + L_KVF + ln16;
        const u32x4 a0 = ldl(kvf), a1 = ldl(kvf + 1024), a2 = ldl(kvf + 2048), a3 = ldl(kvf + 3072);
        SB();
        o = mfma(a0, fp[0], Z16);
        o = mfma(a1, fp[1], o);
        o = mfma(a2, fm[0], o);
        o = mfma(a3, fm[1], o);
        o *= __builtin_amdgcn_rcpf(upper_val(o[12]) + 1e-6f);
      } else {
        lptr kv = lds + L_SCR + w * 4096 + ln16; PIN(kv);
        auto ex4 = [&](f32x16& s, int i0) {
#pragma unroll
          for (int i = i0; i < i0 + 4; ++i) s[i] = __builtin_amdgcn_exp2f(s[i]);
          unsigned a = pk2(s[i0], s[i0 + 1]), c = pk2(s[i0 + 2], s[i0 + 3]); PIN(a); PIN(c);
          SB();
          return (u32x2){a, c};
        };
        auto tile_max = [&](int j, int side, float m) {
          f32x16 t = mfma(ldl(kv + j * 4096 + 1024), qf1, mfma(ldl(kv + j * 4096), qf0, Z16));
          int lim = ((ln16 >> 4) & 31) - ((ln16 >> 7) & 4); PIN(lim);
#pragma unroll
          for (int i = 0; i < 16; ++i) { const bool up = (i & 3) + 8 * (i >> 2) > lim; m = fmaxf(m, (side == 0 || up == (side == 1)) ? t[i] : m); }
          return m;
        };
        float m = -__builtin_inff(), l;
        for (int pass = 0;; ++pass) {
          const u32x4 mk0 = ldl(lds + L_MK + ln16), mk1 = ldl(lds + L_MK + 1024 + ln16);
          f32x16 negm;
          {
            const u32x4 v0 = ldl(kv + 8 * 4096 + 2048), v1 = ldl(kv + 8 * 4096 + 3072);
            f32x16 s = mfma(ldl(kv + 8 * 4096 + 1024), qf1, mfma(ldl(kv + 8 * 4096), qf0, Z16));
#pragma unroll
            for (int i = 0; i < 16; ++i) {
              const unsigned k = (i < 8 ? mk0 : mk1)[(i >> 1) & 3], on = (i & 1) ? (unsigned)((int)k >> 31) : (unsigned)((int)(k << 16) >> 31);
              s[i] = U2F((F2U(s[i]) & ~on) | (on & 0xff800000u));
            }
#pragma unroll
            for (int i = 0; i < 16; i += 2) m = fmax3(m, s[i], s[i + 1]);
            m = xhalf_max(m);
            s -= m;
            negm = splat(-m);
            PIN(negm);
            const u32x2 a = ex4(s, 0), c = ex4(s, 4), e = ex4(s, 8), f = ex4(s, 12);
            o = mfma(v1, cat(e, f), mfma(v0, cat(a, c), Z16));
            PIN(o);
          }
          if (pass == 0) {
            STAMPX(pb + 3);
            bar();
            STAMP(pb + 4);
          }
          u32x4 kk[2][2], vv[2][2], pa, pb2;
          f32x16 st[2];
          kk[0][0] = ldl(kv); kk[0][1] = ldl(kv + 1024); kk[1][0] = ldl(kv + 4096); kk[1][1] = ldl(kv + 4096 + 1024);
          SB();
          st[0] = mfma(kk[0][1], qf1, mfma(kk[0][0], qf0, negm));
          SB();
#pragma unroll
          for (int j = 0; j < 8; ++j) {
            f32x16& s = st[j & 1];
            vv[j & 1][0] = ldl(kv + j * 4096 + 2048); vv[j & 1][1] = ldl(kv + j * 4096 + 3072);
            if (j < 6) { kk[j & 1][0] = ldl(kv + (j + 2) * 4096); kk[j & 1][1] = ldl(kv + (j + 2) * 4096 + 1024); }
            SB();
            if (j == 0) { st[1] = mfma(kk[1][0], qf0, negm); SB(); }
            const u32x2 a = ex4(s, 0);
            if (j == 0) st[1] = mfma(kk[1][1], qf1, st[1]); else o = mfma(vv[(j - 1) & 1][0], pa, o);
            SB();
            const u32x2 c = ex4(s, 4);
            if (j == 7) o = mfma(vv[0][1], pb2, o); else if (j > 0) st[(j + 1) & 1] = mfma(kk[(j + 1) & 1][0], qf0, negm);
            SB();
            const u32x2 e = ex4(s, 8);
            if (j == 7) o = mfma(vv[1][0], cat(a, c), o); else if (j > 0) st[(j + 1) & 1] = mfma(kk[(j + 1) & 1][1], qf1, st[(j + 1) & 1]);
            SB();
            const u32x2 f = ex4(s, 12);
            if (j == 7) o = mfma(vv[1][1], cat(e, f), o); else if (j > 0) o = mfma(vv[(j - 1) & 1][1], pb2, o);
            SB();
            pa = cat(a, c); pb2 = cat(e, f);
            if (j == 0) { pa &= mk0; pb2 &= mk1; PIN(pa); PIN(pb2); }
          }
          l = upper_val(o[12]);
          if (pass > 0 || __all(l < LMAX)) break;
          RET(13);
          m = tile_max(0, 1, m);
#pragma unroll 1
          for (int j = 1; j < 8; ++j) m = tile_max(j, 0, m);
        }
        arrive(4);
        o *= __builtin_amdgcn_rcpf(l);
      }
      DUMP(1, o, 0, 1.f);
      store_o(pack8<0>(o), pack8<1>(o), WS_XO + (2 * layer) * XO_B);
      CALS(3 * layer + 1);
      STAMPX(pb + 6);
      lptr bown, both;
      {
        int ln = lane; PIN(ln);
        const int q = (ln >> 2) & 3, rp = ((ln >> 2) & 4) + (ln & 3), lam = (rp & 4) | ((rp & 1) << 1) | ((rp >> 1) & 1);
        const int pe = lam, po = 6 + ((0x01327654 >> (4 * lam)) & 7);
        const lptr rb = lds + L_WO + kh * SLAB + (8 * (ln >> 5) + q) * 128;
        const lptr ae = rb + ((pe * 8) ^ ((q >> 1) * 64)), ao = rb + ((po * 8) ^ ((q >> 1) * 64));
        bown = kh ? ao : ae; both = kh ? ae : ao;
        PIN(bown); PIN(both);
      }
      unsigned base = WS_XO + (2 * layer) * XO_B + XOFF + kh * 12288;
      int v16 = ln16, vs = (v16 >> 1) + ((v16 >> 1) & 256);
      PIN(vs);
      release(3 * layer + 1);
      STAMP(pb + 7);
      f32x16 xn, xo;
      u32x4 wa[4];
        auto tr = [](lptr p) {
          bf16x4 a0 = __builtin_amdgcn_ds_read_tr16_b64_v4bf16((LAS bf16x4*)p), a1 = __builtin_amdgcn_ds_read_tr16_b64_v4bf16((LAS bf16x4*)(p + 512));
          return __builtin_bit_cast(u32x4, __builtin_shufflevector(a0, a1, 0, 1, 2, 3, 4, 5, 6, 7));
        };
      for (;;) {
        PIN(v16); PIN(vs); asm volatile("" : "+s"(base));
        u32x2 sp[4];
        u32x4 wb[4];
#pragma unroll
        for (int u = 0; u < 4; ++u) { sp[u] = __builtin_amdgcn_raw_buffer_load_b64(rWS, vs + (u >> 1) * 1024 + (u & 1) * 256, base + 24576 - kh * 10240, SC1); SB(); if (u & 1) PACED(); }
#pragma unroll
        for (int s = 0; s < LAO; ++s) { xf[2 + s] = __builtin_amdgcn_raw_buffer_load_b128(rWS, v16 + (s & 3) * 1024, base + (s >> 2) * 4096, SC1); SB(); if (s < 3) wb[s] = tr(both + s * 2048); else if (s < 6) wa[s - 3] = tr(bown + (s - 3) * 2048); SB(); if (s < 6) PACE2(); else PACED(); }
        xf[0] = cat(sp[0], sp[1]); xf[1] = cat(sp[2], sp[3]);
        xn = xs; xo = Z16;
        unsigned m = 1;
#pragma unroll
        for (int i = 0; i < 14; ++i) {
          if (i + LAO < 12) { const int s = i + LAO; xf[2 + s] = __builtin_amdgcn_raw_buffer_load_b128(rWS, v16 + (s & 3) * 1024, base + (s >> 2) * 4096, SC1); SB(); }
          if (i + 3 < 14) wb[(i + 3) & 3] = tr(both + (i + 3) * 2048);
          SB();
          xo = mfma(wb[i & 3], xf[i], xo);
          SB();
          m = max4(m, xf[i]); PIN(m);
          if (i >= LAG) {
            wa[(i - LAG + 3) & 3] = tr(bown + (i - LAG + 3) * 2048);
            SB();
            xn = mfma(wa[(i - LAG) & 3], xf[i - LAG], xn);
          }
          SB();
        }
        if (__builtin_expect(!__any(m == sent), 1)) break;
        if (--budget == 0) sent = 0u;
        RET(layer * 3 + 2);
        __builtin_amdgcn_s_sleep(BACK);
        asm volatile("" ::: "memory");
      }
      {
        const lptr ex = lds + L_WQ + ln16;
        f32x4 t[4];
#pragma unroll
        for (int i = 14 - LAG; i < 14; ++i) {
          if (i + 3 < 14) wa[(i + 3) & 3] = tr(bown + (i + 3) * 2048);
          SB();
          xn = mfma(wa[i & 3], xf[i], xn);
          SB();
          if (i == 15 - LAG) {
#pragma unroll
            for (int g = 0; g < 4; ++g) *(LAS f32x4*)(ex + w * 4096 + g * 1024) = (f32x4){xo[4 * g], xo[4 * g + 1], xo[4 * g + 2], xo[4 * g + 3]};
          }
          if (i == 11) {
            bar();
#pragma unroll
            for (int g = 0; g < 4; ++g) t[g] = *(LAS f32x4*)(ex + (w ^ 1) * 4096 + g * 1024);
          }
          SB();
        }
#pragma unroll
        for (int g = 0; g < 4; ++g) {
#pragma unroll
          for (int j = 0; j < 4; ++j) xs[4 * g + j] = xn[4 * g + j] + t[g][j];
        }
      }
      STAMPX(pb + 8);
      if (layer == NL - 1) {
        const rsrc_t rOUT = __builtin_amdgcn_make_buffer_rsrc((void*)out, 0, SEQ * DM * 2, 0x00020000);
        const u32x4 f0 = pack8<0>(xs), f1 = pack8<1>(xs);
        int ln = lane; PIN(ln);
        const int row = (ln & 31) * 896, up = ln >> 5;
        const int ob = (tb * 4 + tx) * 32 * 896 + 7 * pair * 16;
        st128<0>(f0, rOUT, row + up * 16, ob + 5 * kh * 16);
        st128<0>(f1, rOUT, row + up * OOB, ob + (2 + 2 * kh) * 16);
        __builtin_amdgcn_raw_buffer_store_b64(lo2(f1), rOUT, row + (1 - up) * OOB, ob + 3 * 16 + kh * 8, 0);
      } else {
        const u32x4 f0 = pack8<0>(xs);
        store_x0(f0, WS_XO + (2 * layer + 1) * XO_B);
        u32x4 f1 = pack8<1>(xs);
        f1[2] &= lomask; f1[3] &= lomask;
        float s1 = 0.f, s2 = 0.f, t1 = 0.f, t2 = 0.f;
#pragma unroll
        for (int j = 0; j < 4; ++j) { const unsigned u = f0[j], v = f1[j]; s1 = dot2(u, ONES, s1); s2 = dot2(u, u, s2); t1 = dot2(v, ONES, t1); t2 = dot2(v, v, t2); }
        s1 += t1; s2 += t2;
        red2<false>(s1, s2);
        f1[2] |= ~lomask & F2U(s1); f1[3] |= ~lomask & F2U(s2);
        store_x1(f1, WS_XO + (2 * layer + 1) * XO_B);
        CALS(3 * layer + 2);
        STAMPX(pb + 9);
      }
      asm volatile("" : "=v"(ak), "=v"(av), "=v"(rstd), "=v"(mf), "=v"(phoff));
#pragma unroll
      for (int i = 14; i < 28; ++i) asm volatile("" : "=v"(xf[i]));
    }
    STAMP(61); RSTAMP(63);
    if (b == 77 && w == 0) while (__builtin_amdgcn_s_memrealtime() - tstart < 6000 + probe) __builtin_amdgcn_s_sleep(2);
    if (b == 0 && w == 0) __builtin_amdgcn_raw_buffer_store_b64((u32x2){(unsigned)(epoch + 1), (unsigned)((epoch + 1) >> 32)}, rWS, WS_EPOCH, 0, SC1);
  } else {
    STAMPX(53);
    u32x4 pA[21], pB[21];
    unsigned tch0, tch1;
    asm volatile("s_load_dword %0, %2, 0x0\n\ts_load_dword %1, %3, 0x0" : "=&s"(tch0), "=&s"(tch1)
                 : "s"(w == 0 ? (const void*)ws : w == 1 ? (const void*)wqkvB : w == 2 ? (const void*)woA : (const void*)wphi), "s"(w < 2 ? (const void*)woB : (const void*)out));
    dma_x0(0, 4);
    unsigned warm0;
    {
      u64 pc; asm volatile("s_getpc_b64 %0" : "=s"(pc));
      const rsrc_t rs = __builtin_amdgcn_make_buffer_rsrc(w == 3 ? (void*)(pc & ~127ull) : (void*)wqkvA, 0, w == 3 ? CODE_B : DM * 3 * DM * 2, 0x00020000);
      const int n = w == 3 ? 9 : 13;
      warm0 = __builtin_amdgcn_raw_buffer_load_b32(rs, lane < n ? (lane + (w == 3 ? (b >> 3) : b * 3 + w) * n) * 128 : OOB, 0, 0);
      SB();
    }
#pragma unroll
    for (int i = 0; i < 7; ++i) { mkrow(i); PIN(krow[i]); }
    lptr trb = make_trb(); PIN(trb);
    u32x4 id0 = ident_frag(0, r, hh); PIN(id0);
    SB();
    dma_x0(4, 28);
#pragma unroll
    for (int i = 0; i < NV0; ++i) pA[14 + i] = ldw(rWA, i, 2688, 1792);
    STAMPX(54);
    asm volatile("s_waitcnt vmcnt(%2) lgkmcnt(0)" :: "s"(tch0), "s"(tch1), "n"(NV0) : "memory");
    const u32x4 sv = {SENT, SENT, SENT, SENT};
    auto clean_kv = [&](int layer) { if (layer < NL) st128<SC1>(sv, rWS, lane * 16, WS_MID + (h * 4 + layer) * MID_B + (tb * 4 + w) * 1024); };
    auto clean_o = [&](int layer) { if (layer < NL) store_o(sv, sv, WS_XO + (2 * layer) * XO_B); };
    auto clean_x = [&](int layer) { if (layer < NL - 1) { store_x0(sv, WS_XO + (2 * layer + 1) * XO_B); store_x1(sv, WS_XO + (2 * layer + 1) * XO_B); } };
    unsigned r0 = (unsigned)__builtin_amdgcn_s_memrealtime(), c0 = (unsigned)__builtin_amdgcn_s_memtime(), tann = 0;
    auto mark = [&](int cyc) {
      const unsigned r1 = (unsigned)__builtin_amdgcn_s_memrealtime(), c1 = (unsigned)__builtin_amdgcn_s_memtime();
      tann = r1 + LAND + (unsigned)((float)cyc * (float)(r1 - r0) * __builtin_amdgcn_rcpf((float)(c1 - c0)));
      r0 = r1; c0 = c1;
    };
    auto push = [&](int hop, unsigned voff) {
      CALA(hop);
      __builtin_amdgcn_raw_buffer_store_b64((u32x2){tag[0], (tag[1] & 0xfff00000u) | (tann & 0xfffffu)}, rWS, voff, WS_FLAGS + hop * HOP_B, SC1);
    };
    auto push_grp = [&](int hop) { push(hop, lane < 16 ? (tb * 16 + lane) * 128 + h * 8 : OOB); };
    auto push_mid = [&](int layer) {
      if (layer & 1) push(3 * layer, (lane < 2 && tb + 1 + lane < 16) ? ((tb + 1 + lane) * 16 + h) * 128 + lane * 8 : OOB);
      else push(3 * layer, lane < 16 ? (lane * 16 + h) * 128 + tb * 8 : OOB);
    };
    const rsrc_t rWB = __builtin_amdgcn_make_buffer_rsrc((void*)wqkvB, 0, DM * 3 * DM * 2, 0x00020000);
    const rsrc_t rOA = __builtin_amdgcn_make_buffer_rsrc((void*)woA, 0, DM * DM * 2, 0x00020000);
    const rsrc_t rOB = __builtin_amdgcn_make_buffer_rsrc((void*)woB, 0, DM * DM * 2, 0x00020000);
    auto spin = [&](int k, unsigned n) { while (__builtin_amdgcn_readfirstlane(cnt[k]) < n) __builtin_amdgcn_s_sleep(1); asm volatile("" ::: "memory"); };
    auto wait_h = [&](int hop, int mask) {
      if (w == 0) { if (mask >= 0) poll(hop, mask); cnt[1] = hop + 1; }
      else while (__builtin_amdgcn_readfirstlane(cnt[1]) != hop + 1) { }
      asm volatile("" ::: "memory");
    };
    const unsigned pkb = WS_PK + h * 262144;
    auto ldp = [&](unsigned off) { const u32x4 v = __builtin_amdgcn_raw_buffer_load_b128(rWS, lane * 16, pkb + off + w * 1024, SC1); SB(); return v; };
    STAMPX(55);
    bar();
#pragma unroll
    for (int i = NV0; i < 7; ++i) pA[14 + i] = ldw(rWA, i, 2688, 1792);
    load_epoch();
    u32x4 pw[4];
    {
      const unsigned act = tb < 14 ? 0u : OOB;
#pragma unroll
      for (int j = 0; j < 2; ++j) {
        const int st = 2 * tb + j, p = (st * 43) >> 7, m = st - 3 * p;
        const int c = st < 24 ? 7 * p + ((0x520 >> (4 * m)) & 15) + hh * (1 + (m & 1)) : 7 * (2 * (st - 24) + hh) + 3;
        pw[j] = __builtin_amdgcn_raw_buffer_load_b128(rWB, ((8 * c + ((lane >> 2) & 7)) * 2688 + sub * 16) | (w < 3 ? act : OOB), col + (w == 2 ? 0 : 896 + 896 * w), 0);
        SB();
      }
      {
        const int n = 4 * tb + w, k2 = n >= 28, i = (n - 28 * k2) >> 1, st = i - 2, p = (st * 43) >> 7, m = st - 3 * p;
        const int c = 28 * k2 + (i < 2 ? 14 * i + 3 + 7 * (n & 1) : 7 * p + ((0x520 >> (4 * m)) & 15) + (n & 1) * (1 + (m & 1)));
        const unsigned vo = (c * 7168 + (lane >> 3) * 896 + 112 * pair + (((lane & 7) ^ ((lane >> 2) & 4)) * 16)) | act;
        pw[2] = __builtin_amdgcn_raw_buffer_load_b128(rOA, vo, 0, 0); SB();
        pw[3] = __builtin_amdgcn_raw_buffer_load_b128(rOB, vo, 0, 0); SB();
      }
    }
    __builtin_amdgcn_s_setprio(3);
    STAMPX(57);
    spin(5, 4);
    put_slab(pA + 14, 0, 1);
#pragma unroll
    for (int j = 0; j < NQ1; ++j) pA[j] = ldw(rWA, j, 2688, 0);
    sum_slab(0, 1, trb);
#pragma unroll
    for (int j = NQ1; j < NQ0; ++j) pA[j] = ldw(rWA, j, 2688, 0);
#pragma unroll
    for (int i = 0; i < 7; ++i) pA[7 + i] = ldl(lds + L_WK + i * 4096 + ht * 16);
    STAMPX(58);
    bar();
    if (w == 3) mark(TKA0);
    __builtin_amdgcn_s_setprio(0);
#pragma unroll
    for (int j = NQ0; j < 7; ++j) pA[j] = ldw(rWA, j, 2688, 0);
    {
      const unsigned keep01 = (odd && sub == 0) ? 0u : ~0u, keep23 = (!odd && sub == 3) ? 0u : ~0u, act = (tb < 14 ? 0u : OOB) | (lane * 16);
#pragma unroll
      for (int j = 0; j < 2; ++j) st128<SC1>(pw[j] & (u32x4){keep01, keep01, keep23, keep23}, rWS, w < 3 ? act : OOB, pkb + w * SLAB + (2 * tb + j) * 1024);
      st128<SC1>(pw[2], rWS, act, pkb + 3 * SLAB + (4 * tb + w) * 1024);
      st128<SC1>(pw[3], rWS, act, pkb + 5 * SLAB + (4 * tb + w) * 1024);
    }
    clean_kv(0); clean_o(0);
    make_tag();
    STAMPX(59);
    first_slab(pA, 0, 2, trb);
    drain();
    arrive(3);
    if (w == 3) { spin(3, 4); push_mid(0); }
    asm volatile("" :: "v"(warm0));
    __builtin_amdgcn_s_setprio(3);
    u32x4 po[14];
    auto swap_wo = [&](bool keep) {
      lptr d = lds + L_WO + ht * 16; PIN(d);
#pragma unroll
      for (int i = 0; i < 14; ++i) { u32x4 t; if (keep) t = ldl(d + i * 4096); *(LAS u32x4*)(d + i * 4096) = po[i]; if (keep) po[i] = t; SB(); }
    };
    STAMPX(60);
#pragma unroll
    for (int layer = 0; layer < NL; ++layer) {
      const int type = layer & 1;
      const int pb = 2 + layer * 14;
      const unsigned mid = WS_MID + (h * 4 + layer) * MID_B;
      if (type == 0) {
        lptr ta0, ta1;
        {
          const int rb = (lane >> 4) & 1, q = (lane >> 2) & 3, p = lane & 3;
          const int rp = 4 * rb + p, lam = (rp & 4) | ((rp & 1) << 1) | ((rp >> 1) & 1);
          ta0 = lds + L_SCR + (8 * hh + q) * 64 + (((lam >> 1) ^ (q >> 1)) * 16) + (lam & 1) * 8;
          ta1 = lds + L_SCR + (8 * hh + q + 4) * 64 + (((lam >> 1) ^ (q >> 1) ^ 2) * 16) + (lam & 1) * 8;
          PIN(ta0); PIN(ta1);
        }
        bar();
        STAMPX(pb + 0);
        {
          auto tr2 = [&](unsigned off) {
            bf16x4 a0 = __builtin_amdgcn_ds_read_tr16_b64_v4bf16((LAS bf16x4*)(ta0 + off));
            bf16x4 a1 = __builtin_amdgcn_ds_read_tr16_b64_v4bf16((LAS bf16x4*)(ta1 + off));
            return __builtin_bit_cast(u32x4, __builtin_shufflevector(a0, a1, 0, 1, 2, 3, 4, 5, 6, 7));
          };
          const unsigned so = (w >> 1) * 8192;
          u32x4 fa[4], fb[4];
          f32x16 kv = Z16;
#pragma unroll
          for (int i = 0; i < 3; ++i) { fa[i] = tr2(so + i * 1024); fb[i] = tr2(16384 + i * 1024); }
          SB();
#pragma unroll
          for (int i = 0; i < 8; ++i) {
            if (i + 3 < 8) { fa[(i + 3) & 3] = tr2(so + (i + 3) * 1024); fb[(i + 3) & 3] = tr2(16384 + (i + 3) * 1024); }
            SB();
            kv = mfma(fa[i & 3], fb[i & 3], kv);
            SB();
          }
          st128<SC1>((w & 1) ? pack8<1>(kv) : pack8<0>(kv), rWS, lane * 16, mid + (tb * 4 + w) * 1024);
          CALS(3 * layer);
        }
        STAMPX(pb + 1);
        if (layer == 0) clean_x(0);
        wait_h(3 * layer, 15);
        STAMPX(pb + 2);
        f32x16 sum;
        for (;;) {
          sum = Z16;
          unsigned m = 0;
          u32x4 pr[16];
          const unsigned g = mid + w * 1024;
#pragma unroll
          for (int i = 0; i < 16; ++i) {
            pr[i] = __builtin_amdgcn_raw_buffer_load_b128(rWS, lane * 16, g + i * 4096, SC1); SB();
            if (i < 15) PACED();
          }
          if (w == 3) { mark(layer ? TOA : TOA0); push_grp(3 * layer + 1); SB(); }
#pragma unroll
          for (int i = 0; i < 16; ++i) {
            if (layer == 0 && i < NPO) po[i] = ldp(3 * SLAB + i * 4096);
            sum = mfma(id0, pr[i], sum); m = max4(m, pr[i]); PIN(m); SB();
          }
          if (!__any(m == SENT) || budget == 0) break;
          --budget; RET(layer * 3 + 1);
          asm volatile("" ::: "memory");
        }
        *(LAS u32x4*)(lds + L_KVF + w * 1024 + lane * 16) = pack8<0>(sum);
        STAMPX(pb + 3);
        bar();
        if (layer == 0) {
#pragma unroll
          for (int i = NPO; i < 14; ++i) po[i] = ldp(3 * SLAB + i * 4096);
        } else __builtin_amdgcn_s_sleep(5);
      } else {
        gather_all(layer, true);
        STAMPX(pb + 0);
        u32x4 t[8];
        wait_h(3 * layer, tb >= 2 ? 1 : tb - 1);
        STAMPX(pb + 2);
        if (tb - 2 + (w >> 1) >= 0) {
          const unsigned g = mid + (tb * 4 - 8 + 2 * w) * 4096;
          for (;;) {
            unsigned c[2] = {0u, 0u};
#pragma unroll
            for (int i = 0; i < 8; ++i) { t[i] = __builtin_amdgcn_raw_buffer_load_b128(rWS, lane * 16 + (i & 3) * 1024, g + (i >> 2) * 4096, SC1); SB(); if (i < 7) PACED(); }
#pragma unroll
            for (int i = 0; i < 8; ++i) {
              c[i >> 2] = add4(c[i >> 2], t[i]); PIN(c[i >> 2]);
              if ((i & 3) == 1) t[i][3] &= (unsigned)hh - 1u;
              *(LAS u32x4*)(lds + L_SCR + 2 * w * 4096 + i * 1024 + lane * 16) = t[i];
              SB();
            }
            const auto cs = __builtin_amdgcn_permlane32_swap(c[0], c[1], false, false);
            if (__all(cs[0] + cs[1] == tag[0] + layer) || budget == 0) break;
            --budget; RET(layer * 3 + 1);
            __builtin_amdgcn_s_sleep(8);
            asm volatile("" ::: "memory");
          }
        } else {
#pragma unroll
          for (int i = 0; i < 8; ++i)
            *(LAS u32x4*)(lds + L_SCR + 2 * w * 4096 + i * 1024 + lane * 16) = (u32x4){0u, 0u, ((i & 3) == 1 && hh) ? 0xff80u : 0u, 0u};
        }
        drain();
        STAMPX(pb + 3);
        bar();
        if (w == 3) { mark(layer == 1 ? TOB1 : TOB); push_grp(3 * layer + 1); }
        __builtin_amdgcn_s_setprio(0);
        if (layer == 1) {
          clean_x(1); clean_kv(2);
#pragma unroll
          for (int i = 0; i < 14; ++i) po[i] = ldp(5 * SLAB + i * 4096);
          swap_wo(true);
          drain();
          spin(4, 4);
        }
      }
      __builtin_amdgcn_s_setprio(0);
      if (layer == 0) {
        swap_wo(false);
      } else if (layer < NL - 1) {
        write_slab((type == 0 ? pB : pA) + 7, L_WK);
      }
      STAMPX(pb + 4);
      bar();
      if (w == 3 && layer < NL - 1) { mark(layer == 2 ? TX2 : layer ? TX : TX0); push_grp(3 * layer + 2); }
      STAMPX(pb + 5);
      if (layer > 0 && layer < NL - 1) { __builtin_amdgcn_s_sleep(WSL); write_slab((type == 0 ? pB : pA) + 14, L_WV); }
      if (layer == 0) {
        __builtin_amdgcn_s_sleep(12);
#pragma unroll
        for (int i = 0; i < 21; ++i) pB[(i + 7) % 21] = ldp(i * 4096);
      }
      bar();
      if (layer < NL - 1) {
        __builtin_amdgcn_s_sleep(SWS);
        if (layer == 0) { write_slab(pB + 7, L_WK); write_slab(pB + 14, L_WV); sum_slab(1, 0, trb); sum_slab(1, 1, trb); }
        else swap_wo(layer == 1);
        bar();
        __builtin_amdgcn_s_sleep(WSL);
        write_slab(type == 0 ? pB : pA, L_WQ);
        if (layer == 0) sum_slab(1, 2, trb);
        arrive(0);
        while (!__all(cnt[8 + (lane & 3)] == layer + 1u)) __builtin_amdgcn_s_sleep(1);
        if (w == 3) { mark(layer == 0 ? TKB1 : layer == 1 ? TKA : TKB); push_mid(layer + 1); }
        __builtin_amdgcn_s_sleep(CSL);
        clean_o(layer + 1);
        if (layer == 1) { clean_x(2); drain(); }
        __builtin_amdgcn_s_setprio(3);
      }
    }
  }
}
}

extern "C" void kernel_launch(void* const* d_in, const int* in_sizes, int n_in, void* d_out, int out_size, void* d_ws, size_t ws_size, hipStream_t stream) {
  (void)hipFuncSetAttribute((const void*)fused_kernel, hipFuncAttributeMaxDynamicSharedMemorySize, L_END);
  fused_kernel<<<256, 512, L_END, stream>>>((const u16*)d_in[0], (const u16*)d_in[1], (const u16*)d_in[2], (const u16*)d_in[3],
                                            (const u16*)d_in[5], (const u16*)d_in[6], (u16*)d_out, (unsigned char*)d_ws);
}
```

```cpp
#define NL 4
#include <hip/hip_runtime.h>
#include <hip/hip_bf16.h>

typedef unsigned short u16;
typedef unsigned long long u64;
typedef __bf16 bf16x8 __attribute__((ext_vector_type(8)));
typedef __bf16 bf16x4 __attribute__((ext_vector_type(4)));
typedef __bf16 bf16x2 __attribute__((ext_vector_type(2)));
typedef float f32x16 __attribute__((ext_vector_type(16)));
typedef float f32x4 __attribute__((ext_vector_type(4)));
typedef float f32x2 __attribute__((ext_vector_type(2)));
typedef unsigned u32x4 __attribute__((ext_vector_type(4)));
typedef unsigned u32x2 __attribute__((ext_vector_type(2)));
#define LAS __attribute__((address_space(3)))
#define GAS __attribute__((address_space(1)))
#define DI __device__ __forceinline__
#define SB() __builtin_amdgcn_sched_barrier(0)
#define PIN(x) asm volatile("" : "+v"(x))
typedef __amdgpu_buffer_rsrc_t rsrc_t;
typedef LAS unsigned char* lptr;

namespace {
constexpr int SEQ = 2048, DM = 448;
constexpr int SC1 = 16;
constexpr unsigned WS_EPOCH = 0;
constexpr unsigned WS_FLAGS = 4096;
constexpr unsigned HOP_B = 256 * 128;
constexpr unsigned TILE_B = 32768;
constexpr unsigned XO_B = 4 * TILE_B;
constexpr unsigned WS_XO = 1u << 20;
constexpr unsigned MID_B = 64 * 4096;
constexpr unsigned WS_MID = WS_XO + 16 * 8 * XO_B;
constexpr unsigned WS_PK = WS_MID + 64 * MID_B;
constexpr unsigned WS_PROF = WS_PK + 16 * 262144;
constexpr unsigned WS_END = WS_PROF + 256 * 2048;
constexpr unsigned SLAB = 448 * 64;
constexpr unsigned L_WK = 0, L_WV = SLAB, L_WQ = 2 * SLAB, L_WO = 3 * SLAB;
constexpr unsigned L_SCR = 0;
constexpr unsigned L_X0 = SLAB;
constexpr unsigned L_KVF = 5 * SLAB;
constexpr unsigned L_MK = L_KVF + 4096;
constexpr unsigned L_WPH = L_MK + 2048;
constexpr unsigned L_CS = L_WPH + 2048;
constexpr unsigned L_SYNC = L_CS + 6144;
constexpr unsigned L_PROF = L_SYNC + 64;
constexpr unsigned L_END = L_PROF + 2048;
static_assert(L_END <= 163840 && L_X0 + 4 * SLAB <= L_KVF, "lds");
constexpr unsigned SPIN_CAP = 1u << 15;
constexpr unsigned SENT = 0xffffffffu;
constexpr unsigned OOB = 0x7ff00000u;
constexpr unsigned ONES = 0x3f803f80u;
constexpr unsigned CODE_B = 36 * 1024;
constexpr float L2E = 1.4426950408889634f;

DI f32x16 mfma(u32x4 a, u32x4 b, f32x16 c) {
  return __builtin_amdgcn_mfma_f32_32x32x16_bf16(__builtin_bit_cast(bf16x8, a), __builtin_bit_cast(bf16x8, b), c, 0, 0, 0);
}
DI unsigned pk2(float a, float b) { f32x2 t = {a, b}; return __builtin_bit_cast(unsigned, __builtin_convertvector(t, bf16x2)); }
template <int S_> DI u32x4 pack8(const f32x16& c) {
  return (u32x4){pk2(c[8 * S_], c[8 * S_ + 1]), pk2(c[8 * S_ + 2], c[8 * S_ + 3]), pk2(c[8 * S_ + 4], c[8 * S_ + 5]), pk2(c[8 * S_ + 6], c[8 * S_ + 7])};
}
DI u32x4 cat(u32x2 a, u32x2 b) { return (u32x4){a[0], a[1], b[0], b[1]}; }
DI u32x2 lo2(u32x4 v) { return (u32x2){v[0], v[1]}; }
DI unsigned umax3(unsigned a, unsigned b, unsigned c) { return max(max(a, b), c); }
DI unsigned max4(unsigned m, u32x4 v) { return umax3(umax3(m, v[0], v[1]), v[2], v[3]); }
DI unsigned add4(unsigned m, u32x4 v) { return (m + v[0] + v[1]) + v[2] + v[3]; }
DI unsigned xhalf_addu(unsigned v) { auto r = __builtin_amdgcn_permlane32_swap(v, v, false, false); return r[0] + r[1]; }
DI float fmax3(float a, float b, float c) { return fmaxf(fmaxf(a, b), c); }
template <int CTRL> DI float dppadd(float v) { return v + __builtin_bit_cast(float, __builtin_amdgcn_update_dpp(0, __builtin_bit_cast(int, v), CTRL, 0xf, 0xf, true)); }
DI float fmin3(float a, float b, float c) { return fminf(fminf(a, b), c); }
DI f32x16 splat(float v) { f32x16 z; _Pragma("unroll") for (int i = 0; i < 16; ++i) z[i] = v; return z; }
#define Z16 splat(0.f)
DI u32x4 trfrag(lptr p) {
  bf16x4 a0 = __builtin_amdgcn_ds_read_tr16_b64_v4bf16((LAS bf16x4*)p);
  bf16x4 a1 = __builtin_amdgcn_ds_read_tr16_b64_v4bf16((LAS bf16x4*)(p + 256));
  return __builtin_bit_cast(u32x4, __builtin_shufflevector(a0, a1, 0, 1, 2, 3, 4, 5, 6, 7));
}
DI u32x4 ldl(lptr p) { return *(LAS u32x4*)p; }
DI float U2F(unsigned u) { return __uint_as_float(u); }
DI unsigned F2U(float f) { return __float_as_uint(f); }
template <bool MX> DI void red2(float& a, float& b) {
  auto r = __builtin_amdgcn_permlane32_swap(F2U(a), F2U(b), false, false);
  const float t = MX ? fmaxf(U2F(r[0]), U2F(r[1])) : U2F(r[0]) + U2F(r[1]);
  r = __builtin_amdgcn_permlane32_swap(F2U(t), F2U(t), false, false);
  a = U2F(r[0]); b = U2F(r[1]);
}
DI float xhalf_max(float v) { auto r = __builtin_amdgcn_permlane32_swap(F2U(v), F2U(v), false, false); return fmaxf(U2F(r[0]), U2F(r[1])); }
DI float upper_val(float v) { auto r = __builtin_amdgcn_permlane32_swap(F2U(v), F2U(v), false, false); return U2F(r[1]); }
DI float dot2(unsigned u, unsigned v, float c) { return __builtin_amdgcn_fdot2_f32_bf16(__builtin_bit_cast(bf16x2, u), __builtin_bit_cast(bf16x2, v), c, false); }
DI float bflo(unsigned u) { return U2F(u << 16); }
DI float bfhi(unsigned u) { return U2F(u & 0xffff0000u); }
DI void split2(float v, unsigned& hi, unsigned& lo) { hi = pk2(v, v) & 0xffffu; lo = pk2(v - bflo(hi), 0.f) & 0xffffu; }
template <int AUX> DI void st128(u32x4 v, rsrc_t rs, unsigned voff, unsigned soff) {
  SB();
  __builtin_amdgcn_raw_buffer_store_b128(v, rs, voff, soff, AUX);
  SB();
  asm volatile("s_nop 1" :: "v"(v));
  SB();
}
DI void drain() { asm volatile("s_waitcnt vmcnt(0)" ::: "memory"); }
DI void bar() { SB(); asm volatile("s_waitcnt lgkmcnt(0)\n\ts_barrier" ::: "memory"); SB(); }
constexpr int chunk_of(int s, int hh) { return s < 24 ? 7 * (s / 3) + (s % 3 == 0 ? hh : (s % 3 == 1 ? 2 + 2 * hh : 5 + hh)) : 7 * (2 * (s - 24) + hh) + 3; }
constexpr int ord(int i) { return i < 4 ? 24 + i : i - 4; }

#define STAMP(i) do { } while (0)
#define RET(i) do { } while (0)
#define RSTAMP(i) do { } while (0)
#define STAMPX(i) STAMP(i)
#define CALS(hop) do { } while (0)
#define CALA(hop) do { } while (0)
#define TOT0
#define TOT1
#define DUMP(buf, acc, part, sc) do { } while (0)

DI void dual_softmax(const f32x16& p, float c, float wtail, u32x4 (&fp)[2], u32x4 (&fm)[2]) {
  float mx = fmax3(p[0], p[1], p[2]), mn = fmax3(-p[0], -p[1], -p[2]);
#pragma unroll
  for (int i = 3; i < 15; i += 2) { mx = fmax3(mx, p[i], p[i + 1]); mn = fmax3(mn, -p[i], -p[i + 1]); }
  mx = fmax3(mx, p[15], p[15]); mn = fmax3(mn, -p[15], -p[15]);
  red2<true>(mx, mn);
  const float a = -c * mx, b = -c * mn;
  float nc = -c; PIN(nc);
  f32x16 ep = p * c + a, em = p * nc + b;
  SB();
#pragma unroll
  for (int i = 0; i < 16; ++i) ep[i] = __builtin_amdgcn_exp2f(ep[i]);
#pragma unroll
  for (int i = 0; i < 16; ++i) em[i] = __builtin_amdgcn_exp2f(em[i]);
  f32x2 sp = {ep[0], ep[1]}, sm = {em[0], em[1]};
#pragma unroll
  for (int i = 2; i < 12; i += 2) { sp += (f32x2){ep[i], ep[i + 1]}; sm += (f32x2){em[i], em[i + 1]}; }
  sp += wtail * ((f32x2){ep[12], ep[13]} + (f32x2){ep[14], ep[15]});
  sm += wtail * ((f32x2){em[12], em[13]} + (f32x2){em[14], em[15]});
  float u = sp[0] + sp[1], v = sm[0] + sm[1];
  red2<false>(u, v);
  ep *= __builtin_amdgcn_rcpf(u); em *= __builtin_amdgcn_rcpf(v);
  fp[0] = pack8<0>(ep); fp[1] = pack8<1>(ep); fm[0] = pack8<0>(em); fm[1] = pack8<1>(em);
}
DI u32x4 ident_frag(int s, int r, int hh) {
  const unsigned val = 0x3f80u << (16 * (r & 1));
  const int idx = (((r >> 2) ^ hh) & 1) * 8 + (r >> 4) * 4 + ((r >> 2) & 2) + ((r >> 1) & 1) - 4 * s;
  return (u32x4){idx == 0 ? val : 0u, idx == 1 ? val : 0u, idx == 2 ? val : 0u, idx == 3 ? val : 0u};
}
#define CHK1 3
#define CHK2 12
template <typename S, typename A> DI void steps28(S step, A abort) {
#pragma unroll
  for (int i = 0; i <= CHK1; ++i) step(i);
  if (abort()) return;
#pragma unroll
  for (int i = CHK1 + 1; i < 28; ++i) step(i);
}
template <int PD, typename F, typename G, typename A> DI void gemm1(f32x16& acc, lptr wb, const u32x4 (&xf)[28], F pre, G post, A abort) {
  u32x4 wr[PD];
#pragma unroll
  for (int i = 0; i < PD - 1; ++i) wr[i] = trfrag(wb + ord(i) * 1024);
  SB();
  steps28([&](int i) __attribute__((always_inline)) {
    pre(i);
    if (i + PD - 1 < 28) wr[(i + PD - 1) % PD] = trfrag(wb + ord(i + PD - 1) * 1024);
    SB();
    acc = mfma(wr[i % PD], xf[i], acc);
    SB();
    post(i);
    SB();
  }, abort);
}
template <typename H, typename F, typename G, typename A>
DI void gemm2(f32x16& ak, f32x16& av, lptr bk, const u32x4 (&xf)[28], H head, F pre, G post, A abort) {
  u32x4 wk[3], wv[3];
  head([&](int u) {
    if (u == 0) wv[0] = trfrag(bk + SLAB + ord(0) * 1024);
    if (u == 1) wk[0] = trfrag(bk + ord(0) * 1024);
    if (u == 2) wv[1] = trfrag(bk + SLAB + ord(1) * 1024);
    if (u == 3) wk[1] = trfrag(bk + ord(1) * 1024);
    SB();
  });
  steps28([&](int i) __attribute__((always_inline)) {
    pre(i);
    if (i + 2 < 28) wv[(i + 2) % 3] = trfrag(bk + SLAB + ord(i + 2) * 1024);
    SB();
    av = mfma(wv[i % 3], xf[i], av);
    SB();
    if (i + 2 < 28) wk[(i + 2) % 3] = trfrag(bk + ord(i + 2) * 1024);
    post(i, 0);
    SB();
    ak = mfma(wk[i % 3], xf[i], ak);
    SB();
    post(i, 1);
    SB();
  }, abort);
}
#define NOAB [] { return false; }
#define EARLYAB [&] { return __builtin_expect(__any(m == sent), 0); }
#define LA 12
#define XH 27
#define VSLEEP 8
#define QSLEEP 28
#define LAO 8
#define XBAR 26
#define DOA 3
#define DOB 6
#define DX 3
#define KVI 6
#define KVB 3
#define NKB 5
#define XM 4
#ifndef NV0
#define NV0 7
#endif
#define NK2 8
#define NK3 9
#define PACE 8
#define LAG 6
#define PACED() do { if (PACE) asm volatile("s_nop %0" :: "n"(PACE)); SB(); } while (0)
#define PSLP 4
#define RDLY 0
#define KDLY 0
#define LAND 10
#define TKA0 4100
#define TKA 3600
#define TKB1 1950
#define TKB 2050
#define TOA0 2050
#define TOA 1850
#define TOB1 2750
#define TOB 2520
#define TX0 3800
#define TX 2850
#define BACK 8
#define NQ0 7
#define NQB 0
#define TX2 3200
#define ND0 10
#define NPO 14
#define SWS 6
#define LMAX 1.8446744e19f
#define CSL 2
#define WSL 4
#define PACE2() do { asm volatile("s_nop %0" :: "n"(PACE - 2)); SB(); } while (0)
#define NQ1 4
#define PDELAY 14

__global__ void __launch_bounds__(512, 2) fused_kernel(const u16* __restrict__ x0, const u16* __restrict__ wqkvA, const u16* __restrict__ wphi,
                                                       const u16* __restrict__ woA, const u16* __restrict__ wqkvB, const u16* __restrict__ woB,
                                                       u16* __restrict__ out, unsigned char* ws) {
  extern __shared__ __attribute__((aligned(16))) unsigned char smem[];
  const lptr lds = (lptr)smem;
  const int tid = threadIdx.x, lane = tid & 63, wave = __builtin_amdgcn_readfirstlane(tid >> 6);
  TOT0
  const int w = wave & 3;
  const int r = lane & 31, hh = lane >> 5;
  const int b = blockIdx.x;
  const int tb = ((b >> 1) & 3) * 4 + (b >> 6), h = (b & 1) * 8 + ((b >> 3) & 7);
  const int me = tb * 16 + h;
  const int odd = h & 1, pair = h >> 1;
  const int T = tb * 4 + w;
  const unsigned TOFF = tb * 8 * XO_B + w * TILE_B;
  const int kh = w & 1, tx = 2 * odd + (w >> 1);
  const unsigned XOFF = tb * 8 * XO_B + tx * TILE_B;
  const rsrc_t rWS = __builtin_amdgcn_make_buffer_rsrc((void*)ws, 0, WS_END, 0x00020000);
  unsigned budget = SPIN_CAP;
  u64 epoch;
  u32x2 tag;
  u32x2 ev;
  auto load_epoch = [&]() { ev = __builtin_amdgcn_raw_buffer_load_b64(rWS, 0, WS_EPOCH, SC1); SB(); };
  auto make_tag = [&]() {
    epoch = (u64)(unsigned)__builtin_amdgcn_readfirstlane(ev[0]) | ((u64)(unsigned)__builtin_amdgcn_readfirstlane(ev[1]) << 32);
    u64 z = epoch + 0x9E3779B97F4A7C15ull;
    z = (z ^ (z >> 30)) * 0xBF58476D1CE4E5B9ull; z = (z ^ (z >> 27)) * 0x94D049BB133111EBull; z ^= z >> 31;
    unsigned t0 = (unsigned)z, t1 = (unsigned)(z >> 32);
    asm volatile("" : "+s"(t0), "+s"(t1));
    tag = (u32x2){t0, t1};
  };
  volatile LAS unsigned* const cnt = (volatile LAS unsigned*)(lds + L_SYNC);
  auto arrive = [&](int k) { if (lane == 0) asm volatile("ds_add_u32 %0, %1" :: "v"(lds + L_SYNC + 4 * k), "v"(1u) : "memory"); };
  auto gather_all = [&](int layer, bool nap) {
    while (__builtin_amdgcn_readfirstlane(*cnt) < 8u * layer + 4u) { if (nap) __builtin_amdgcn_s_sleep(2); }
    asm volatile("" ::: "memory");
  };
  auto poll = [&](int hop, int mask) {
    int ln = lane & mask; PIN(ln);
    u32x2 v;
    for (;;) {
      v = __builtin_amdgcn_raw_buffer_load_b64(rWS, ln * 8, WS_FLAGS + hop * HOP_B + me * 128, SC1);
      asm volatile("" ::: "memory");
      if (__all(v[0] == tag[0] && ((v[1] ^ tag[1]) >> 20) == 0)) break;
      if (budget <= 1) break;
      --budget;
      if (PSLP) __builtin_amdgcn_s_sleep(PSLP);
    }
    const unsigned t = v[1] << 12;
    for (int n = 0; n < 64; ++n) {
      const unsigned now = (unsigned)__builtin_amdgcn_s_memrealtime() << 12;
      if (!__any((int)(t - now) > 0)) break;
    }
    asm volatile("" ::: "memory");
  };
  auto make_trb = [&]() {
    const int rb = (lane >> 4) & 1, q = (lane >> 2) & 3, p = lane & 3;
    const int rp = 4 * rb + p, lam = (rp & 4) | ((rp & 1) << 1) | ((rp >> 1) & 1);
    const int pc = ((odd ? 0x01327654 : 0x76543210) >> (4 * lam)) & 7;
    return lds + (8 * hh + q) * 64 + pc * 8;
  };
  auto store_x0 = [&](u32x4 f0, unsigned base) { int ln = lane; PIN(ln); st128<SC1>(f0, rWS, ln * 16, base + XOFF + (3 * pair + 2 * kh) * 1024); };
  auto store_x1 = [&](u32x4 f1, unsigned base) {
    int ln = lane; PIN(ln);
    st128<SC1>(f1, rWS, ln * 16 + __umul24(ln >> 5, 24576 - 512 - 1024 - pair * 2048), base + XOFF + (3 * pair + 1) * 1024 + kh * 512);
  };
  auto store_o = [&](u32x4 f0, u32x4 f1, unsigned base) {
    int ln = lane; PIN(ln);
    st128<SC1>(f0, rWS, ln * 16, base + TOFF + (3 * pair + 2 * odd) * 1024);
    st128<SC1>(f1, rWS, ln * 16 + (ln >> 5) * OOB, base + TOFF + (3 * pair + 1) * 1024 + odd * 512);
    __builtin_amdgcn_raw_buffer_store_b64(lo2(f1), rWS, ln * 8 + (1 - (ln >> 5)) * OOB, base + TOFF + 24576 - 256 + h * 256, SC1);
  };
  const int ht = tid & 255, sub = lane & 3;
  const int col = h * 56 - odd * 8;
  int krow[7];
  auto mkrow = [&](int i) {
    const int s = 4 * i + w;
    const int p = (s * 43) >> 7, m = s - 3 * p;
    const int c = i < 6 ? 7 * p + ((0x520 >> (4 * m)) & 15) + hh * (1 + (m & 1)) : 7 * (2 * w + hh) + 3;
    krow[i] = 8 * c + ((lane >> 2) & 7);
  };
  auto ldw = [&](rsrc_t rs, int i, int stride, int off) { int k = krow[i]; PIN(k); const u32x4 v = __builtin_amdgcn_raw_buffer_load_b128(rs, k * stride + sub * 16, col + off, 0); SB(); return v; };
  auto write_slab = [&](const u32x4* p, unsigned off) {
    lptr d = lds + off + ht * 16; PIN(d);
#pragma unroll
    for (int i = 0; i < 7; ++i) *(LAS u32x4*)(d + i * 4096) = p[i];
  };
  auto put_slab = [&](u32x4* p, int type, int part) {
    const unsigned keep01 = (odd && sub == 0) ? 0u : ~0u, keep23 = (!odd && sub == 3) ? 0u : ~0u;
#pragma unroll
    for (int i = 0; i < 7; ++i) {
      p[i][0] &= keep01; p[i][1] &= keep01; p[i][2] &= keep23; p[i][3] &= keep23;
      if (type == 0 && part == 0) { p[i][0] |= ~keep01 & 0x3f80u; p[i][2] |= ~keep23 & 0x3f80u; }
    }
    write_slab(p, part * SLAB);
  };
  auto sum_slab = [&](int type, int part, lptr trb) {
    lptr wb = trb + part * SLAB + w * 1024; PIN(wb);
    u32x4 wr[7];
#pragma unroll
    for (int i = 0; i < 7; ++i) wr[i] = trfrag(wb + i * 4096);
    f32x16 a = Z16;
#pragma unroll
    for (int i = 0; i < 7; ++i) a = mfma((u32x4){ONES, ONES, ONES, ONES}, wr[i], a);
    unsigned hi, lo; split2(a[0], hi, lo);
    if (hh == (w >> 1)) *(LAS u32x2*)(lds + L_CS + type * 3072 + part * 1024 + lane * 16 + (w & 1) * 8) = (u32x2){hi | (lo << 16), hi};
  };
  auto first_slab = [&](u32x4* p, int type, int part, lptr trb) { put_slab(p, type, part); sum_slab(type, part, trb); };
  const rsrc_t rWA = __builtin_amdgcn_make_buffer_rsrc((void*)wqkvA, 0, DM * 3 * DM * 2, 0x00020000);
  auto dma_x0 = [&](int i0, int i1) __attribute__((always_inline)) {
    const rsrc_t rX0 = __builtin_amdgcn_make_buffer_rsrc((void*)x0, 0, SEQ * DM * 2, 0x00020000);
    const int o0 = (lane >> 3) * 896 + (((lane & 7) ^ (lane >> 4)) * 16), o1 = o0 ^ 64;
#pragma unroll
    for (int i = i0; i < i1; ++i) {
      __builtin_amdgcn_raw_ptr_buffer_load_lds(rX0, (LAS void*)(lds + L_X0 + w * SLAB + i * 1024), 16, (i & 1) ? o1 : o0, (T * 32 + (i & 3) * 8) * 896 + (i >> 2) * 128, 0, 0);
      SB();
    }
  };

  if (wave < 4) {
    RSTAMP(62); STAMP(0);
    u32x4 xf[28];
    f32x16 xs, ak, av;
    float rstd;
    u32x4 mf;
    auto stats = [&](float s1, float s2) {
      const float mean = s1 * (1.0f / DM);
      rstd = __builtin_amdgcn_rsqf(fmaxf(s2 * (1.0f / DM) - mean * mean, 0.f) + 1e-5f);
      unsigned hi, lo; split2(-mean, hi, lo);
      mf = (u32x4){hi | (hi << 16), lo, hi | (hi << 16), lo};
      PIN(rstd); PIN(mf);
    };
    lptr trb;
    u32x4 id0, id1;
    float wtail;
    unsigned lomask, phoff;
    {
      __builtin_amdgcn_s_setprio(3);
#pragma unroll
      for (int i = 0; i < 7; ++i) { mkrow(i); xf[21 + i] = ldw(rWA, i, 2688, 896); }
      STAMPX(54);
      trb = make_trb();
      id0 = ident_frag(0, r, hh); id1 = ident_frag(1, r, hh);
      wtail = hh ? 0.f : 1.f; lomask = hh ? 0u : ~0u;
      PIN(trb); PIN(id0); PIN(id1); PIN(wtail); PIN(lomask);
      if (w >= 2) {
        u32x4 mk;
#pragma unroll
        for (int i = 0; i < 4; ++i) { const int k0 = ((2 * i) & 3) + 8 * (i >> 1) + 4 * hh + 16 * (w & 1); mk[i] = (k0 > r ? 0xffffu : 0u) | (k0 + 1 > r ? 0xffff0000u : 0u); }
        *(LAS u32x4*)(lds + L_MK + (w & 1) * 1024 + lane * 16) = mk;
      }
      if (w == 0) { cnt[0] = 0u; cnt[1] = 0u; cnt[2] = 0u; cnt[3] = 0u; cnt[4] = 0u; cnt[5] = 0u; cnt[6] = 0u; }
      cnt[8 + w] = 0u;
      const int lam = 4 * (w >> 1) + 2 * hh + (w & 1);
      const int pc = ((odd ? 0x01327654 : 0x76543210) >> (4 * lam)) & 7;
      unsigned off = lam == 7 ? OOB : ((h * 28 + 4 * pc - 4 * odd) * 28 + (r < 28 ? r : r - 8)) * 2;
      phoff = off; PIN(phoff);
      __builtin_amdgcn_s_setprio(0);
    }
    tag = (u32x2){0u, 0u}; epoch = 0;
    asm volatile("" : "=v"(ak), "=v"(av), "=v"(rstd), "=v"(mf), "=v"(xs));
#pragma unroll
    for (int i = 0; i < 21; ++i) asm volatile("" : "=v"(xf[i]));
    unsigned sent = SENT;
    auto release = [&](int hop) { if (w == 0) poll(hop, 15); bar(); };
    for (int layer = 0; layer < NL; ++layer) {
      const int type = layer & 1;
      const int pb = 2 + layer * 14;
      if (layer > 0) {
        unsigned base = WS_XO + (2 * layer - 1) * XO_B + TOFF;
        int v16 = lane * 16, vs = v16 + (v16 & 512);
        lptr bk = trb + L_WK;
        PIN(vs);
        release(3 * layer - 1);
        STAMP(pb + 0);
        for (;;) {
          PIN(v16); PIN(vs); PIN(bk); asm volatile("" : "+s"(base));
          u32x4 sp[8];
          auto ld = [&](int s) { xf[4 + s] = __builtin_amdgcn_raw_buffer_load_b128(rWS, v16 + (s & 3) * 1024, base + (s >> 2) * 4096, SC1); SB(); };
          unsigned m = 1;
          ak = Z16; av = Z16;
          float s1 = 0.f, s2 = 0.f, mean;
          unsigned hi;
          u32x4 ck, cv;
          gemm2(ak, av, bk, xf,
                    [&](auto gap) {
#pragma unroll
                      for (int u = 0; u < 8; ++u) { sp[u] = __builtin_amdgcn_raw_buffer_load_b128(rWS, vs + ((u >> 1) & 1) * 2048 + (u & 1) * 512, base + 24576 + (u >> 2) * 4096, SC1); SB(); gap(u); if (u < 4) PACE2(); else PACED(); }
#pragma unroll
                      for (int s = 0; s < LA - 8; ++s) { ld(s); PACED(); }
                    },
                    [&](int i) {
                      if (i + LA - 8 < 24) ld(i + LA - 8);
                      if (i < 4) { PIN(sp[2 * i]); PIN(sp[2 * i + 1]); xf[i] = cat(lo2(sp[2 * i]), lo2(sp[2 * i + 1])); }
                      if (i == 22) { av = mfma(cv, mf, av); SB(); }
                      if (i == 24) { ak = mfma(ck, mf, ak); SB(); }
                    },
                    [&](int i, int hf) {
                      if (i < 4) m = max4(m, sp[2 * i + hf]); else m = umax3(m, xf[i][2 * hf], xf[i][2 * hf + 1]);
                      PIN(m);
                      if (i < 8) { if (hf) { s2 += U2F(sp[i][3]); PIN(s2); } else { s1 += U2F(sp[i][2]); PIN(s1); } }
                      if (hf) return;
                      if (i == 13) asm volatile("ds_write_b32 %0, %1" :: "v"(lds + L_SYNC + 32 + 4 * w), "v"(layer) : "memory");
                      if (i == 9) { auto t = __builtin_amdgcn_permlane32_swap(F2U(s1), F2U(s2), false, false); s1 = U2F(t[0]) + U2F(t[1]); PIN(s1); }
                      if (i == 10) { auto t = __builtin_amdgcn_permlane32_swap(F2U(s1), F2U(s1), false, false); s1 = U2F(t[0]); s2 = U2F(t[1]); PIN(s1); PIN(s2); }
                      if (i == 14) { mean = s1 * (-1.0f / DM); s2 = s2 * (1.0f / DM); PIN(mean); PIN(s2); }
                      if (i == 15) { s2 = fmaxf(fmaf(-mean, mean, s2), 0.f); PIN(s2); }
                      if (i == 16) { rstd = __builtin_amdgcn_rsqf(s2 + 1e-5f); PIN(rstd); }
                      if (i == 17) { hi = pk2(mean, mean); s1 = mean - bfhi(hi); PIN(hi); PIN(s1); }
                      if (i == 18) { const unsigned lo = pk2(s1, 0.f); mf = (u32x4){hi, lo, hi, lo}; PIN(mf); }
                      if (i == 19) { const lptr cs = lds + L_CS + type * 3072 + v16; ck = ldl(cs); cv = ldl(cs + 1024); }
                    }, EARLYAB);
          if (__builtin_expect(!__any(m == sent), 1)) break;
          if (--budget == 0) sent = 0u;
          RET(layer * 3);
          __builtin_amdgcn_s_sleep(BACK);
          asm volatile("" ::: "memory");
        }
      }
      int first = layer; asm volatile("" : "+s"(first));
      if (first == 0) {
        int ln = lane; PIN(ln);
        const int hh = ln >> 5;
        const unsigned ra = (ln & 31) * 128 + ((ln >> 1) & 7) * 16;
        const lptr stg = lds + L_X0 + w * SLAB;
        auto kof = [](int c) { return (c >> 3) * 4096 + (c & 7) * 16; };
        lptr ad[28];
#pragma unroll
        for (int i = 0; i < 28; ++i) {
          const int k0 = kof(chunk_of(ord(i), 0)), k1 = kof(chunk_of(ord(i), 1));
          ad[i] = stg + (ra ^ k0 ^ (~lomask & (k0 ^ k1))); PIN(ad[i]);
        }
        lptr pab = lds + L_X0 + tx * SLAB + (ra ^ kof(7 * pair + 5 * kh + hh)), pc = lds + L_X0 + tx * SLAB + (ra ^ kof(7 * pair + (hh ? 3 : 2 + 2 * kh))); PIN(pab); PIN(pc);
        SB();
        first_slab(xf + 21, 0, 0, trb);
        STAMPX(53);
        bar();
        STAMPX(55);
#pragma unroll
        for (int i = 0; i < 28; ++i) xf[i] = ldl(ad[i]);
        u32x4 xa = ldl(pab), xc = ldl(pc);
        arrive(5);
        SB();
        float s2a = 0.f, s2b = 0.f;
#pragma unroll
        for (int i = 0; i < 28; ++i) {
          const u32x4 u = xf[i]; s2a = dot2(u[1], u[1], dot2(u[0], u[0], s2a)); s2b = dot2(u[3], u[3], dot2(u[2], u[2], s2b));
          PIN(s2a); PIN(s2b);
          SB();
        }
        const rsrc_t rPH = __builtin_amdgcn_make_buffer_rsrc((void*)wphi, 0, 16 * 28 * 28 * 2, 0x00020000);
        unsigned e[4];
#pragma unroll
        for (int j = 0; j < 4; ++j) e[j] = __builtin_amdgcn_raw_buffer_load_b16(rPH, phoff + j * 56, 0, 0);
        load_epoch();
        STAMP(1);
        u32x4 cc = ldl(lds + L_CS + ln * 16);
        ak = Z16;
        lptr bk = trb + L_WK; PIN(bk);
        gemm1<5>(ak, bk, xf, [](int) {}, [&](int i) {
          if (i == 4) {
            const unsigned sel = hh && kh ? 0u : ~0u;
            xc[0] = (xc[0] & sel) | (xc[2] & ~sel); xc[1] = (xc[1] & sel) | (xc[3] & ~sel); PIN(xc);
          }
          if (i >= 16 && i < 24) { const unsigned u = i < 20 ? xa[i & 3] : xc[i & 3]; xs[2 * (i - 16)] = bflo(u); xs[2 * (i - 16) + 1] = bfhi(u); PIN(xs); }
        }, NOAB);
        asm volatile("" :: "v"(ak));
        *(LAS u32x2*)(lds + L_WPH + (w >> 1) * 1024 + ln * 16 + (w & 1) * 8) = (u32x2){e[0] | (e[1] << 16), e[2] | (e[3] << 16)};
        make_tag();
        STAMPX(57);
        bar();
        STAMP(2);
        const u32x4 cv = ldl(lds + L_CS + 1024 + ln * 16);
        av = Z16;
        gemm1<5>(av, bk + SLAB, xf, [&](int i) { if (i == 8) { ak = mfma(cc, mf, ak); SB(); } if (i == 10) { av = mfma(cv, mf, av); SB(); } }, [&](int i) {
          if (i == 2) {
            float s1 = U2F(F2U(ak[12]) & ~lomask), s2 = s2a + s2b;
            red2<false>(s1, s2);
            stats(s1, s2);
          }
        }, NOAB);
      }
      asm volatile("" ::: "memory");
      arrive(0);
      STAMPX(pb + 1);
      const unsigned mid = WS_MID + (h * 4 + layer) * MID_B;
      int ln16 = lane * 16; PIN(ln16);
      u32x4 wph[2], kf0, kf1;
      if (type == 0) {
        wph[0] = ldl(lds + L_WPH + ln16); wph[1] = ldl(lds + L_WPH + 1024 + ln16);
        DUMP(0, ak, 1, rstd); DUMP(0, av, 2, rstd);
        kf0 = pack8<0>(ak); kf1 = pack8<1>(ak);
        f32x16 p = mfma(wph[0], kf0, Z16);
        p = mfma(wph[1], kf1, p);
        SB();
#pragma unroll
        for (int i = 0; i < 16; ++i) { float t = av[i] * rstd; PIN(t); av[i] = t; }
        u32x4 vb0 = pack8<0>(av), vb1 = pack8<1>(av);
        vb1[2] |= ~lomask & 0x3f80u;
        PIN(vb0); PIN(vb1);
        SB();
        u32x4 fp[2], fm[2];
        dual_softmax(p, rstd * L2E, wtail, fp, fm);
        fp[1][2] &= lomask; fp[1][3] &= lomask; fm[1][2] &= lomask; fm[1][3] &= lomask;
        PIN(fp[0]); PIN(fp[1]); PIN(fm[0]); PIN(fm[1]);
        gather_all(layer, false);
        const int rr = (ln16 >> 4) & 31, pc = (ln16 >> 9) ^ ((rr >> 1) & 3);
        const lptr d0 = lds + L_SCR + w * 2048 + rr * 64 + pc * 16, d1 = lds + L_SCR + w * 2048 + rr * 64 + (pc ^ 2) * 16;
        *(LAS u32x4*)(d0) = fp[0]; *(LAS u32x4*)(d1) = fp[1];
        *(LAS u32x4*)(d0 + 8192) = fm[0]; *(LAS u32x4*)(d1 + 8192) = fm[1];
        *(LAS u32x4*)(d0 + 16384) = vb0; *(LAS u32x4*)(d1 + 16384) = vb1;
        bar();
      } else {
        av *= rstd;
        DUMP(0, av, 2, 1.f);
        u32x4 vb1 = pack8<1>(av);
        vb1[2] |= ~lomask & 0x3f80u;
        f32x16 vt = mfma(pack8<0>(av), id0, Z16);
        vt = mfma(vb1, id1, vt);
        SB();
#pragma unroll
        for (int i = 0; i < 16; ++i) { float t = ak[i] * rstd; PIN(t); ak[i] = t; }
        DUMP(0, ak, 1, 1.f);
        kf0 = pack8<0>(ak); kf1 = pack8<1>(ak);
        const unsigned g = mid + T * 4096;
        const lptr d = lds + L_SCR + (8 + w) * 4096 + ln16;
        st128<SC1>(kf0, rWS, ln16, g);
        const u32x4 vf0 = pack8<0>(vt), vf1 = pack8<1>(vt);
        st128<SC1>(vf0, rWS, ln16 + 2048, g);
        st128<SC1>(vf1, rWS, ln16 + 3072, g);
        {
          u32x4 kc = kf1;
          kc[3] |= ~lomask & (tag[0] + layer - xhalf_addu(add4(add4(0u, kf0), kf1) + add4(add4(0u, vf0), vf1)));
          st128<SC1>(kc, rWS, ln16 + 1024, g);
          CALS(3 * layer);
        }
        gather_all(layer, false);
        *(LAS u32x4*)(d) = kf0; *(LAS u32x4*)(d + 1024) = kf1; *(LAS u32x4*)(d + 2048) = vf0; *(LAS u32x4*)(d + 3072) = vf1;
      }
      STAMPX(pb + 2);
      u32x4 qf0, qf1;
      {
        f32x16 aq = mfma(ldl(lds + L_CS + type * 3072 + 2048 + ln16), mf, Z16);
        lptr bq = trb + L_WQ; PIN(bq);
        gemm1<5>(aq, bq, xf, [](int) {}, [](int) {}, NOAB);
        aq *= type ? rstd * (0.18898223650461361f * L2E) : 1.0f;
        DUMP(0, aq, 0, type ? 1.f : rstd);
        qf0 = pack8<0>(aq); qf1 = pack8<1>(aq);
        qf1[2] |= ~lomask & 0x3f80u;
      }
      f32x16 o;
      if (type == 0) {
        u32x4 fp[2], fm[2];
        {
          f32x16 p = mfma(wph[0], qf0, Z16);
          p = mfma(wph[1], qf1, p);
          dual_softmax(p, rstd * L2E, wtail, fp, fm);
        }
        PIN(fp[0]); PIN(fp[1]); PIN(fm[0]); PIN(fm[1]);
        STAMPX(pb + 3);
        bar();
        STAMP(pb + 4);
        const lptr kvf = lds + L_KVF + ln16;
        const u32x4 a0 = ldl(kvf), a1 = ldl(kvf + 1024), a2 = ldl(kvf + 2048), a3 = ldl(kvf + 3072);
        SB();
        o = mfma(a0, fp[0], Z16);
        o = mfma(a1, fp[1], o);
        o = mfma(a2, fm[0], o);
        o = mfma(a3, fm[1], o);
        o *= __builtin_amdgcn_rcpf(upper_val(o[12]) + 1e-6f);
      } else {
        lptr kv = lds + L_SCR + w * 4096 + ln16; PIN(kv);
        auto ex4 = [&](f32x16& s, int i0) {
#pragma unroll
          for (int i = i0; i < i0 + 4; ++i) s[i] = __builtin_amdgcn_exp2f(s[i]);
          unsigned a = pk2(s[i0], s[i0 + 1]), c = pk2(s[i0 + 2], s[i0 + 3]); PIN(a); PIN(c);
          SB();
          return (u32x2){a, c};
        };
        auto tile_max = [&](int j, int side, float m) {
          f32x16 t = mfma(ldl(kv + j * 4096 + 1024), qf1, mfma(ldl(kv + j * 4096), qf0, Z16));
          int lim = ((ln16 >> 4) & 31) - ((ln16 >> 7) & 4); PIN(lim);
#pragma unroll
          for (int i = 0; i < 16; ++i) { const bool up = (i & 3) + 8 * (i >> 2) > lim; m = fmaxf(m, (side == 0 || up == (side == 1)) ? t[i] : m); }
          return m;
        };
        float m = -__builtin_inff(), l;
        for (int pass = 0;; ++pass) {
          const u32x4 mk0 = ldl(lds + L_MK + ln16), mk1 = ldl(lds + L_MK + 1024 + ln16);
          f32x16 negm;
          {
            const u32x4 v0 = ldl(kv + 8 * 4096 + 2048), v1 = ldl(kv + 8 * 4096 + 3072);
            f32x16 s = mfma(ldl(kv + 8 * 4096 + 1024), qf1, mfma(ldl(kv + 8 * 4096), qf0, Z16));
#pragma unroll
            for (int i = 0; i < 16; ++i) {
              const unsigned k = (i < 8 ? mk0 : mk1)[(i >> 1) & 3], on = (i & 1) ? (unsigned)((int)k >> 31) : (unsigned)((int)(k << 16) >> 31);
              s[i] = U2F((F2U(s[i]) & ~on) | (on & 0xff800000u));
            }
#pragma unroll
            for (int i = 0; i < 16; i += 2) m = fmax3(m, s[i], s[i + 1]);
            m = xhalf_max(m);
            s -= m;
            negm = splat(-m);
            PIN(negm);
            const u32x2 a = ex4(s, 0), c = ex4(s, 4), e = ex4(s, 8), f = ex4(s, 12);
            o = mfma(v1, cat(e, f), mfma(v0, cat(a, c), Z16));
            PIN(o);
          }
          if (pass == 0) {
            STAMPX(pb + 3);
            bar();
            STAMP(pb + 4);
          }
          u32x4 kk[2][2], vv[2][2], pa, pb2;
          f32x16 st[2];
          kk[0][0] = ldl(kv); kk[0][1] = ldl(kv + 1024); kk[1][0] = ldl(kv + 4096); kk[1][1] = ldl(kv + 4096 + 1024);
          SB();
          st[0] = mfma(kk[0][1], qf1, mfma(kk[0][0], qf0, negm));
          SB();
#pragma unroll
          for (int j = 0; j < 8; ++j) {
            f32x16& s = st[j & 1];
            vv[j & 1][0] = ldl(kv + j * 4096 + 2048); vv[j & 1][1] = ldl(kv + j * 4096 + 3072);
            if (j < 6) { kk[j & 1][0] = ldl(kv + (j + 2) * 4096); kk[j & 1][1] = ldl(kv + (j + 2) * 4096 + 1024); }
            SB();
            if (j == 0) { st[1] = mfma(kk[1][0], qf0, negm); SB(); }
            const u32x2 a = ex4(s, 0);
            if (j == 0) st[1] = mfma(kk[1][1], qf1, st[1]); else o = mfma(vv[(j - 1) & 1][0], pa, o);
            SB();
            const u32x2 c = ex4(s, 4);
            if (j == 7) o = mfma(vv[0][1], pb2, o); else if (j > 0) st[(j + 1) & 1] = mfma(kk[(j + 1) & 1][0], qf0, negm);
            SB();
            const u32x2 e = ex4(s, 8);
            if (j == 7) o = mfma(vv[1][0], cat(a, c), o); else if (j > 0) st[(j + 1) & 1] = mfma(kk[(j + 1) & 1][1], qf1, st[(j + 1) & 1]);
            SB();
            const u32x2 f = ex4(s, 12);
            if (j == 7) o = mfma(vv[1][1], cat(e, f), o); else if (j > 0) o = mfma(vv[(j - 1) & 1][1], pb2, o);
            SB();
            pa = cat(a, c); pb2 = cat(e, f);
            if (j == 0) { pa &= mk0; pb2 &= mk1; PIN(pa); PIN(pb2); }
          }
          l = upper_val(o[12]);
          if (pass > 0 || __all(l < LMAX)) break;
          RET(13);
          m = tile_max(0, 1, m);
#pragma unroll 1
          for (int j = 1; j < 8; ++j) m = tile_max(j, 0, m);
        }
        arrive(4);
        o *= __builtin_amdgcn_rcpf(l);
      }
      DUMP(1, o, 0, 1.f);
      store_o(pack8<0>(o), pack8<1>(o), WS_XO + (2 * layer) * XO_B);
      CALS(3 * layer + 1);
      STAMPX(pb + 6);
      lptr bown, both;
      {
        int ln = lane; PIN(ln);
        const int q = (ln >> 2) & 3, rp = ((ln >> 2) & 4) + (ln & 3), lam = (rp & 4) | ((rp & 1) << 1) | ((rp >> 1) & 1);
        const int pe = lam, po = 6 + ((0x01327654 >> (4 * lam)) & 7);
        const lptr rb = lds + L_WO + kh * SLAB + (8 * (ln >> 5) + q) * 128;
        const lptr ae = rb + ((pe * 8) ^ ((q >> 1) * 64)), ao = rb + ((po * 8) ^ ((q >> 1) * 64));
        bown = kh ? ao : ae; both = kh ? ae : ao;
        PIN(bown); PIN(both);
      }
      unsigned base = WS_XO + (2 * layer) * XO_B + XOFF + kh * 12288;
      int v16 = ln16, vs = (v16 >> 1) + ((v16 >> 1) & 256);
      PIN(vs);
      release(3 * layer + 1);
      STAMP(pb + 7);
      f32x16 xn, xo;
      u32x4 wa[4];
        auto tr = [](lptr p) {
          bf16x4 a0 = __builtin_amdgcn_ds_read_tr16_b64_v4bf16((LAS bf16x4*)p), a1 = __builtin_amdgcn_ds_read_tr16_b64_v4bf16((LAS bf16x4*)(p + 512));
          return __builtin_bit_cast(u32x4, __builtin_shufflevector(a0, a1, 0, 1, 2, 3, 4, 5, 6, 7));
        };
      for (;;) {
        PIN(v16); PIN(vs); asm volatile("" : "+s"(base));
        u32x2 sp[4];
        u32x4 wb[4];
#pragma unroll
        for (int u = 0; u < 4; ++u) { sp[u] = __builtin_amdgcn_raw_buffer_load_b64(rWS, vs + (u >> 1) * 1024 + (u & 1) * 256, base + 24576 - kh * 10240, SC1); SB(); if (u & 1) PACED(); }
#pragma unroll
        for (int s = 0; s < LAO; ++s) { xf[2 + s] = __builtin_amdgcn_raw_buffer_load_b128(rWS, v16 + (s & 3) * 1024, base + (s >> 2) * 4096, SC1); SB(); if (s < 3) wb[s] = tr(both + s * 2048); else if (s < 6) wa[s - 3] = tr(bown + (s - 3) * 2048); SB(); if (s < 6) PACE2(); else PACED(); }
        xf[0] = cat(sp[0], sp[1]); xf[1] = cat(sp[2], sp[3]);
        xn = xs; xo = Z16;
        unsigned m = 1;
#pragma unroll
        for (int i = 0; i < 14; ++i) {
          if (i + LAO < 12) { const int s = i + LAO; xf[2 + s] = __builtin_amdgcn_raw_buffer_load_b128(rWS, v16 + (s & 3) * 1024, base + (s >> 2) * 4096, SC1); SB(); }
          if (i + 3 < 14) wb[(i + 3) & 3] = tr(both + (i + 3) * 2048);
          SB();
          xo = mfma(wb[i & 3], xf[i], xo);
          SB();
          m = max4(m, xf[i]); PIN(m);
          if (i >= LAG) {
            wa[(i - LAG + 3) & 3] = tr(bown + (i - LAG + 3) * 2048);
            SB();
            xn = mfma(wa[(i - LAG) & 3], xf[i - LAG], xn);
          }
          SB();
        }
        if (__builtin_expect(!__any(m == sent), 1)) break;
        if (--budget == 0) sent = 0u;
        RET(layer * 3 + 2);
        __builtin_amdgcn_s_sleep(BACK);
        asm volatile("" ::: "memory");
      }
      {
        const lptr ex = lds + L_WQ + ln16;
        f32x4 t[4];
#pragma unroll
        for (int i = 14 - LAG; i < 14; ++i) {
          if (i + 3 < 14) wa[(i + 3) & 3] = tr(bown + (i + 3) * 2048);
          SB();
          xn = mfma(wa[i & 3], xf[i], xn);
          SB();
          if (i == 15 - LAG) {
#pragma unroll
            for (int g = 0; g < 4; ++g) *(LAS f32x4*)(ex + w * 4096 + g * 1024) = (f32x4){xo[4 * g], xo[4 * g + 1], xo[4 * g + 2], xo[4 * g + 3]};
          }
          if (i == 11) {
            bar();
#pragma unroll
            for (int g = 0; g < 4; ++g) t[g] = *(LAS f32x4*)(ex + (w ^ 1) * 4096 + g * 1024);
          }
          SB();
        }
#pragma unroll
        for (int g = 0; g < 4; ++g) {
#pragma unroll
          for (int j = 0; j < 4; ++j) xs[4 * g + j] = xn[4 * g + j] + t[g][j];
        }
      }
      STAMPX(pb + 8);
      if (layer == NL - 1) {
        const rsrc_t rOUT = __builtin_amdgcn_make_buffer_rsrc((void*)out, 0, SEQ * DM * 2, 0x00020000);
        const u32x4 f0 = pack8<0>(xs), f1 = pack8<1>(xs);
        int ln = lane; PIN(ln);
        const int row = (ln & 31) * 896, up = ln >> 5;
        const int ob = (tb * 4 + tx) * 32 * 896 + 7 * pair * 16;
        st128<0>(f0, rOUT, row + up * 16, ob + 5 * kh * 16);
        st128<0>(f1, rOUT, row + up * OOB, ob + (2 + 2 * kh) * 16);
        __builtin_amdgcn_raw_buffer_store_b64(lo2(f1), rOUT, row + (1 - up) * OOB, ob + 3 * 16 + kh * 8, 0);
      } else {
        const u32x4 f0 = pack8<0>(xs);
        store_x0(f0, WS_XO + (2 * layer + 1) * XO_B);
        u32x4 f1 = pack8<1>(xs);
        f1[2] &= lomask; f1[3] &= lomask;
        float s1 = 0.f, s2 = 0.f, t1 = 0.f, t2 = 0.f;
#pragma unroll
        for (int j = 0; j < 4; ++j) { const unsigned u = f0[j], v = f1[j]; s1 = dot2(u, ONES, s1); s2 = dot2(u, u, s2); t1 = dot2(v, ONES, t1); t2 = dot2(v, v, t2); }
        s1 += t1; s2 += t2;
        red2<false>(s1, s2);
        f1[2] |= ~lomask & F2U(s1); f1[3] |= ~lomask & F2U(s2);
        store_x1(f1, WS_XO + (2 * layer + 1) * XO_B);
        CALS(3 * layer + 2);
        STAMPX(pb + 9);
      }
      asm volatile("" : "=v"(ak), "=v"(av), "=v"(rstd), "=v"(mf), "=v"(phoff));
#pragma unroll
      for (int i = 14; i < 28; ++i) asm volatile("" : "=v"(xf[i]));
    }
    STAMP(61); RSTAMP(63);
    TOT1
    if (b == 0 && w == 0) __builtin_amdgcn_raw_buffer_store_b64((u32x2){(unsigned)(epoch + 1), (unsigned)((epoch + 1) >> 32)}, rWS, WS_EPOCH, 0, SC1);
  } else {
    STAMPX(53);
    u32x4 pA[21], pB[21];
    unsigned tch0, tch1;
    asm volatile("s_load_dword %0, %2, 0x0\n\ts_load_dword %1, %3, 0x0" : "=&s"(tch0), "=&s"(tch1)
                 : "s"(w == 0 ? (const void*)ws : w == 1 ? (const void*)wqkvB : w == 2 ? (const void*)woA : (const void*)wphi), "s"(w < 2 ? (const void*)woB : (const void*)out));
    dma_x0(0, 4);
    unsigned warm0;
    {
      u64 pc; asm volatile("s_getpc_b64 %0" : "=s"(pc));
      const rsrc_t rs = __builtin_amdgcn_make_buffer_rsrc(w == 3 ? (void*)(pc & ~127ull) : (void*)wqkvA, 0, w == 3 ? CODE_B : DM * 3 * DM * 2, 0x00020000);
      const int n = w == 3 ? 9 : 13;
      warm0 = __builtin_amdgcn_raw_buffer_load_b32(rs, lane < n ? (lane + (w == 3 ? (b >> 3) : b * 3 + w) * n) * 128 : OOB, 0, 0);
      SB();
    }
#pragma unroll
    for (int i = 0; i < 7; ++i) { mkrow(i); PIN(krow[i]); }
    lptr trb = make_trb(); PIN(trb);
    u32x4 id0 = ident_frag(0, r, hh); PIN(id0);
    SB();
    dma_x0(4, 28);
#pragma unroll
    for (int i = 0; i < NV0; ++i) pA[14 + i] = ldw(rWA, i, 2688, 1792);
    STAMPX(54);
    asm volatile("s_waitcnt vmcnt(%2) lgkmcnt(0)" :: "s"(tch0), "s"(tch1), "n"(NV0) : "memory");
    const u32x4 sv = {SENT, SENT, SENT, SENT};
    auto clean_kv = [&](int layer) { if (layer < NL) st128<SC1>(sv, rWS, lane * 16, WS_MID + (h * 4 + layer) * MID_B + (tb * 4 + w) * 1024); };
    auto clean_o = [&](int layer) { if (layer < NL) store_o(sv, sv, WS_XO + (2 * layer) * XO_B); };
    auto clean_x = [&](int layer) { if (layer < NL - 1) { store_x0(sv, WS_XO + (2 * layer + 1) * XO_B); store_x1(sv, WS_XO + (2 * layer + 1) * XO_B); } };
    unsigned r0 = (unsigned)__builtin_amdgcn_s_memrealtime(), c0 = (unsigned)__builtin_amdgcn_s_memtime(), tann = 0;
    auto mark = [&](int cyc) {
      const unsigned r1 = (unsigned)__builtin_amdgcn_s_memrealtime(), c1 = (unsigned)__builtin_amdgcn_s_memtime();
      tann = r1 + LAND + (unsigned)((float)cyc * (float)(r1 - r0) * __builtin_amdgcn_rcpf((float)(c1 - c0)));
      r0 = r1; c0 = c1;
    };
    auto push = [&](int hop, unsigned voff) {
      CALA(hop);
      __builtin_amdgcn_raw_buffer_store_b64((u32x2){tag[0], (tag[1] & 0xfff00000u) | (tann & 0xfffffu)}, rWS, voff, WS_FLAGS + hop * HOP_B, SC1);
    };
    auto push_grp = [&](int hop) { push(hop, lane < 16 ? (tb * 16 + lane) * 128 + h * 8 : OOB); };
    auto push_mid = [&](int layer) {
      if (layer & 1) push(3 * layer, (lane < 2 && tb + 1 + lane < 16) ? ((tb + 1 + lane) * 16 + h) * 128 + lane * 8 : OOB);
      else push(3 * layer, lane < 16 ? (lane * 16 + h) * 128 + tb * 8 : OOB);
    };
    const rsrc_t rWB = __builtin_amdgcn_make_buffer_rsrc((void*)wqkvB, 0, DM * 3 * DM * 2, 0x00020000);
    const rsrc_t rOA = __builtin_amdgcn_make_buffer_rsrc((void*)woA, 0, DM * DM * 2, 0x00020000);
    const rsrc_t rOB = __builtin_amdgcn_make_buffer_rsrc((void*)woB, 0, DM * DM * 2, 0x00020000);
    auto spin = [&](int k, unsigned n) { while (__builtin_amdgcn_readfirstlane(cnt[k]) < n) __builtin_amdgcn_s_sleep(1); asm volatile("" ::: "memory"); };
    auto wait_h = [&](int hop, int mask) {
      if (w == 0) { if (mask >= 0) poll(hop, mask); cnt[1] = hop + 1; }
      else while (__builtin_amdgcn_readfirstlane(cnt[1]) != hop + 1) { }
      asm volatile("" ::: "memory");
    };
    const unsigned pkb = WS_PK + h * 262144;
    auto ldp = [&](unsigned off) { const u32x4 v = __builtin_amdgcn_raw_buffer_load_b128(rWS, lane * 16, pkb + off + w * 1024, SC1); SB(); return v; };
    STAMPX(55);
    bar();
#pragma unroll
    for (int i = NV0; i < 7; ++i) pA[14 + i] = ldw(rWA, i, 2688, 1792);
    load_epoch();
    u32x4 pw[4];
    {
      const unsigned act = tb < 14 ? 0u : OOB;
#pragma unroll
      for (int j = 0; j < 2; ++j) {
        const int st = 2 * tb + j, p = (st * 43) >> 7, m = st - 3 * p;
        const int c = st < 24 ? 7 * p + ((0x520 >> (4 * m)) & 15) + hh * (1 + (m & 1)) : 7 * (2 * (st - 24) + hh) + 3;
        pw[j] = __builtin_amdgcn_raw_buffer_load_b128(rWB, ((8 * c + ((lane >> 2) & 7)) * 2688 + sub * 16) | (w < 3 ? act : OOB), col + (w == 2 ? 0 : 896 + 896 * w), 0);
        SB();
      }
      {
        const int n = 4 * tb + w, k2 = n >= 28, i = (n - 28 * k2) >> 1, st = i - 2, p = (st * 43) >> 7, m = st - 3 * p;
        const int c = 28 * k2 + (i < 2 ? 14 * i + 3 + 7 * (n & 1) : 7 * p + ((0x520 >> (4 * m)) & 15) + (n & 1) * (1 + (m & 1)));
        const unsigned vo = (c * 7168 + (lane >> 3) * 896 + 112 * pair + (((lane & 7) ^ ((lane >> 2) & 4)) * 16)) | act;
        pw[2] = __builtin_amdgcn_raw_buffer_load_b128(rOA, vo, 0, 0); SB();
        pw[3] = __builtin_amdgcn_raw_buffer_load_b128(rOB, vo, 0, 0); SB();
      }
    }
    __builtin_amdgcn_s_setprio(3);
    STAMPX(57);
    spin(5, 4);
    put_slab(pA + 14, 0, 1);
#pragma unroll
    for (int j = 0; j < NQ1; ++j) pA[j] = ldw(rWA, j, 2688, 0);
    sum_slab(0, 1, trb);
#pragma unroll
    for (int j = NQ1; j < NQ0; ++j) pA[j] = ldw(rWA, j, 2688, 0);
#pragma unroll
    for (int i = 0; i < 7; ++i) pA[7 + i] = ldl(lds + L_WK + i * 4096 + ht * 16);
    STAMPX(58);
    bar();
    if (w == 3) mark(TKA0);
    __builtin_amdgcn_s_setprio(0);
#pragma unroll
    for (int j = NQ0; j < 7; ++j) pA[j] = ldw(rWA, j, 2688, 0);
    {
      const unsigned keep01 = (odd && sub == 0) ? 0u : ~0u, keep23 = (!odd && sub == 3) ? 0u : ~0u, act = (tb < 14 ? 0u : OOB) | (lane * 16);
#pragma unroll
      for (int j = 0; j < 2; ++j) st128<SC1>(pw[j] & (u32x4){keep01, keep01, keep23, keep23}, rWS, w < 3 ? act : OOB, pkb + w * SLAB + (2 * tb + j) * 1024);
      st128<SC1>(pw[2], rWS, act, pkb + 3 * SLAB + (4 * tb + w) * 1024);
      st128<SC1>(pw[3], rWS, act, pkb + 5 * SLAB + (4 * tb + w) * 1024);
    }
    clean_kv(0); clean_o(0);
    make_tag();
    STAMPX(59);
    first_slab(pA, 0, 2, trb);
    drain();
    arrive(3);
    if (w == 3) { spin(3, 4); push_mid(0); }
    asm volatile("" :: "v"(warm0));
    __builtin_amdgcn_s_setprio(3);
    u32x4 po[14];
    auto swap_wo = [&](bool keep) {
      lptr d = lds + L_WO + ht * 16; PIN(d);
#pragma unroll
      for (int i = 0; i < 14; ++i) { u32x4 t; if (keep) t = ldl(d + i * 4096); *(LAS u32x4*)(d + i * 4096) = po[i]; if (keep) po[i] = t; SB(); }
    };
    STAMPX(60);
#pragma unroll
    for (int layer = 0; layer < NL; ++layer) {
      const int type = layer & 1;
      const int pb = 2 + layer * 14;
      const unsigned mid = WS_MID + (h * 4 + layer) * MID_B;
      if (type == 0) {
        lptr ta0, ta1;
        {
          const int rb = (lane >> 4) & 1, q = (lane >> 2) & 3, p = lane & 3;
          const int rp = 4 * rb + p, lam = (rp & 4) | ((rp & 1) << 1) | ((rp >> 1) & 1);
          ta0 = lds + L_SCR + (8 * hh + q) * 64 + (((lam >> 1) ^ (q >> 1)) * 16) + (lam & 1) * 8;
          ta1 = lds + L_SCR + (8 * hh + q + 4) * 64 + (((lam >> 1) ^ (q >> 1) ^ 2) * 16) + (lam & 1) * 8;
          PIN(ta0); PIN(ta1);
        }
        bar();
        STAMPX(pb + 0);
        {
          auto tr2 = [&](unsigned off) {
            bf16x4 a0 = __builtin_amdgcn_ds_read_tr16_b64_v4bf16((LAS bf16x4*)(ta0 + off));
            bf16x4 a1 = __builtin_amdgcn_ds_read_tr16_b64_v4bf16((LAS bf16x4*)(ta1 + off));
            return __builtin_bit_cast(u32x4, __builtin_shufflevector(a0, a1, 0, 1, 2, 3, 4, 5, 6, 7));
          };
          const unsigned so = (w >> 1) * 8192;
          u32x4 fa[4], fb[4];
          f32x16 kv = Z16;
#pragma unroll
          for (int i = 0; i < 3; ++i) { fa[i] = tr2(so + i * 1024); fb[i] = tr2(16384 + i * 1024); }
          SB();
#pragma unroll
          for (int i = 0; i < 8; ++i) {
            if (i + 3 < 8) { fa[(i + 3) & 3] = tr2(so + (i + 3) * 1024); fb[(i + 3) & 3] = tr2(16384 + (i + 3) * 1024); }
            SB();
            kv = mfma(fa[i & 3], fb[i & 3], kv);
            SB();
          }
          st128<SC1>((w & 1) ? pack8<1>(kv) : pack8<0>(kv), rWS, lane * 16, mid + (tb * 4 + w) * 1024);
          CALS(3 * layer);
        }
        STAMPX(pb + 1);
        if (layer == 0) clean_x(0);
        wait_h(3 * layer, 15);
        STAMPX(pb + 2);
        f32x16 sum;
        for (;;) {
          sum = Z16;
          unsigned m = 0;
          u32x4 pr[16];
          const unsigned g = mid + w * 1024;
#pragma unroll
          for (int i = 0; i < 16; ++i) {
            pr[i] = __builtin_amdgcn_raw_buffer_load_b128(rWS, lane * 16, g + i * 4096, SC1); SB();
            if (i < 15) PACED();
          }
          if (w == 3) { mark(layer ? TOA : TOA0); push_grp(3 * layer + 1); SB(); }
#pragma unroll
          for (int i = 0; i < 16; ++i) {
            if (layer == 0 && i < NPO) po[i] = ldp(3 * SLAB + i * 4096);
            sum = mfma(id0, pr[i], sum); m = max4(m, pr[i]); PIN(m); SB();
          }
          if (!__any(m == SENT) || budget == 0) break;
          --budget; RET(layer * 3 + 1);
          asm volatile("" ::: "memory");
        }
        *(LAS u32x4*)(lds + L_KVF + w * 1024 + lane * 16) = pack8<0>(sum);
        STAMPX(pb + 3);
        bar();
        if (layer == 0) {
#pragma unroll
          for (int i = NPO; i < 14; ++i) po[i] = ldp(3 * SLAB + i * 4096);
        } else __builtin_amdgcn_s_sleep(5);
      } else {
        gather_all(layer, true);
        STAMPX(pb + 0);
        u32x4 t[8];
        wait_h(3 * layer, tb >= 2 ? 1 : tb - 1);
        STAMPX(pb + 2);
        if (tb - 2 + (w >> 1) >= 0) {
          const unsigned g = mid + (tb * 4 - 8 + 2 * w) * 4096;
          for (;;) {
            unsigned c[2] = {0u, 0u};
#pragma unroll
            for (int i = 0; i < 8; ++i) { t[i] = __builtin_amdgcn_raw_buffer_load_b128(rWS, lane * 16 + (i & 3) * 1024, g + (i >> 2) * 4096, SC1); SB(); if (i < 7) PACED(); }
#pragma unroll
            for (int i = 0; i < 8; ++i) {
              c[i >> 2] = add4(c[i >> 2], t[i]); PIN(c[i >> 2]);
              if ((i & 3) == 1) t[i][3] &= (unsigned)hh - 1u;
              *(LAS u32x4*)(lds + L_SCR + 2 * w * 4096 + i * 1024 + lane * 16) = t[i];
              SB();
            }
            const auto cs = __builtin_amdgcn_permlane32_swap(c[0], c[1], false, false);
            if (__all(cs[0] + cs[1] == tag[0] + layer) || budget == 0) break;
            --budget; RET(layer * 3 + 1);
            __builtin_amdgcn_s_sleep(8);
            asm volatile("" ::: "memory");
          }
        } else {
#pragma unroll
          for (int i = 0; i < 8; ++i)
            *(LAS u32x4*)(lds + L_SCR + 2 * w * 4096 + i * 1024 + lane * 16) = (u32x4){0u, 0u, ((i & 3) == 1 && hh) ? 0xff80u : 0u, 0u};
        }
        drain();
        STAMPX(pb + 3);
        bar();
        if (w == 3) { mark(layer == 1 ? TOB1 : TOB); push_grp(3 * layer + 1); }
        __builtin_amdgcn_s_setprio(0);
        if (layer == 3) {
          const rsrc_t rOUT = __builtin_amdgcn_make_buffer_rsrc((void*)out, 0, SEQ * DM * 2, 0x00020000);
          const unsigned d = __builtin_amdgcn_raw_buffer_load_b32(rOUT, hh ? OOB : r * 896 + kh * 108, (tb * 4 + tx) * 32 * 896 + pair * 112, 0);
          asm volatile("" :: "v"(d));
        }
        if (layer == 1) {
          clean_x(1); clean_kv(2);
#pragma unroll
          for (int i = 0; i < 14; ++i) po[i] = ldp(5 * SLAB + i * 4096);
          swap_wo(true);
          drain();
          spin(4, 4);
        }
      }
      __builtin_amdgcn_s_setprio(0);
      if (layer == 0) {
        swap_wo(false);
      } else if (layer < NL - 1) {
        write_slab((type == 0 ? pB : pA) + 7, L_WK);
      }
      STAMPX(pb + 4);
      bar();
      if (w == 3 && layer < NL - 1) { mark(layer == 2 ? TX2 : layer ? TX : TX0); push_grp(3 * layer + 2); }
      STAMPX(pb + 5);
      if (layer > 0 && layer < NL - 1) { __builtin_amdgcn_s_sleep(WSL); write_slab((type == 0 ? pB : pA) + 14, L_WV); }
      if (layer == 0) {
        __builtin_amdgcn_s_sleep(12);
#pragma unroll
        for (int i = 0; i < 21; ++i) pB[(i + 7) % 21] = ldp(i * 4096);
      }
      bar();
      if (layer < NL - 1) {
        __builtin_amdgcn_s_sleep(SWS);
        if (layer == 0) { write_slab(pB + 7, L_WK); write_slab(pB + 14, L_WV); sum_slab(1, 0, trb); sum_slab(1, 1, trb); }
        else swap_wo(layer == 1);
        bar();
        __builtin_amdgcn_s_sleep(WSL);
        write_slab(type == 0 ? pB : pA, L_WQ);
        if (layer == 0) sum_slab(1, 2, trb);
        arrive(0);
        while (!__all(cnt[8 + (lane & 3)] == layer + 1u)) __builtin_amdgcn_s_sleep(1);
        if (w == 3) { mark(layer == 0 ? TKB1 : layer == 1 ? TKA : TKB); push_mid(layer + 1); }
        __builtin_amdgcn_s_sleep(CSL);
        clean_o(layer + 1);
        if (layer == 1) { clean_x(2); drain(); }
        __builtin_amdgcn_s_setprio(3);
      }
    }
  }
}
}

extern "C" void kernel_launch(void* const* d_in, const int* in_sizes, int n_in, void* d_out, int out_size, void* d_ws, size_t ws_size, hipStream_t stream) {
  (void)hipFuncSetAttribute((const void*)fused_kernel, hipFuncAttributeMaxDynamicSharedMemorySize, L_END);
  fused_kernel<<<256, 512, L_END, stream>>>((const u16*)d_in[0], (const u16*)d_in[1], (const u16*)d_in[2], (const u16*)d_in[3],
                                            (const u16*)d_in[5], (const u16*)d_in[6], (u16*)d_out, (unsigned char*)d_ws);
}
```

```cpp
#define NL 4
#include <hip/hip_runtime.h>
#include <hip/hip_bf16.h>

typedef unsigned short u16;
typedef unsigned long long u64;
typedef __bf16 bf16x8 __attribute__((ext_vector_type(8)));
typedef __bf16 bf16x4 __attribute__((ext_vector_type(4)));
typedef __bf16 bf16x2 __attribute__((ext_vector_type(2)));
typedef float f32x16 __attribute__((ext_vector_type(16)));
typedef float f32x4 __attribute__((ext_vector_type(4)));
typedef float f32x2 __attribute__((ext_vector_type(2)));
typedef unsigned u32x4 __attribute__((ext_vector_type(4)));
typedef unsigned u32x2 __attribute__((ext_vector_type(2)));
#define LAS __attribute__((address_space(3)))
#define GAS __attribute__((address_space(1)))
#define DI __device__ __forceinline__
#define SB() __builtin_amdgcn_sched_barrier(0)
#define PIN(x) asm volatile("" : "+v"(x))
typedef __amdgpu_buffer_rsrc_t rsrc_t;
typedef LAS unsigned char* lptr;

namespace {
constexpr int SEQ = 2048, DM = 448;
constexpr int SC1 = 16;
constexpr unsigned WS_EPOCH = 0;
constexpr unsigned WS_FLAGS = 4096;
constexpr unsigned HOP_B = 256 * 128;
constexpr unsigned TILE_B = 32768;
constexpr unsigned XO_B = 4 * TILE_B;
constexpr unsigned WS_XO = 1u << 20;
constexpr unsigned MID_B = 64 * 4096;
constexpr unsigned WS_MID = WS_XO + 16 * 8 * XO_B;
constexpr unsigned WS_PK = WS_MID + 64 * MID_B;
constexpr unsigned WS_PROF = WS_PK + 16 * 262144;
constexpr unsigned WS_END = WS_PROF + 256 * 2048;
constexpr unsigned SLAB = 448 * 64;
constexpr unsigned L_WK = 0, L_WV = SLAB, L_WQ = 2 * SLAB, L_WO = 3 * SLAB;
constexpr unsigned L_SCR = 0;
constexpr unsigned L_X0 = SLAB;
constexpr unsigned L_KVF = 5 * SLAB;
constexpr unsigned L_MK = L_KVF + 4096;
constexpr unsigned L_WPH = L_MK + 2048;
constexpr unsigned L_CS = L_WPH + 2048;
constexpr unsigned L_SYNC = L_CS + 6144;
constexpr unsigned L_PROF = L_SYNC + 64;
constexpr unsigned L_END = L_PROF + 2048;
static_assert(L_END <= 163840 && L_X0 + 4 * SLAB <= L_KVF, "lds");
constexpr unsigned SPIN_CAP = 1u << 15;
constexpr unsigned SENT = 0xffffffffu;
constexpr unsigned OOB = 0x7ff00000u;
constexpr unsigned ONES = 0x3f803f80u;
constexpr unsigned CODE_B = 36 * 1024;
constexpr float L2E = 1.4426950408889634f;

DI f32x16 mfma(u32x4 a, u32x4 b, f32x16 c) {
  return __builtin_amdgcn_mfma_f32_32x32x16_bf16(__builtin_bit_cast(bf16x8, a), __builtin_bit_cast(bf16x8, b), c, 0, 0, 0);
}
DI unsigned pk2(float a, float b) { f32x2 t = {a, b}; return __builtin_bit_cast(unsigned, __builtin_convertvector(t, bf16x2)); }
template <int S_> DI u32x4 pack8(const f32x16& c) {
  return (u32x4){pk2(c[8 * S_], c[8 * S_ + 1]), pk2(c[8 * S_ + 2], c[8 * S_ + 3]), pk2(c[8 * S_ + 4], c[8 * S_ + 5]), pk2(c[8 * S_ + 6], c[8 * S_ + 7])};
}
DI u32x4 cat(u32x2 a, u32x2 b) { return (u32x4){a[0], a[1], b[0], b[1]}; }
DI u32x2 lo2(u32x4 v) { return (u32x2){v[0], v[1]}; }
DI unsigned umax3(unsigned a, unsigned b, unsigned c) { return max(max(a, b), c); }
DI unsigned max4(unsigned m, u32x4 v) { return umax3(umax3(m, v[0], v[1]), v[2], v[3]); }
DI unsigned add4(unsigned m, u32x4 v) { return (m + v[0] + v[1]) + v[2] + v[3]; }
DI unsigned xhalf_addu(unsigned v) { auto r = __builtin_amdgcn_permlane32_swap(v, v, false, false); return r[0] + r[1]; }
DI float fmax3(float a, float b, float c) { return fmaxf(fmaxf(a, b), c); }
template <int CTRL> DI float dppadd(float v) { return v + __builtin_bit_cast(float, __builtin_amdgcn_update_dpp(0, __builtin_bit_cast(int, v), CTRL, 0xf, 0xf, true)); }
DI float fmin3(float a, float b, float c) { return fminf(fminf(a, b), c); }
DI f32x16 splat(float v) { f32x16 z; _Pragma("unroll") for (int i = 0; i < 16; ++i) z[i] = v; return z; }
#define Z16 splat(0.f)
DI u32x4 trfrag(lptr p) {
  bf16x4 a0 = __builtin_amdgcn_ds_read_tr16_b64_v4bf16((LAS bf16x4*)p);
  bf16x4 a1 = __builtin_amdgcn_ds_read_tr16_b64_v4bf16((LAS bf16x4*)(p + 256));
  return __builtin_bit_cast(u32x4, __builtin_shufflevector(a0, a1, 0, 1, 2, 3, 4, 5, 6, 7));
}
DI u32x4 ldl(lptr p) { return *(LAS u32x4*)p; }
DI float U2F(unsigned u) { return __uint_as_float(u); }
DI unsigned F2U(float f) { return __float_as_uint(f); }
template <bool MX> DI void red2(float& a, float& b) {
  auto r = __builtin_amdgcn_permlane32_swap(F2U(a), F2U(b), false, false);
  const float t = MX ? fmaxf(U2F(r[0]), U2F(r[1])) : U2F(r[0]) + U2F(r[1]);
  r = __builtin_amdgcn_permlane32_swap(F2U(t), F2U(t), false, false);
  a = U2F(r[0]); b = U2F(r[1]);
}
DI float xhalf_max(float v) { auto r = __builtin_amdgcn_permlane32_swap(F2U(v), F2U(v), false, false); return fmaxf(U2F(r[0]), U2F(r[1])); }
DI float upper_val(float v) { auto r = __builtin_amdgcn_permlane32_swap(F2U(v), F2U(v), false, false); return U2F(r[1]); }
DI float dot2(unsigned u, unsigned v, float c) { return __builtin_amdgcn_fdot2_f32_bf16(__builtin_bit_cast(bf16x2, u), __builtin_bit_cast(bf16x2, v), c, false); }
DI float bflo(unsigned u) { return U2F(u << 16); }
DI float bfhi(unsigned u) { return U2F(u & 0xffff0000u); }
DI void split2(float v, unsigned& hi, unsigned& lo) { hi = pk2(v, v) & 0xffffu; lo = pk2(v - bflo(hi), 0.f) & 0xffffu; }
template <int AUX> DI void st128(u32x4 v, rsrc_t rs, unsigned voff, unsigned soff) {
  SB();
  __builtin_amdgcn_raw_buffer_store_b128(v, rs, voff, soff, AUX);
  SB();
  asm volatile("s_nop 1" :: "v"(v));
  SB();
}
DI void drain() { asm volatile("s_waitcnt vmcnt(0)" ::: "memory"); }
DI void bar() { SB(); asm volatile("s_waitcnt lgkmcnt(0)\n\ts_barrier" ::: "memory"); SB(); }
constexpr int chunk_of(int s, int hh) { return s < 24 ? 7 * (s / 3) + (s % 3 == 0 ? hh : (s % 3 == 1 ? 2 + 2 * hh : 5 + hh)) : 7 * (2 * (s - 24) + hh) + 3; }
constexpr int ord(int i) { return i < 4 ? 24 + i : i - 4; }

#define STAMP(i) do { } while (0)
#define RET(i) do { } while (0)
#define RSTAMP(i) do { } while (0)
#define STAMPX(i) STAMP(i)
#define CALS(hop) do { } while (0)
#define CALA(hop) do { } while (0)
#define TOT0
#define TOT1
#define DUMP(buf, acc, part, sc) do { } while (0)

DI void dual_softmax(const f32x16& p, float c, float wtail, u32x4 (&fp)[2], u32x4 (&fm)[2]) {
  float mx = fmax3(p[0], p[1], p[2]), mn = fmax3(-p[0], -p[1], -p[2]);
#pragma unroll
  for (int i = 3; i < 15; i += 2) { mx = fmax3(mx, p[i], p[i + 1]); mn = fmax3(mn, -p[i], -p[i + 1]); }
  mx = fmax3(mx, p[15], p[15]); mn = fmax3(mn, -p[15], -p[15]);
  red2<true>(mx, mn);
  const float a = -c * mx, b = -c * mn;
  float nc = -c; PIN(nc);
  f32x16 ep = p * c + a, em = p * nc + b;
  SB();
#pragma unroll
  for (int i = 0; i < 16; ++i) ep[i] = __builtin_amdgcn_exp2f(ep[i]);
#pragma unroll
  for (int i = 0; i < 16; ++i) em[i] = __builtin_amdgcn_exp2f(em[i]);
  f32x2 sp = {ep[0], ep[1]}, sm = {em[0], em[1]};
#pragma unroll
  for (int i = 2; i < 12; i += 2) { sp += (f32x2){ep[i], ep[i + 1]}; sm += (f32x2){em[i], em[i + 1]}; }
  sp += wtail * ((f32x2){ep[12], ep[13]} + (f32x2){ep[14], ep[15]});
  sm += wtail * ((f32x2){em[12], em[13]} + (f32x2){em[14], em[15]});
  float u = sp[0] + sp[1], v = sm[0] + sm[1];
  red2<false>(u, v);
  ep *= __builtin_amdgcn_rcpf(u); em *= __builtin_amdgcn_rcpf(v);
  fp[0] = pack8<0>(ep); fp[1] = pack8<1>(ep); fm[0] = pack8<0>(em); fm[1] = pack8<1>(em);
}
DI u32x4 ident_frag(int s, int r, int hh) {
  const unsigned val = 0x3f80u << (16 * (r & 1));
  const int idx = (((r >> 2) ^ hh) & 1) * 8 + (r >> 4) * 4 + ((r >> 2) & 2) + ((r >> 1) & 1) - 4 * s;
  return (u32x4){idx == 0 ? val : 0u, idx == 1 ? val : 0u, idx == 2 ? val : 0u, idx == 3 ? val : 0u};
}
#define CHK1 3
#define CHK2 12
template <typename S, typename A> DI void steps28(S step, A abort) {
#pragma unroll
  for (int i = 0; i <= CHK1; ++i) step(i);
  if (abort()) return;
#pragma unroll
  for (int i = CHK1 + 1; i < 28; ++i) step(i);
}
template <int PD, typename F, typename G, typename A> DI void gemm1(f32x16& acc, lptr wb, const u32x4 (&xf)[28], F pre, G post, A abort) {
  u32x4 wr[PD];
#pragma unroll
  for (int i = 0; i < PD - 1; ++i) wr[i] = trfrag(wb + ord(i) * 1024);
  SB();
  steps28([&](int i) __attribute__((always_inline)) {
    pre(i);
    if (i + PD - 1 < 28) wr[(i + PD - 1) % PD] = trfrag(wb + ord(i + PD - 1) * 1024);
    SB();
    acc = mfma(wr[i % PD], xf[i], acc);
    SB();
    post(i);
    SB();
  }, abort);
}
template <typename H, typename F, typename G, typename A>
DI void gemm2(f32x16& ak, f32x16& av, lptr bk, const u32x4 (&xf)[28], H head, F pre, G post, A abort) {
  u32x4 wk[3], wv[3];
  head([&](int u) {
    if (u == 0) wv[0] = trfrag(bk + SLAB + ord(0) * 1024);
    if (u == 1) wk[0] = trfrag(bk + ord(0) * 1024);
    if (u == 2) wv[1] = trfrag(bk + SLAB + ord(1) * 1024);
    if (u == 3) wk[1] = trfrag(bk + ord(1) * 1024);
    SB();
  });
  steps28([&](int i) __attribute__((always_inline)) {
    pre(i);
    if (i + 2 < 28) wv[(i + 2) % 3] = trfrag(bk + SLAB + ord(i + 2) * 1024);
    SB();
    av = mfma(wv[i % 3], xf[i], av);
    SB();
    if (i + 2 < 28) wk[(i + 2) % 3] = trfrag(bk + ord(i + 2) * 1024);
    post(i, 0);
    SB();
    ak = mfma(wk[i % 3], xf[i], ak);
    SB();
    post(i, 1);
    SB();
  }, abort);
}
#define NOAB [] { return false; }
#define EARLYAB [&] { return __builtin_expect(__any(m == sent), 0); }
#define LA 12
#define XH 27
#define VSLEEP 8
#define QSLEEP 28
#define LAO 8
#define XBAR 26
#define DOA 3
#define DOB 6
#define DX 3
#define KVI 6
#define KVB 3
#define NKB 5
#define XM 4
#ifndef NV0
#define NV0 7
#endif
#define NK2 8
#define NK3 9
#define PACE 8
#define LAG 6
#define PACED() do { if (PACE) asm volatile("s_nop %0" :: "n"(PACE)); SB(); } while (0)
#define PSLP 4
#define RDLY 0
#define KDLY 0
#define LAND 10
#define TKA0 4100
#define TKA 3600
#define TKB1 1950
#define TKB 2050
#define TOA0 2050
#define TOA 1850
#define TOB1 2750
#define TOB 2520
#define TX0 3800
#define TX 2850
#define BACK 8
#define NQ0 7
#define NQB 0
#define TX2 3200
#define ND0 10
#define NPO 14
#define SWS 6
#define LMAX 1.8446744e19f
#define CSL 2
#define WSL 4
#define PACE2() do { asm volatile("s_nop %0" :: "n"(PACE - 2)); SB(); } while (0)
#define NQ1 4
#define PDELAY 14

__global__ void __launch_bounds__(512, 2) fused_kernel(const u16* __restrict__ x0, const u16* __restrict__ wqkvA, const u16* __restrict__ wphi,
                                                       const u16* __restrict__ woA, const u16* __restrict__ wqkvB, const u16* __restrict__ woB,
                                                       u16* __restrict__ out, unsigned char* ws) {
  extern __shared__ __attribute__((aligned(16))) unsigned char smem[];
  u64 tstart; asm volatile("s_memrealtime %0\n\ts_waitcnt lgkmcnt(0)" : "=s"(tstart) :: "memory"); SB();
  while (__builtin_amdgcn_s_memrealtime() - tstart < 600) __builtin_amdgcn_s_sleep(2);
  unsigned probe = 0;
  const lptr lds = (lptr)smem;
  const int tid = threadIdx.x, lane = tid & 63, wave = __builtin_amdgcn_readfirstlane(tid >> 6);
  TOT0
  const int w = wave & 3;
  const int r = lane & 31, hh = lane >> 5;
  const int b = blockIdx.x;
  const int tb = ((b >> 1) & 3) * 4 + (b >> 6), h = (b & 1) * 8 + ((b >> 3) & 7);
  const int me = tb * 16 + h;
  const int odd = h & 1, pair = h >> 1;
  const int T = tb * 4 + w;
  const unsigned TOFF = tb * 8 * XO_B + w * TILE_B;
  const int kh = w & 1, tx = 2 * odd + (w >> 1);
  const unsigned XOFF = tb * 8 * XO_B + tx * TILE_B;
  const rsrc_t rWS = __builtin_amdgcn_make_buffer_rsrc((void*)ws, 0, WS_END, 0x00020000);
  unsigned budget = SPIN_CAP;
  u64 epoch;
  u32x2 tag;
  u32x2 ev;
  auto load_epoch = [&]() { ev = __builtin_amdgcn_raw_buffer_load_b64(rWS, 0, WS_EPOCH, SC1); SB(); };
  auto make_tag = [&]() {
    epoch = (u64)(unsigned)__builtin_amdgcn_readfirstlane(ev[0]) | ((u64)(unsigned)__builtin_amdgcn_readfirstlane(ev[1]) << 32);
    u64 z = epoch + 0x9E3779B97F4A7C15ull;
    z = (z ^ (z >> 30)) * 0xBF58476D1CE4E5B9ull; z = (z ^ (z >> 27)) * 0x94D049BB133111EBull; z ^= z >> 31;
    unsigned t0 = (unsigned)z, t1 = (unsigned)(z >> 32);
    asm volatile("" : "+s"(t0), "+s"(t1));
    tag = (u32x2){t0, t1};
  };
  volatile LAS unsigned* const cnt = (volatile LAS unsigned*)(lds + L_SYNC);
  auto arrive = [&](int k) { if (lane == 0) asm volatile("ds_add_u32 %0, %1" :: "v"(lds + L_SYNC + 4 * k), "v"(1u) : "memory"); };
  auto gather_all = [&](int layer, bool nap) {
    while (__builtin_amdgcn_readfirstlane(*cnt) < 8u * layer + 4u) { if (nap) __builtin_amdgcn_s_sleep(2); }
    asm volatile("" ::: "memory");
  };
  auto poll = [&](int hop, int mask) {
    int ln = lane & mask; PIN(ln);
    u32x2 v;
    for (;;) {
      v = __builtin_amdgcn_raw_buffer_load_b64(rWS, ln * 8, WS_FLAGS + hop * HOP_B + me * 128, SC1);
      asm volatile("" ::: "memory");
      if (__all(v[0] == tag[0] && ((v[1] ^ tag[1]) >> 20) == 0)) break;
      if (budget <= 1) break;
      --budget;
      if (PSLP) __builtin_amdgcn_s_sleep(PSLP);
    }
    const unsigned t = v[1] << 12;
    for (int n = 0; n < 64; ++n) {
      const unsigned now = (unsigned)__builtin_amdgcn_s_memrealtime() << 12;
      if (!__any((int)(t - now) > 0)) break;
    }
    asm volatile("" ::: "memory");
  };
  auto make_trb = [&]() {
    const int rb = (lane >> 4) & 1, q = (lane >> 2) & 3, p = lane & 3;
    const int rp = 4 * rb + p, lam = (rp & 4) | ((rp & 1) << 1) | ((rp >> 1) & 1);
    const int pc = ((odd ? 0x01327654 : 0x76543210) >> (4 * lam)) & 7;
    return lds + (8 * hh + q) * 64 + pc * 8;
  };
  auto store_x0 = [&](u32x4 f0, unsigned base) { int ln = lane; PIN(ln); st128<SC1>(f0, rWS, ln * 16, base + XOFF + (3 * pair + 2 * kh) * 1024); };
  auto store_x1 = [&](u32x4 f1, unsigned base) {
    int ln = lane; PIN(ln);
    st128<SC1>(f1, rWS, ln * 16 + __umul24(ln >> 5, 24576 - 512 - 1024 - pair * 2048), base + XOFF + (3 * pair + 1) * 1024 + kh * 512);
  };
  auto store_o = [&](u32x4 f0, u32x4 f1, unsigned base) {
    int ln = lane; PIN(ln);
    st128<SC1>(f0, rWS, ln * 16, base + TOFF + (3 * pair + 2 * odd) * 1024);
    st128<SC1>(f1, rWS, ln * 16 + (ln >> 5) * OOB, base + TOFF + (3 * pair + 1) * 1024 + odd * 512);
    __builtin_amdgcn_raw_buffer_store_b64(lo2(f1), rWS, ln * 8 + (1 - (ln >> 5)) * OOB, base + TOFF + 24576 - 256 + h * 256, SC1);
  };
  const int ht = tid & 255, sub = lane & 3;
  const int col = h * 56 - odd * 8;
  int krow[7];
  auto mkrow = [&](int i) {
    const int s = 4 * i + w;
    const int p = (s * 43) >> 7, m = s - 3 * p;
    const int c = i < 6 ? 7 * p + ((0x520 >> (4 * m)) & 15) + hh * (1 + (m & 1)) : 7 * (2 * w + hh) + 3;
    krow[i] = 8 * c + ((lane >> 2) & 7);
  };
  auto ldw = [&](rsrc_t rs, int i, int stride, int off) { int k = krow[i]; PIN(k); const u32x4 v = __builtin_amdgcn_raw_buffer_load_b128(rs, k * stride + sub * 16, col + off, 0); SB(); return v; };
  auto write_slab = [&](const u32x4* p, unsigned off) {
    lptr d = lds + off + ht * 16; PIN(d);
#pragma unroll
    for (int i = 0; i < 7; ++i) *(LAS u32x4*)(d + i * 4096) = p[i];
  };
  auto put_slab = [&](u32x4* p, int type, int part) {
    const unsigned keep01 = (odd && sub == 0) ? 0u : ~0u, keep23 = (!odd && sub == 3) ? 0u : ~0u;
#pragma unroll
    for (int i = 0; i < 7; ++i) {
      p[i][0] &= keep01; p[i][1] &= keep01; p[i][2] &= keep23; p[i][3] &= keep23;
      if (type == 0 && part == 0) { p[i][0] |= ~keep01 & 0x3f80u; p[i][2] |= ~keep23 & 0x3f80u; }
    }
    write_slab(p, part * SLAB);
  };
  auto sum_slab = [&](int type, int part, lptr trb) {
    lptr wb = trb + part * SLAB + w * 1024; PIN(wb);
    u32x4 wr[7];
#pragma unroll
    for (int i = 0; i < 7; ++i) wr[i] = trfrag(wb + i * 4096);
    f32x16 a = Z16;
#pragma unroll
    for (int i = 0; i < 7; ++i) a = mfma((u32x4){ONES, ONES, ONES, ONES}, wr[i], a);
    unsigned hi, lo; split2(a[0], hi, lo);
    if (hh == (w >> 1)) *(LAS u32x2*)(lds + L_CS + type * 3072 + part * 1024 + lane * 16 + (w & 1) * 8) = (u32x2){hi | (lo << 16), hi};
  };
  auto first_slab = [&](u32x4* p, int type, int part, lptr trb) { put_slab(p, type, part); sum_slab(type, part, trb); };
  const rsrc_t rWA = __builtin_amdgcn_make_buffer_rsrc((void*)wqkvA, 0, DM * 3 * DM * 2, 0x00020000);
  auto dma_x0 = [&](int i0, int i1) __attribute__((always_inline)) {
    const rsrc_t rX0 = __builtin_amdgcn_make_buffer_rsrc((void*)x0, 0, SEQ * DM * 2, 0x00020000);
    const int o0 = (lane >> 3) * 896 + (((lane & 7) ^ (lane >> 4)) * 16), o1 = o0 ^ 64;
#pragma unroll
    for (int i = i0; i < i1; ++i) {
      __builtin_amdgcn_raw_ptr_buffer_load_lds(rX0, (LAS void*)(lds + L_X0 + w * SLAB + i * 1024), 16, (i & 1) ? o1 : o0, (T * 32 + (i & 3) * 8) * 896 + (i >> 2) * 128, 0, 0);
      SB();
    }
  };

  if (wave < 4) {
    RSTAMP(62); STAMP(0);
    u32x4 xf[28];
    f32x16 xs, ak, av;
    float rstd;
    u32x4 mf;
    auto stats = [&](float s1, float s2) {
      const float mean = s1 * (1.0f / DM);
      rstd = __builtin_amdgcn_rsqf(fmaxf(s2 * (1.0f / DM) - mean * mean, 0.f) + 1e-5f);
      unsigned hi, lo; split2(-mean, hi, lo);
      mf = (u32x4){hi | (hi << 16), lo, hi | (hi << 16), lo};
      PIN(rstd); PIN(mf);
    };
    lptr trb;
    u32x4 id0, id1;
    float wtail;
    unsigned lomask, phoff;
    {
      __builtin_amdgcn_s_setprio(3);
#pragma unroll
      for (int i = 0; i < 7; ++i) { mkrow(i); xf[21 + i] = ldw(rWA, i, 2688, 896); }
      STAMPX(54);
      trb = make_trb();
      id0 = ident_frag(0, r, hh); id1 = ident_frag(1, r, hh);
      wtail = hh ? 0.f : 1.f; lomask = hh ? 0u : ~0u;
      PIN(trb); PIN(id0); PIN(id1); PIN(wtail); PIN(lomask);
      if (w >= 2) {
        u32x4 mk;
#pragma unroll
        for (int i = 0; i < 4; ++i) { const int k0 = ((2 * i) & 3) + 8 * (i >> 1) + 4 * hh + 16 * (w & 1); mk[i] = (k0 > r ? 0xffffu : 0u) | (k0 + 1 > r ? 0xffff0000u : 0u); }
        *(LAS u32x4*)(lds + L_MK + (w & 1) * 1024 + lane * 16) = mk;
      }
      if (w == 0) { cnt[0] = 0u; cnt[1] = 0u; cnt[2] = 0u; cnt[3] = 0u; cnt[4] = 0u; cnt[5] = 0u; cnt[6] = 0u; }
      cnt[8 + w] = 0u;
      const int lam = 4 * (w >> 1) + 2 * hh + (w & 1);
      const int pc = ((odd ? 0x01327654 : 0x76543210) >> (4 * lam)) & 7;
      unsigned off = lam == 7 ? OOB : ((h * 28 + 4 * pc - 4 * odd) * 28 + (r < 28 ? r : r - 8)) * 2;
      phoff = off; PIN(phoff);
      __builtin_amdgcn_s_setprio(0);
    }
    tag = (u32x2){0u, 0u}; epoch = 0;
    asm volatile("" : "=v"(ak), "=v"(av), "=v"(rstd), "=v"(mf), "=v"(xs));
#pragma unroll
    for (int i = 0; i < 21; ++i) asm volatile("" : "=v"(xf[i]));
    unsigned sent = SENT;
    auto release = [&](int hop) { if (w == 0) poll(hop, 15); bar(); };
    for (int layer = 0; layer < NL; ++layer) {
      const int type = layer & 1;
      const int pb = 2 + layer * 14;
      if (layer > 0) {
        unsigned base = WS_XO + (2 * layer - 1) * XO_B + TOFF;
        int v16 = lane * 16, vs = v16 + (v16 & 512);
        lptr bk = trb + L_WK;
        PIN(vs);
        release(3 * layer - 1);
        if (layer == 3) probe = (unsigned)(__builtin_amdgcn_s_memrealtime() - tstart) - 600;
        for (;;) {
          PIN(v16); PIN(vs); PIN(bk); asm volatile("" : "+s"(base));
          u32x4 sp[8];
          auto ld = [&](int s) { xf[4 + s] = __builtin_amdgcn_raw_buffer_load_b128(rWS, v16 + (s & 3) * 1024, base + (s >> 2) * 4096, SC1); SB(); };
          unsigned m = 1;
          ak = Z16; av = Z16;
          float s1 = 0.f, s2 = 0.f, mean;
          unsigned hi;
          u32x4 ck, cv;
          gemm2(ak, av, bk, xf,
                    [&](auto gap) {
#pragma unroll
                      for (int u = 0; u < 8; ++u) { sp[u] = __builtin_amdgcn_raw_buffer_load_b128(rWS, vs + ((u >> 1) & 1) * 2048 + (u & 1) * 512, base + 24576 + (u >> 2) * 4096, SC1); SB(); gap(u); if (u < 4) PACE2(); else PACED(); }
#pragma unroll
                      for (int s = 0; s < LA - 8; ++s) { ld(s); PACED(); }
                    },
                    [&](int i) {
                      if (i + LA - 8 < 24) ld(i + LA - 8);
                      if (i < 4) { PIN(sp[2 * i]); PIN(sp[2 * i + 1]); xf[i] = cat(lo2(sp[2 * i]), lo2(sp[2 * i + 1])); }
                      if (i == 22) { av = mfma(cv, mf, av); SB(); }
                      if (i == 24) { ak = mfma(ck, mf, ak); SB(); }
                    },
                    [&](int i, int hf) {
                      if (i < 4) m = max4(m, sp[2 * i + hf]); else m = umax3(m, xf[i][2 * hf], xf[i][2 * hf + 1]);
                      PIN(m);
                      if (i < 8) { if (hf) { s2 += U2F(sp[i][3]); PIN(s2); } else { s1 += U2F(sp[i][2]); PIN(s1); } }
                      if (hf) return;
                      if (i == 13) asm volatile("ds_write_b32 %0, %1" :: "v"(lds + L_SYNC + 32 + 4 * w), "v"(layer) : "memory");
                      if (i == 9) { auto t = __builtin_amdgcn_permlane32_swap(F2U(s1), F2U(s2), false, false); s1 = U2F(t[0]) + U2F(t[1]); PIN(s1); }
                      if (i == 10) { auto t = __builtin_amdgcn_permlane32_swap(F2U(s1), F2U(s1), false, false); s1 = U2F(t[0]); s2 = U2F(t[1]); PIN(s1); PIN(s2); }
                      if (i == 14) { mean = s1 * (-1.0f / DM); s2 = s2 * (1.0f / DM); PIN(mean); PIN(s2); }
                      if (i == 15) { s2 = fmaxf(fmaf(-mean, mean, s2), 0.f); PIN(s2); }
                      if (i == 16) { rstd = __builtin_amdgcn_rsqf(s2 + 1e-5f); PIN(rstd); }
                      if (i == 17) { hi = pk2(mean, mean); s1 = mean - bfhi(hi); PIN(hi); PIN(s1); }
                      if (i == 18) { const unsigned lo = pk2(s1, 0.f); mf = (u32x4){hi, lo, hi, lo}; PIN(mf); }
                      if (i == 19) { const lptr cs = lds + L_CS + type * 3072 + v16; ck = ldl(cs); cv = ldl(cs + 1024); }
                    }, EARLYAB);
          if (__builtin_expect(!__any(m == sent), 1)) break;
          if (--budget == 0) sent = 0u;
          RET(layer * 3);
          __builtin_amdgcn_s_sleep(BACK);
          asm volatile("" ::: "memory");
        }
      }
      int first = layer; asm volatile("" : "+s"(first));
      if (first == 0) {
        int ln = lane; PIN(ln);
        const int hh = ln >> 5;
        const unsigned ra = (ln & 31) * 128 + ((ln >> 1) & 7) * 16;
        const lptr stg = lds + L_X0 + w * SLAB;
        auto kof = [](int c) { return (c >> 3) * 4096 + (c & 7) * 16; };
        lptr ad[28];
#pragma unroll
        for (int i = 0; i < 28; ++i) {
          const int k0 = kof(chunk_of(ord(i), 0)), k1 = kof(chunk_of(ord(i), 1));
          ad[i] = stg + (ra ^ k0 ^ (~lomask & (k0 ^ k1))); PIN(ad[i]);
        }
        lptr pab = lds + L_X0 + tx * SLAB + (ra ^ kof(7 * pair + 5 * kh + hh)), pc = lds + L_X0 + tx * SLAB + (ra ^ kof(7 * pair + (hh ? 3 : 2 + 2 * kh))); PIN(pab); PIN(pc);
        SB();
        first_slab(xf + 21, 0, 0, trb);
        STAMPX(53);
        bar();
        STAMPX(55);
#pragma unroll
        for (int i = 0; i < 28; ++i) xf[i] = ldl(ad[i]);
        u32x4 xa = ldl(pab), xc = ldl(pc);
        arrive(5);
        SB();
        float s2a = 0.f, s2b = 0.f;
#pragma unroll
        for (int i = 0; i < 28; ++i) {
          const u32x4 u = xf[i]; s2a = dot2(u[1], u[1], dot2(u[0], u[0], s2a)); s2b = dot2(u[3], u[3], dot2(u[2], u[2], s2b));
          PIN(s2a); PIN(s2b);
          SB();
        }
        const rsrc_t rPH = __builtin_amdgcn_make_buffer_rsrc((void*)wphi, 0, 16 * 28 * 28 * 2, 0x00020000);
        unsigned e[4];
#pragma unroll
        for (int j = 0; j < 4; ++j) e[j] = __builtin_amdgcn_raw_buffer_load_b16(rPH, phoff + j * 56, 0, 0);
        load_epoch();
        STAMP(1);
        u32x4 cc = ldl(lds + L_CS + ln * 16);
        ak = Z16;
        lptr bk = trb + L_WK; PIN(bk);
        gemm1<5>(ak, bk, xf, [](int) {}, [&](int i) {
          if (i == 4) {
            const unsigned sel = hh && kh ? 0u : ~0u;
            xc[0] = (xc[0] & sel) | (xc[2] & ~sel); xc[1] = (xc[1] & sel) | (xc[3] & ~sel); PIN(xc);
          }
          if (i >= 16 && i < 24) { const unsigned u = i < 20 ? xa[i & 3] : xc[i & 3]; xs[2 * (i - 16)] = bflo(u); xs[2 * (i - 16) + 1] = bfhi(u); PIN(xs); }
        }, NOAB);
        asm volatile("" :: "v"(ak));
        *(LAS u32x2*)(lds + L_WPH + (w >> 1) * 1024 + ln * 16 + (w & 1) * 8) = (u32x2){e[0] | (e[1] << 16), e[2] | (e[3] << 16)};
        make_tag();
        STAMPX(57);
        bar();
        STAMP(2);
        const u32x4 cv = ldl(lds + L_CS + 1024 + ln * 16);
        av = Z16;
        gemm1<5>(av, bk + SLAB, xf, [&](int i) { if (i == 8) { ak = mfma(cc, mf, ak); SB(); } if (i == 10) { av = mfma(cv, mf, av); SB(); } }, [&](int i) {
          if (i == 2) {
            float s1 = U2F(F2U(ak[12]) & ~lomask), s2 = s2a + s2b;
            red2<false>(s1, s2);
            stats(s1, s2);
          }
        }, NOAB);
      }
      asm volatile("" ::: "memory");
      arrive(0);
      STAMPX(pb + 1);
      const unsigned mid = WS_MID + (h * 4 + layer) * MID_B;
      int ln16 = lane * 16; PIN(ln16);
      u32x4 wph[2], kf0, kf1;
      if (type == 0) {
        wph[0] = ldl(lds + L_WPH + ln16); wph[1] = ldl(lds + L_WPH + 1024 + ln16);
        DUMP(0, ak, 1, rstd); DUMP(0, av, 2, rstd);
        kf0 = pack8<0>(ak); kf1 = pack8<1>(ak);
        f32x16 p = mfma(wph[0], kf0, Z16);
        p = mfma(wph[1], kf1, p);
        SB();
#pragma unroll
        for (int i = 0; i < 16; ++i) { float t = av[i] * rstd; PIN(t); av[i] = t; }
        u32x4 vb0 = pack8<0>(av), vb1 = pack8<1>(av);
        vb1[2] |= ~lomask & 0x3f80u;
        PIN(vb0); PIN(vb1);
        SB();
        u32x4 fp[2], fm[2];
        dual_softmax(p, rstd * L2E, wtail, fp, fm);
        fp[1][2] &= lomask; fp[1][3] &= lomask; fm[1][2] &= lomask; fm[1][3] &= lomask;
        PIN(fp[0]); PIN(fp[1]); PIN(fm[0]); PIN(fm[1]);
        gather_all(layer, false);
        const int rr = (ln16 >> 4) & 31, pc = (ln16 >> 9) ^ ((rr >> 1) & 3);
        const lptr d0 = lds + L_SCR + w * 2048 + rr * 64 + pc * 16, d1 = lds + L_SCR + w * 2048 + rr * 64 + (pc ^ 2) * 16;
        *(LAS u32x4*)(d0) = fp[0]; *(LAS u32x4*)(d1) = fp[1];
        *(LAS u32x4*)(d0 + 8192) = fm[0]; *(LAS u32x4*)(d1 + 8192) = fm[1];
        *(LAS u32x4*)(d0 + 16384) = vb0; *(LAS u32x4*)(d1 + 16384) = vb1;
        bar();
      } else {
        av *= rstd;
        DUMP(0, av, 2, 1.f);
        u32x4 vb1 = pack8<1>(av);
        vb1[2] |= ~lomask & 0x3f80u;
        f32x16 vt = mfma(pack8<0>(av), id0, Z16);
        vt = mfma(vb1, id1, vt);
        SB();
#pragma unroll
        for (int i = 0; i < 16; ++i) { float t = ak[i] * rstd; PIN(t); ak[i] = t; }
        DUMP(0, ak, 1, 1.f);
        kf0 = pack8<0>(ak); kf1 = pack8<1>(ak);
        const unsigned g = mid + T * 4096;
        const lptr d = lds + L_SCR + (8 + w) * 4096 + ln16;
        st128<SC1>(kf0, rWS, ln16, g);
        const u32x4 vf0 = pack8<0>(vt), vf1 = pack8<1>(vt);
        st128<SC1>(vf0, rWS, ln16 + 2048, g);
        st128<SC1>(vf1, rWS, ln16 + 3072, g);
        {
          u32x4 kc = kf1;
          kc[3] |= ~lomask & (tag[0] + layer - xhalf_addu(add4(add4(0u, kf0), kf1) + add4(add4(0u, vf0), vf1)));
          st128<SC1>(kc, rWS, ln16 + 1024, g);
          CALS(3 * layer);
        }
        gather_all(layer, false);
        *(LAS u32x4*)(d) = kf0; *(LAS u32x4*)(d + 1024) = kf1; *(LAS u32x4*)(d + 2048) = vf0; *(LAS u32x4*)(d + 3072) = vf1;
      }
      STAMPX(pb + 2);
      u32x4 qf0, qf1;
      {
        f32x16 aq = mfma(ldl(lds + L_CS + type * 3072 + 2048 + ln16), mf, Z16);
        lptr bq = trb + L_WQ; PIN(bq);
        gemm1<5>(aq, bq, xf, [](int) {}, [](int) {}, NOAB);
        aq *= type ? rstd * (0.18898223650461361f * L2E) : 1.0f;
        DUMP(0, aq, 0, type ? 1.f : rstd);
        qf0 = pack8<0>(aq); qf1 = pack8<1>(aq);
        qf1[2] |= ~lomask & 0x3f80u;
      }
      f32x16 o;
      if (type == 0) {
        u32x4 fp[2], fm[2];
        {
          f32x16 p = mfma(wph[0], qf0, Z16);
          p = mfma(wph[1], qf1, p);
          dual_softmax(p, rstd * L2E, wtail, fp, fm);
        }
        PIN(fp[0]); PIN(fp[1]); PIN(fm[0]); PIN(fm[1]);
        STAMPX(pb + 3);
        bar();
        STAMP(pb + 4);
        const lptr kvf = lds + L_KVF + ln16;
        const u32x4 a0 = ldl(kvf), a1 = ldl(kvf + 1024), a2 = ldl(kvf + 2048), a3 = ldl(kvf + 3072);
        SB();
        o = mfma(a0, fp[0], Z16);
        o = mfma(a1, fp[1], o);
        o = mfma(a2, fm[0], o);
        o = mfma(a3, fm[1], o);
        o *= __builtin_amdgcn_rcpf(upper_val(o[12]) + 1e-6f);
      } else {
        lptr kv = lds + L_SCR + w * 4096 + ln16; PIN(kv);
        auto ex4 = [&](f32x16& s, int i0) {
#pragma unroll
          for (int i = i0; i < i0 + 4; ++i) s[i] = __builtin_amdgcn_exp2f(s[i]);
          unsigned a = pk2(s[i0], s[i0 + 1]), c = pk2(s[i0 + 2], s[i0 + 3]); PIN(a); PIN(c);
          SB();
          return (u32x2){a, c};
        };
        auto tile_max = [&](int j, int side, float m) {
          f32x16 t = mfma(ldl(kv + j * 4096 + 1024), qf1, mfma(ldl(kv + j * 4096), qf0, Z16));
          int lim = ((ln16 >> 4) & 31) - ((ln16 >> 7) & 4); PIN(lim);
#pragma unroll
          for (int i = 0; i < 16; ++i) { const bool up = (i & 3) + 8 * (i >> 2) > lim; m = fmaxf(m, (side == 0 || up == (side == 1)) ? t[i] : m); }
          return m;
        };
        float m = -__builtin_inff(), l;
        for (int pass = 0;; ++pass) {
          const u32x4 mk0 = ldl(lds + L_MK + ln16), mk1 = ldl(lds + L_MK + 1024 + ln16);
          f32x16 negm;
          {
            const u32x4 v0 = ldl(kv + 8 * 4096 + 2048), v1 = ldl(kv + 8 * 4096 + 3072);
            f32x16 s = mfma(ldl(kv + 8 * 4096 + 1024), qf1, mfma(ldl(kv + 8 * 4096), qf0, Z16));
#pragma unroll
            for (int i = 0; i < 16; ++i) {
              const unsigned k = (i < 8 ? mk0 : mk1)[(i >> 1) & 3], on = (i & 1) ? (unsigned)((int)k >> 31) : (unsigned)((int)(k << 16) >> 31);
              s[i] = U2F((F2U(s[i]) & ~on) | (on & 0xff800000u));
            }
#pragma unroll
            for (int i = 0; i < 16; i += 2) m = fmax3(m, s[i], s[i + 1]);
            m = xhalf_max(m);
            s -= m;
            negm = splat(-m);
            PIN(negm);
            const u32x2 a = ex4(s, 0), c = ex4(s, 4), e = ex4(s, 8), f = ex4(s, 12);
            o = mfma(v1, cat(e, f), mfma(v0, cat(a, c), Z16));
            PIN(o);
          }
          if (pass == 0) {
            STAMPX(pb + 3);
            bar();
            STAMP(pb + 4);
          }
          u32x4 kk[2][2], vv[2][2], pa, pb2;
          f32x16 st[2];
          kk[0][0] = ldl(kv); kk[0][1] = ldl(kv + 1024); kk[1][0] = ldl(kv + 4096); kk[1][1] = ldl(kv + 4096 + 1024);
          SB();
          st[0] = mfma(kk[0][1], qf1, mfma(kk[0][0], qf0, negm));
          SB();
#pragma unroll
          for (int j = 0; j < 8; ++j) {
            f32x16& s = st[j & 1];
            vv[j & 1][0] = ldl(kv + j * 4096 + 2048); vv[j & 1][1] = ldl(kv + j * 4096 + 3072);
            if (j < 6) { kk[j & 1][0] = ldl(kv + (j + 2) * 4096); kk[j & 1][1] = ldl(kv + (j + 2) * 4096 + 1024); }
            SB();
            if (j == 0) { st[1] = mfma(kk[1][0], qf0, negm); SB(); }
            const u32x2 a = ex4(s, 0);
            if (j == 0) st[1] = mfma(kk[1][1], qf1, st[1]); else o = mfma(vv[(j - 1) & 1][0], pa, o);
            SB();
            const u32x2 c = ex4(s, 4);
            if (j == 7) o = mfma(vv[0][1], pb2, o); else if (j > 0) st[(j + 1) & 1] = mfma(kk[(j + 1) & 1][0], qf0, negm);
            SB();
            const u32x2 e = ex4(s, 8);
            if (j == 7) o = mfma(vv[1][0], cat(a, c), o); else if (j > 0) st[(j + 1) & 1] = mfma(kk[(j + 1) & 1][1], qf1, st[(j + 1) & 1]);
            SB();
            const u32x2 f = ex4(s, 12);
            if (j == 7) o = mfma(vv[1][1], cat(e, f), o); else if (j > 0) o = mfma(vv[(j - 1) & 1][1], pb2, o);
            SB();
            pa = cat(a, c); pb2 = cat(e, f);
            if (j == 0) { pa &= mk0; pb2 &= mk1; PIN(pa); PIN(pb2); }
          }
          l = upper_val(o[12]);
          if (pass > 0 || __all(l < LMAX)) break;
          RET(13);
          m = tile_max(0, 1, m);
#pragma unroll 1
          for (int j = 1; j < 8; ++j) m = tile_max(j, 0, m);
        }
        arrive(4);
        o *= __builtin_amdgcn_rcpf(l);
      }
      DUMP(1, o, 0, 1.f);
      store_o(pack8<0>(o), pack8<1>(o), WS_XO + (2 * layer) * XO_B);
      CALS(3 * layer + 1);
      STAMPX(pb + 6);
      lptr bown, both;
      {
        int ln = lane; PIN(ln);
        const int q = (ln >> 2) & 3, rp = ((ln >> 2) & 4) + (ln & 3), lam = (rp & 4) | ((rp & 1) << 1) | ((rp >> 1) & 1);
        const int pe = lam, po = 6 + ((0x01327654 >> (4 * lam)) & 7);
        const lptr rb = lds + L_WO + kh * SLAB + (8 * (ln >> 5) + q) * 128;
        const lptr ae = rb + ((pe * 8) ^ ((q >> 1) * 64)), ao = rb + ((po * 8) ^ ((q >> 1) * 64));
        bown = kh ? ao : ae; both = kh ? ae : ao;
        PIN(bown); PIN(both);
      }
      unsigned base = WS_XO + (2 * layer) * XO_B + XOFF + kh * 12288;
      int v16 = ln16, vs = (v16 >> 1) + ((v16 >> 1) & 256);
      PIN(vs);
      release(3 * layer + 1);
      STAMP(pb + 7);
      f32x16 xn, xo;
      u32x4 wa[4];
        auto tr = [](lptr p) {
          bf16x4 a0 = __builtin_amdgcn_ds_read_tr16_b64_v4bf16((LAS bf16x4*)p), a1 = __builtin_amdgcn_ds_read_tr16_b64_v4bf16((LAS bf16x4*)(p + 512));
          return __builtin_bit_cast(u32x4, __builtin_shufflevector(a0, a1, 0, 1, 2, 3, 4, 5, 6, 7));
        };
      for (;;) {
        PIN(v16); PIN(vs); asm volatile("" : "+s"(base));
        u32x2 sp[4];
        u32x4 wb[4];
#pragma unroll
        for (int u = 0; u < 4; ++u) { sp[u] = __builtin_amdgcn_raw_buffer_load_b64(rWS, vs + (u >> 1) * 1024 + (u & 1) * 256, base + 24576 - kh * 10240, SC1); SB(); if (u & 1) PACED(); }
#pragma unroll
        for (int s = 0; s < LAO; ++s) { xf[2 + s] = __builtin_amdgcn_raw_buffer_load_b128(rWS, v16 + (s & 3) * 1024, base + (s >> 2) * 4096, SC1); SB(); if (s < 3) wb[s] = tr(both + s * 2048); else if (s < 6) wa[s - 3] = tr(bown + (s - 3) * 2048); SB(); if (s < 6) PACE2(); else PACED(); }
        xf[0] = cat(sp[0], sp[1]); xf[1] = cat(sp[2], sp[3]);
        xn = xs; xo = Z16;
        unsigned m = 1;
#pragma unroll
        for (int i = 0; i < 14; ++i) {
          if (i + LAO < 12) { const int s = i + LAO; xf[2 + s] = __builtin_amdgcn_raw_buffer_load_b128(rWS, v16 + (s & 3) * 1024, base + (s >> 2) * 4096, SC1); SB(); }
          if (i + 3 < 14) wb[(i + 3) & 3] = tr(both + (i + 3) * 2048);
          SB();
          xo = mfma(wb[i & 3], xf[i], xo);
          SB();
          m = max4(m, xf[i]); PIN(m);
          if (i >= LAG) {
            wa[(i - LAG + 3) & 3] = tr(bown + (i - LAG + 3) * 2048);
            SB();
            xn = mfma(wa[(i - LAG) & 3], xf[i - LAG], xn);
          }
          SB();
        }
        if (__builtin_expect(!__any(m == sent), 1)) break;
        if (--budget == 0) sent = 0u;
        RET(layer * 3 + 2);
        __builtin_amdgcn_s_sleep(BACK);
        asm volatile("" ::: "memory");
      }
      {
        const lptr ex = lds + L_WQ + ln16;
        f32x4 t[4];
#pragma unroll
        for (int i = 14 - LAG; i < 14; ++i) {
          if (i + 3 < 14) wa[(i + 3) & 3] = tr(bown + (i + 3) * 2048);
          SB();
          xn = mfma(wa[i & 3], xf[i], xn);
          SB();
          if (i == 15 - LAG) {
#pragma unroll
            for (int g = 0; g < 4; ++g) *(LAS f32x4*)(ex + w * 4096 + g * 1024) = (f32x4){xo[4 * g], xo[4 * g + 1], xo[4 * g + 2], xo[4 * g + 3]};
          }
          if (i == 11) {
            bar();
#pragma unroll
            for (int g = 0; g < 4; ++g) t[g] = *(LAS f32x4*)(ex + (w ^ 1) * 4096 + g * 1024);
          }
          SB();
        }
#pragma unroll
        for (int g = 0; g < 4; ++g) {
#pragma unroll
          for (int j = 0; j < 4; ++j) xs[4 * g + j] = xn[4 * g + j] + t[g][j];
        }
      }
      STAMPX(pb + 8);
      if (layer == NL - 1) {
        const rsrc_t rOUT = __builtin_amdgcn_make_buffer_rsrc((void*)out, 0, SEQ * DM * 2, 0x00020000);
        const u32x4 f0 = pack8<0>(xs), f1 = pack8<1>(xs);
        int ln = lane; PIN(ln);
        const int row = (ln & 31) * 896, up = ln >> 5;
        const int ob = (tb * 4 + tx) * 32 * 896 + 7 * pair * 16;
        st128<0>(f0, rOUT, row + up * 16, ob + 5 * kh * 16);
        st128<0>(f1, rOUT, row + up * OOB, ob + (2 + 2 * kh) * 16);
        __builtin_amdgcn_raw_buffer_store_b64(lo2(f1), rOUT, row + (1 - up) * OOB, ob + 3 * 16 + kh * 8, 0);
      } else {
        const u32x4 f0 = pack8<0>(xs);
        store_x0(f0, WS_XO + (2 * layer + 1) * XO_B);
        u32x4 f1 = pack8<1>(xs);
        f1[2] &= lomask; f1[3] &= lomask;
        float s1 = 0.f, s2 = 0.f, t1 = 0.f, t2 = 0.f;
#pragma unroll
        for (int j = 0; j < 4; ++j) { const unsigned u = f0[j], v = f1[j]; s1 = dot2(u, ONES, s1); s2 = dot2(u, u, s2); t1 = dot2(v, ONES, t1); t2 = dot2(v, v, t2); }
        s1 += t1; s2 += t2;
        red2<false>(s1, s2);
        f1[2] |= ~lomask & F2U(s1); f1[3] |= ~lomask & F2U(s2);
        store_x1(f1, WS_XO + (2 * layer + 1) * XO_B);
        CALS(3 * layer + 2);
        STAMPX(pb + 9);
      }
      asm volatile("" : "=v"(ak), "=v"(av), "=v"(rstd), "=v"(mf), "=v"(phoff));
#pragma unroll
      for (int i = 14; i < 28; ++i) asm volatile("" : "=v"(xf[i]));
    }
    STAMP(61); RSTAMP(63);
    if (b == 77 && w == 0) while (__builtin_amdgcn_s_memrealtime() - tstart < 6000 + probe) __builtin_amdgcn_s_sleep(2);
    if (b == 0 && w == 0) __builtin_amdgcn_raw_buffer_store_b64((u32x2){(unsigned)(epoch + 1), (unsigned)((epoch + 1) >> 32)}, rWS, WS_EPOCH, 0, SC1);
  } else {
    STAMPX(53);
    u32x4 pA[21], pB[21];
    unsigned tch0, tch1;
    asm volatile("s_load_dword %0, %2, 0x0\n\ts_load_dword %1, %3, 0x0" : "=&s"(tch0), "=&s"(tch1)
                 : "s"(w == 0 ? (const void*)ws : w == 1 ? (const void*)wqkvB : w == 2 ? (const void*)woA : (const void*)wphi), "s"(w < 2 ? (const void*)woB : (const void*)out));
    dma_x0(0, 4);
    unsigned warm0;
    {
      u64 pc; asm volatile("s_getpc_b64 %0" : "=s"(pc));
      const rsrc_t rs = __builtin_amdgcn_make_buffer_rsrc(w == 3 ? (void*)(pc & ~127ull) : (void*)wqkvA, 0, w == 3 ? CODE_B : DM * 3 * DM * 2, 0x00020000);
      const int n = w == 3 ? 9 : 13;
      warm0 = __builtin_amdgcn_raw_buffer_load_b32(rs, lane < n ? (lane + (w == 3 ? (b >> 3) : b * 3 + w) * n) * 128 : OOB, 0, 0);
      SB();
    }
#pragma unroll
    for (int i = 0; i < 7; ++i) { mkrow(i); PIN(krow[i]); }
    lptr trb = make_trb(); PIN(trb);
    u32x4 id0 = ident_frag(0, r, hh); PIN(id0);
    SB();
    dma_x0(4, 28);
#pragma unroll
    for (int i = 0; i < NV0; ++i) pA[14 + i] = ldw(rWA, i, 2688, 1792);
    STAMPX(54);
    asm volatile("s_waitcnt vmcnt(%2) lgkmcnt(0)" :: "s"(tch0), "s"(tch1), "n"(NV0) : "memory");
    const u32x4 sv = {SENT, SENT, SENT, SENT};
    auto clean_kv = [&](int layer) { if (layer < NL) st128<SC1>(sv, rWS, lane * 16, WS_MID + (h * 4 + layer) * MID_B + (tb * 4 + w) * 1024); };
    auto clean_o = [&](int layer) { if (layer < NL) store_o(sv, sv, WS_XO + (2 * layer) * XO_B); };
    auto clean_x = [&](int layer) { if (layer < NL - 1) { store_x0(sv, WS_XO + (2 * layer + 1) * XO_B); store_x1(sv, WS_XO + (2 * layer + 1) * XO_B); } };
    unsigned r0 = (unsigned)__builtin_amdgcn_s_memrealtime(), c0 = (unsigned)__builtin_amdgcn_s_memtime(), tann = 0;
    auto mark = [&](int cyc) {
      const unsigned r1 = (unsigned)__builtin_amdgcn_s_memrealtime(), c1 = (unsigned)__builtin_amdgcn_s_memtime();
      tann = r1 + LAND + (unsigned)((float)cyc * (float)(r1 - r0) * __builtin_amdgcn_rcpf((float)(c1 - c0)));
      r0 = r1; c0 = c1;
    };
    auto push = [&](int hop, unsigned voff) {
      CALA(hop);
      __builtin_amdgcn_raw_buffer_store_b64((u32x2){tag[0], (tag[1] & 0xfff00000u) | (tann & 0xfffffu)}, rWS, voff, WS_FLAGS + hop * HOP_B, SC1);
    };
    auto push_grp = [&](int hop) { push(hop, lane < 16 ? (tb * 16 + lane) * 128 + h * 8 : OOB); };
    auto push_mid = [&](int layer) {
      if (layer & 1) push(3 * layer, (lane < 2 && tb + 1 + lane < 16) ? ((tb + 1 + lane) * 16 + h) * 128 + lane * 8 : OOB);
      else push(3 * layer, lane < 16 ? (lane * 16 + h) * 128 + tb * 8 : OOB);
    };
    const rsrc_t rWB = __builtin_amdgcn_make_buffer_rsrc((void*)wqkvB, 0, DM * 3 * DM * 2, 0x00020000);
    const rsrc_t rOA = __builtin_amdgcn_make_buffer_rsrc((void*)woA, 0, DM * DM * 2, 0x00020000);
    const rsrc_t rOB = __builtin_amdgcn_make_buffer_rsrc((void*)woB, 0, DM * DM * 2, 0x00020000);
    auto spin = [&](int k, unsigned n) { while (__builtin_amdgcn_readfirstlane(cnt[k]) < n) __builtin_amdgcn_s_sleep(1); asm volatile("" ::: "memory"); };
    auto wait_h = [&](int hop, int mask) {
      if (w == 0) { if (mask >= 0) poll(hop, mask); cnt[1] = hop + 1; }
      else while (__builtin_amdgcn_readfirstlane(cnt[1]) != hop + 1) { }
      asm volatile("" ::: "memory");
    };
    const unsigned pkb = WS_PK + h * 262144;
    auto ldp = [&](unsigned off) { const u32x4 v = __builtin_amdgcn_raw_buffer_load_b128(rWS, lane * 16, pkb + off + w * 1024, SC1); SB(); return v; };
    STAMPX(55);
    bar();
#pragma unroll
    for (int i = NV0; i < 7; ++i) pA[14 + i] = ldw(rWA, i, 2688, 1792);
    load_epoch();
    u32x4 pw[4];
    {
      const unsigned act = tb < 14 ? 0u : OOB;
#pragma unroll
      for (int j = 0; j < 2; ++j) {
        const int st = 2 * tb + j, p = (st * 43) >> 7, m = st - 3 * p;
        const int c = st < 24 ? 7 * p + ((0x520 >> (4 * m)) & 15) + hh * (1 + (m & 1)) : 7 * (2 * (st - 24) + hh) + 3;
        pw[j] = __builtin_amdgcn_raw_buffer_load_b128(rWB, ((8 * c + ((lane >> 2) & 7)) * 2688 + sub * 16) | (w < 3 ? act : OOB), col + (w == 2 ? 0 : 896 + 896 * w), 0);
        SB();
      }
      {
        const int n = 4 * tb + w, k2 = n >= 28, i = (n - 28 * k2) >> 1, st = i - 2, p = (st * 43) >> 7, m = st - 3 * p;
        const int c = 28 * k2 + (i < 2 ? 14 * i + 3 + 7 * (n & 1) : 7 * p + ((0x520 >> (4 * m)) & 15) + (n & 1) * (1 + (m & 1)));
        const unsigned vo = (c * 7168 + (lane >> 3) * 896 + 112 * pair + (((lane & 7) ^ ((lane >> 2) & 4)) * 16)) | act;
        pw[2] = __builtin_amdgcn_raw_buffer_load_b128(rOA, vo, 0, 0); SB();
        pw[3] = __builtin_amdgcn_raw_buffer_load_b128(rOB, vo, 0, 0); SB();
      }
    }
    __builtin_amdgcn_s_setprio(3);
    STAMPX(57);
    spin(5, 4);
    put_slab(pA + 14, 0, 1);
#pragma unroll
    for (int j = 0; j < NQ1; ++j) pA[j] = ldw(rWA, j, 2688, 0);
    sum_slab(0, 1, trb);
#pragma unroll
    for (int j = NQ1; j < NQ0; ++j) pA[j] = ldw(rWA, j, 2688, 0);
#pragma unroll
    for (int i = 0; i < 7; ++i) pA[7 + i] = ldl(lds + L_WK + i * 4096 + ht * 16);
    STAMPX(58);
    bar();
    if (w == 3) mark(TKA0);
    __builtin_amdgcn_s_setprio(0);
#pragma unroll
    for (int j = NQ0; j < 7; ++j) pA[j] = ldw(rWA, j, 2688, 0);
    {
      const unsigned keep01 = (odd && sub == 0) ? 0u : ~0u, keep23 = (!odd && sub == 3) ? 0u : ~0u, act = (tb < 14 ? 0u : OOB) | (lane * 16);
#pragma unroll
      for (int j = 0; j < 2; ++j) st128<SC1>(pw[j] & (u32x4){keep01, keep01, keep23, keep23}, rWS, w < 3 ? act : OOB, pkb + w * SLAB + (2 * tb + j) * 1024);
      st128<SC1>(pw[2], rWS, act, pkb + 3 * SLAB + (4 * tb + w) * 1024);
      st128<SC1>(pw[3], rWS, act, pkb + 5 * SLAB + (4 * tb + w) * 1024);
    }
    clean_kv(0); clean_o(0);
    make_tag();
    STAMPX(59);
    first_slab(pA, 0, 2, trb);
    drain();
    arrive(3);
    if (w == 3) { spin(3, 4); push_mid(0); }
    asm volatile("" :: "v"(warm0));
    __builtin_amdgcn_s_setprio(3);
    u32x4 po[14];
    auto swap_wo = [&](bool keep) {
      lptr d = lds + L_WO + ht * 16; PIN(d);
#pragma unroll
      for (int i = 0; i < 14; ++i) { u32x4 t; if (keep) t = ldl(d + i * 4096); *(LAS u32x4*)(d + i * 4096) = po[i]; if (keep) po[i] = t; SB(); }
    };
    STAMPX(60);
#pragma unroll
    for (int layer = 0; layer < NL; ++layer) {
      const int type = layer & 1;
      const int pb = 2 + layer * 14;
      const unsigned mid = WS_MID + (h * 4 + layer) * MID_B;
      if (type == 0) {
        lptr ta0, ta1;
        {
          const int rb = (lane >> 4) & 1, q = (lane >> 2) & 3, p = lane & 3;
          const int rp = 4 * rb + p, lam = (rp & 4) | ((rp & 1) << 1) | ((rp >> 1) & 1);
          ta0 = lds + L_SCR + (8 * hh + q) * 64 + (((lam >> 1) ^ (q >> 1)) * 16) + (lam & 1) * 8;
          ta1 = lds + L_SCR + (8 * hh + q + 4) * 64 + (((lam >> 1) ^ (q >> 1) ^ 2) * 16) + (lam & 1) * 8;
          PIN(ta0); PIN(ta1);
        }
        bar();
        STAMPX(pb + 0);
        {
          auto tr2 = [&](unsigned off) {
            bf16x4 a0 = __builtin_amdgcn_ds_read_tr16_b64_v4bf16((LAS bf16x4*)(ta0 + off));
            bf16x4 a1 = __builtin_amdgcn_ds_read_tr16_b64_v4bf16((LAS bf16x4*)(ta1 + off));
            return __builtin_bit_cast(u32x4, __builtin_shufflevector(a0, a1, 0, 1, 2, 3, 4, 5, 6, 7));
          };
          const unsigned so = (w >> 1) * 8192;
          u32x4 fa[4], fb[4];
          f32x16 kv = Z16;
#pragma unroll
          for (int i = 0; i < 3; ++i) { fa[i] = tr2(so + i * 1024); fb[i] = tr2(16384 + i * 1024); }
          SB();
#pragma unroll
          for (int i = 0; i < 8; ++i) {
            if (i + 3 < 8) { fa[(i + 3) & 3] = tr2(so + (i + 3) * 1024); fb[(i + 3) & 3] = tr2(16384 + (i + 3) * 1024); }
            SB();
            kv = mfma(fa[i & 3], fb[i & 3], kv);
            SB();
          }
          st128<SC1>((w & 1) ? pack8<1>(kv) : pack8<0>(kv), rWS, lane * 16, mid + (tb * 4 + w) * 1024);
          CALS(3 * layer);
        }
        STAMPX(pb + 1);
        if (layer == 0) clean_x(0);
        wait_h(3 * layer, 15);
        STAMPX(pb + 2);
        f32x16 sum;
        for (;;) {
          sum = Z16;
          unsigned m = 0;
          u32x4 pr[16];
          const unsigned g = mid + w * 1024;
#pragma unroll
          for (int i = 0; i < 16; ++i) {
            pr[i] = __builtin_amdgcn_raw_buffer_load_b128(rWS, lane * 16, g + i * 4096, SC1); SB();
            if (i < 15) PACED();
          }
          if (w == 3) { mark(layer ? TOA : TOA0); push_grp(3 * layer + 1); SB(); }
#pragma unroll
          for (int i = 0; i < 16; ++i) {
            if (layer == 0 && i < NPO) po[i] = ldp(3 * SLAB + i * 4096);
            sum = mfma(id0, pr[i], sum); m = max4(m, pr[i]); PIN(m); SB();
          }
          if (!__any(m == SENT) || budget == 0) break;
          --budget; RET(layer * 3 + 1);
          asm volatile("" ::: "memory");
        }
        *(LAS u32x4*)(lds + L_KVF + w * 1024 + lane * 16) = pack8<0>(sum);
        STAMPX(pb + 3);
        bar();
        if (layer == 0) {
#pragma unroll
          for (int i = NPO; i < 14; ++i) po[i] = ldp(3 * SLAB + i * 4096);
        } else __builtin_amdgcn_s_sleep(5);
      } else {
        gather_all(layer, true);
        STAMPX(pb + 0);
        u32x4 t[8];
        wait_h(3 * layer, tb >= 2 ? 1 : tb - 1);
        STAMPX(pb + 2);
        if (tb - 2 + (w >> 1) >= 0) {
          const unsigned g = mid + (tb * 4 - 8 + 2 * w) * 4096;
          for (;;) {
            unsigned c[2] = {0u, 0u};
#pragma unroll
            for (int i = 0; i < 8; ++i) { t[i] = __builtin_amdgcn_raw_buffer_load_b128(rWS, lane * 16 + (i & 3) * 1024, g + (i >> 2) * 4096, SC1); SB(); if (i < 7) PACED(); }
#pragma unroll
            for (int i = 0; i < 8; ++i) {
              c[i >> 2] = add4(c[i >> 2], t[i]); PIN(c[i >> 2]);
              if ((i & 3) == 1) t[i][3] &= (unsigned)hh - 1u;
              *(LAS u32x4*)(lds + L_SCR + 2 * w * 4096 + i * 1024 + lane * 16) = t[i];
              SB();
            }
            const auto cs = __builtin_amdgcn_permlane32_swap(c[0], c[1], false, false);
            if (__all(cs[0] + cs[1] == tag[0] + layer) || budget == 0) break;
            --budget; RET(layer * 3 + 1);
            __builtin_amdgcn_s_sleep(8);
            asm volatile("" ::: "memory");
          }
        } else {
#pragma unroll
          for (int i = 0; i < 8; ++i)
            *(LAS u32x4*)(lds + L_SCR + 2 * w * 4096 + i * 1024 + lane * 16) = (u32x4){0u, 0u, ((i & 3) == 1 && hh) ? 0xff80u : 0u, 0u};
        }
        drain();
        STAMPX(pb + 3);
        bar();
        if (w == 3) { mark(layer == 1 ? TOB1 : TOB); push_grp(3 * layer + 1); }
        __builtin_amdgcn_s_setprio(0);
        if (layer == 1) {
          clean_x(1); clean_kv(2);
#pragma unroll
          for (int i = 0; i < 14; ++i) po[i] = ldp(5 * SLAB + i * 4096);
          swap_wo(true);
          drain();
          spin(4, 4);
        }
      }
      __builtin_amdgcn_s_setprio(0);
      if (layer == 0) {
        swap_wo(false);
      } else if (layer < NL - 1) {
        write_slab((type == 0 ? pB : pA) + 7, L_WK);
      }
      STAMPX(pb + 4);
      bar();
      if (w == 3 && layer < NL - 1) { mark(layer == 2 ? TX2 : layer ? TX : TX0); push_grp(3 * layer + 2); }
      STAMPX(pb + 5);
      if (layer > 0 && layer < NL - 1) { __builtin_amdgcn_s_sleep(WSL); write_slab((type == 0 ? pB : pA) + 14, L_WV); }
      if (layer == 0) {
        __builtin_amdgcn_s_sleep(12);
#pragma unroll
        for (int i = 0; i < 21; ++i) pB[(i + 7) % 21] = ldp(i * 4096);
      }
      bar();
      if (layer < NL - 1) {
        __builtin_amdgcn_s_sleep(SWS);
        if (layer == 0) { write_slab(pB + 7, L_WK); write_slab(pB + 14, L_WV); sum_slab(1, 0, trb); sum_slab(1, 1, trb); }
        else swap_wo(layer == 1);
        bar();
        __builtin_amdgcn_s_sleep(WSL);
        write_slab(type == 0 ? pB : pA, L_WQ);
        if (layer == 0) sum_slab(1, 2, trb);
        arrive(0);
        while (!__all(cnt[8 + (lane & 3)] == layer + 1u)) __builtin_amdgcn_s_sleep(1);
        if (w == 3) { mark(layer == 0 ? TKB1 : layer == 1 ? TKA : TKB); push_mid(layer + 1); }
        __builtin_amdgcn_s_sleep(CSL);
        clean_o(layer + 1);
        if (layer == 1) { clean_x(2); drain(); }
        __builtin_amdgcn_s_setprio(3);
      }
    }
  }
}
}

extern "C" void kernel_launch(void* const* d_in, const int* in_sizes, int n_in, void* d_out, int out_size, void* d_ws, size_t ws_size, hipStream_t stream) {
  (void)hipFuncSetAttribute((const void*)fused_kernel, hipFuncAttributeMaxDynamicSharedMemorySize, L_END);
  fused_kernel<<<256, 512, L_END, stream>>>((const u16*)d_in[0], (const u16*)d_in[1], (const u16*)d_in[2], (const u16*)d_in[3],
                                            (const u16*)d_in[5], (const u16*)d_in[6], (u16*)d_out, (unsigned char*)d_ws);
}
```

```cpp
#define NL 4
#include <hip/hip_runtime.h>
#include <hip/hip_bf16.h>

typedef unsigned short u16;
typedef unsigned long long u64;
typedef __bf16 bf16x8 __attribute__((ext_vector_type(8)));
typedef __bf16 bf16x4 __attribute__((ext_vector_type(4)));
typedef __bf16 bf16x2 __attribute__((ext_vector_type(2)));
typedef float f32x16 __attribute__((ext_vector_type(16)));
typedef float f32x4 __attribute__((ext_vector_type(4)));
typedef float f32x2 __attribute__((ext_vector_type(2)));
typedef unsigned u32x4 __attribute__((ext_vector_type(4)));
typedef unsigned u32x2 __attribute__((ext_vector_type(2)));
#define LAS __attribute__((address_space(3)))
#define GAS __attribute__((address_space(1)))
#define DI __device__ __forceinline__
#define SB() __builtin_amdgcn_sched_barrier(0)
#define PIN(x) asm volatile("" : "+v"(x))
typedef __amdgpu_buffer_rsrc_t rsrc_t;
typedef LAS unsigned char* lptr;

namespace {
constexpr int SEQ = 2048, DM = 448;
constexpr int SC1 = 16;
constexpr unsigned WS_EPOCH = 0;
constexpr unsigned WS_FLAGS = 4096;
constexpr unsigned HOP_B = 256 * 128;
constexpr unsigned TILE_B = 32768;
constexpr unsigned XO_B = 4 * TILE_B;
constexpr unsigned WS_XO = 1u << 20;
constexpr unsigned MID_B = 64 * 4096;
constexpr unsigned WS_MID = WS_XO + 16 * 8 * XO_B;
constexpr unsigned WS_PK = WS_MID + 64 * MID_B;
constexpr unsigned WS_PROF = WS_PK + 16 * 262144;
constexpr unsigned WS_END = WS_PROF + 256 * 2048;
constexpr unsigned SLAB = 448 * 64;
constexpr unsigned L_WK = 0, L_WV = SLAB, L_WQ = 2 * SLAB, L_WO = 3 * SLAB;
constexpr unsigned L_SCR = 0;
constexpr unsigned L_X0 = SLAB;
constexpr unsigned L_KVF = 5 * SLAB;
constexpr unsigned L_MK = L_KVF + 4096;
constexpr unsigned L_WPH = L_MK + 2048;
constexpr unsigned L_CS = L_WPH + 2048;
constexpr unsigned L_SYNC = L_CS + 6144;
constexpr unsigned L_PROF = L_SYNC + 64;
constexpr unsigned L_END = L_PROF + 2048;
static_assert(L_END <= 163840 && L_X0 + 4 * SLAB <= L_KVF, "lds");
constexpr unsigned SPIN_CAP = 1u << 15;
constexpr unsigned SENT = 0xffffffffu;
constexpr unsigned OOB = 0x7ff00000u;
constexpr unsigned ONES = 0x3f803f80u;
constexpr unsigned CODE_B = 36 * 1024;
constexpr float L2E = 1.4426950408889634f;

DI f32x16 mfma(u32x4 a, u32x4 b, f32x16 c) {
  return __builtin_amdgcn_mfma_f32_32x32x16_bf16(__builtin_bit_cast(bf16x8, a), __builtin_bit_cast(bf16x8, b), c, 0, 0, 0);
}
DI unsigned pk2(float a, float b) { f32x2 t = {a, b}; return __builtin_bit_cast(unsigned, __builtin_convertvector(t, bf16x2)); }
template <int S_> DI u32x4 pack8(const f32x16& c) {
  return (u32x4){pk2(c[8 * S_], c[8 * S_ + 1]), pk2(c[8 * S_ + 2], c[8 * S_ + 3]), pk2(c[8 * S_ + 4], c[8 * S_ + 5]), pk2(c[8 * S_ + 6], c[8 * S_ + 7])};
}
DI u32x4 cat(u32x2 a, u32x2 b) { return (u32x4){a[0], a[1], b[0], b[1]}; }
DI u32x2 lo2(u32x4 v) { return (u32x2){v[0], v[1]}; }
DI unsigned umax3(unsigned a, unsigned b, unsigned c) { return max(max(a, b), c); }
DI unsigned max4(unsigned m, u32x4 v) { return umax3(umax3(m, v[0], v[1]), v[2], v[3]); }
DI unsigned add4(unsigned m, u32x4 v) { return (m + v[0] + v[1]) + v[2] + v[3]; }
DI unsigned xhalf_addu(unsigned v) { auto r = __builtin_amdgcn_permlane32_swap(v, v, false, false); return r[0] + r[1]; }
DI float fmax3(float a, float b, float c) { return fmaxf(fmaxf(a, b), c); }
template <int CTRL> DI float dppadd(float v) { return v + __builtin_bit_cast(float, __builtin_amdgcn_update_dpp(0, __builtin_bit_cast(int, v), CTRL, 0xf, 0xf, true)); }
DI float fmin3(float a, float b, float c) { return fminf(fminf(a, b), c); }
DI f32x16 splat(float v) { f32x16 z; _Pragma("unroll") for (int i = 0; i < 16; ++i) z[i] = v; return z; }
#define Z16 splat(0.f)
DI u32x4 trfrag(lptr p) {
  bf16x4 a0 = __builtin_amdgcn_ds_read_tr16_b64_v4bf16((LAS bf16x4*)p);
  bf16x4 a1 = __builtin_amdgcn_ds_read_tr16_b64_v4bf16((LAS bf16x4*)(p + 256));
  return __builtin_bit_cast(u32x4, __builtin_shufflevector(a0, a1, 0, 1, 2, 3, 4, 5, 6, 7));
}
DI u32x4 ldl(lptr p) { return *(LAS u32x4*)p; }
DI float U2F(unsigned u) { return __uint_as_float(u); }
DI unsigned F2U(float f) { return __float_as_uint(f); }
template <bool MX> DI void red2(float& a, float& b) {
  auto r = __builtin_amdgcn_permlane32_swap(F2U(a), F2U(b), false, false);
  const float t = MX ? fmaxf(U2F(r[0]), U2F(r[1])) : U2F(r[0]) + U2F(r[1]);
  r = __builtin_amdgcn_permlane32_swap(F2U(t), F2U(t), false, false);
  a = U2F(r[0]); b = U2F(r[1]);
}
DI float xhalf_max(float v) { auto r = __builtin_amdgcn_permlane32_swap(F2U(v), F2U(v), false, false); return fmaxf(U2F(r[0]), U2F(r[1])); }
DI float upper_val(float v) { auto r = __builtin_amdgcn_permlane32_swap(F2U(v), F2U(v), false, false); return U2F(r[1]); }
DI float dot2(unsigned u, unsigned v, float c) { return __builtin_amdgcn_fdot2_f32_bf16(__builtin_bit_cast(bf16x2, u), __builtin_bit_cast(bf16x2, v), c, false); }
DI float bflo(unsigned u) { return U2F(u << 16); }
DI float bfhi(unsigned u) { return U2F(u & 0xffff0000u); }
DI void split2(float v, unsigned& hi, unsigned& lo) { hi = pk2(v, v) & 0xffffu; lo = pk2(v - bflo(hi), 0.f) & 0xffffu; }
template <int AUX> DI void st128(u32x4 v, rsrc_t rs, unsigned voff, unsigned soff) {
  SB();
  __builtin_amdgcn_raw_buffer_store_b128(v, rs, voff, soff, AUX);
  SB();
  asm volatile("s_nop 1" :: "v"(v));
  SB();
}
DI void drain() { asm volatile("s_waitcnt vmcnt(0)" ::: "memory"); }
DI void bar() { SB(); asm volatile("s_waitcnt lgkmcnt(0)\n\ts_barrier" ::: "memory"); SB(); }
constexpr int chunk_of(int s, int hh) { return s < 24 ? 7 * (s / 3) + (s % 3 == 0 ? hh : (s % 3 == 1 ? 2 + 2 * hh : 5 + hh)) : 7 * (2 * (s - 24) + hh) + 3; }
constexpr int ord(int i) { return i < 4 ? 24 + i : i - 4; }

#define STAMP(i) do { } while (0)
#define RET(i) do { } while (0)
#define RSTAMP(i) do { } while (0)
#define STAMPX(i) STAMP(i)
#define CALS(hop) do { } while (0)
#define CALA(hop) do { } while (0)
#define TOT0
#define TOT1
#define DUMP(buf, acc, part, sc) do { } while (0)

DI void dual_softmax(const f32x16& p, float c, float wtail, u32x4 (&fp)[2], u32x4 (&fm)[2]) {
  float mx = fmax3(p[0], p[1], p[2]), mn = fmax3(-p[0], -p[1], -p[2]);
#pragma unroll
  for (int i = 3; i < 15; i += 2) { mx = fmax3(mx, p[i], p[i + 1]); mn = fmax3(mn, -p[i], -p[i + 1]); }
  mx = fmax3(mx, p[15], p[15]); mn = fmax3(mn, -p[15], -p[15]);
  red2<true>(mx, mn);
  const float a = -c * mx, b = -c * mn;
  float nc = -c; PIN(nc);
  f32x16 ep = p * c + a, em = p * nc + b;
  SB();
#pragma unroll
  for (int i = 0; i < 16; ++i) ep[i] = __builtin_amdgcn_exp2f(ep[i]);
#pragma unroll
  for (int i = 0; i < 16; ++i) em[i] = __builtin_amdgcn_exp2f(em[i]);
  f32x2 sp = {ep[0], ep[1]}, sm = {em[0], em[1]};
#pragma unroll
  for (int i = 2; i < 12; i += 2) { sp += (f32x2){ep[i], ep[i + 1]}; sm += (f32x2){em[i], em[i + 1]}; }
  sp += wtail * ((f32x2){ep[12], ep[13]} + (f32x2){ep[14], ep[15]});
  sm += wtail * ((f32x2){em[12], em[13]} + (f32x2){em[14], em[15]});
  float u = sp[0] + sp[1], v = sm[0] + sm[1];
  red2<false>(u, v);
  ep *= __builtin_amdgcn_rcpf(u); em *= __builtin_amdgcn_rcpf(v);
  fp[0] = pack8<0>(ep); fp[1] = pack8<1>(ep); fm[0] = pack8<0>(em); fm[1] = pack8<1>(em);
}
DI u32x4 ident_frag(int s, int r, int hh) {
  const unsigned val = 0x3f80u << (16 * (r & 1));
  const int idx = (((r >> 2) ^ hh) & 1) * 8 + (r >> 4) * 4 + ((r >> 2) & 2) + ((r >> 1) & 1) - 4 * s;
  return (u32x4){idx == 0 ? val : 0u, idx == 1 ? val : 0u, idx == 2 ? val : 0u, idx == 3 ? val : 0u};
}
#define CHK1 3
#define CHK2 12
template <typename S, typename A> DI void steps28(S step, A abort) {
#pragma unroll
  for (int i = 0; i <= CHK1; ++i) step(i);
  if (abort()) return;
#pragma unroll
  for (int i = CHK1 + 1; i < 28; ++i) step(i);
}
template <int PD, typename F, typename G, typename A> DI void gemm1(f32x16& acc, lptr wb, const u32x4 (&xf)[28], F pre, G post, A abort) {
  u32x4 wr[PD];
#pragma unroll
  for (int i = 0; i < PD - 1; ++i) wr[i] = trfrag(wb + ord(i) * 1024);
  SB();
  steps28([&](int i) __attribute__((always_inline)) {
    pre(i);
    if (i + PD - 1 < 28) wr[(i + PD - 1) % PD] = trfrag(wb + ord(i + PD - 1) * 1024);
    SB();
    acc = mfma(wr[i % PD], xf[i], acc);
    SB();
    post(i);
    SB();
  }, abort);
}
template <typename H, typename F, typename G, typename A>
DI void gemm2(f32x16& ak, f32x16& av, lptr bk, const u32x4 (&xf)[28], H head, F pre, G post, A abort) {
  u32x4 wk[3], wv[3];
  head([&](int u) {
    if (u == 0) wv[0] = trfrag(bk + SLAB + ord(0) * 1024);
    if (u == 1) wk[0] = trfrag(bk + ord(0) * 1024);
    if (u == 2) wv[1] = trfrag(bk + SLAB + ord(1) * 1024);
    if (u == 3) wk[1] = trfrag(bk + ord(1) * 1024);
    SB();
  });
  steps28([&](int i) __attribute__((always_inline)) {
    pre(i);
    if (i + 2 < 28) wv[(i + 2) % 3] = trfrag(bk + SLAB + ord(i + 2) * 1024);
    SB();
    av = mfma(wv[i % 3], xf[i], av);
    SB();
    if (i + 2 < 28) wk[(i + 2) % 3] = trfrag(bk + ord(i + 2) * 1024);
    post(i, 0);
    SB();
    ak = mfma(wk[i % 3], xf[i], ak);
    SB();
    post(i, 1);
    SB();
  }, abort);
}
#define NOAB [] { return false; }
#define EARLYAB [&] { return __builtin_expect(__any(m == sent), 0); }
#define LA 12
#define XH 27
#define VSLEEP 8
#define QSLEEP 28
#define LAO 8
#define XBAR 26
#define DOA 3
#define DOB 6
#define DX 3
#define KVI 6
#define KVB 3
#define NKB 5
#define XM 4
#ifndef NV0
#define NV0 7
#endif
#define NK2 8
#define NK3 9
#define PACE 8
#define LAG 6
#define PACED() do { if (PACE) asm volatile("s_nop %0" :: "n"(PACE)); SB(); } while (0)
#define PSLP 4
#define RDLY 0
#define KDLY 0
#define LAND 30
#define TKA0 4100
#define TKA 3600
#define TKB1 1950
#define TKB 2050
#define TOA0 2050
#define TOA 1850
#define TOB1 2750
#define TOB 2520
#define TX0 3800
#define TX 2850
#define BACK 8
#define NQ0 7
#define NQB 0
#define TX2 3200
#define ND0 10
#define NPO 14
#define SWS 6
#define LMAX 1.8446744e19f
#define CSL 2
#define WSL 4
#define PACE2() do { asm volatile("s_nop %0" :: "n"(PACE - 2)); SB(); } while (0)
#define NQ1 4
#define PDELAY 14

__global__ void __launch_bounds__(512, 2) fused_kernel(const u16* __restrict__ x0, const u16* __restrict__ wqkvA, const u16* __restrict__ wphi,
                                                       const u16* __restrict__ woA, const u16* __restrict__ wqkvB, const u16* __restrict__ woB,
                                                       u16* __restrict__ out, unsigned char* ws) {
  extern __shared__ __attribute__((aligned(16))) unsigned char smem[];
  const lptr lds = (lptr)smem;
  const int tid = threadIdx.x, lane = tid & 63, wave = __builtin_amdgcn_readfirstlane(tid >> 6);
  TOT0
  const int w = wave & 3;
  const int r = lane & 31, hh = lane >> 5;
  const int b = blockIdx.x;
  const int tb = ((b >> 1) & 3) * 4 + (b >> 6), h = (b & 1) * 8 + ((b >> 3) & 7);
  const int me = tb * 16 + h;
  const int odd = h & 1, pair = h >> 1;
  const int T = tb * 4 + w;
  const unsigned TOFF = tb * 8 * XO_B + w * TILE_B;
  const int kh = w & 1, tx = 2 * odd + (w >> 1);
  const unsigned XOFF = tb * 8 * XO_B + tx * TILE_B;
  const rsrc_t rWS = __builtin_amdgcn_make_buffer_rsrc((void*)ws, 0, WS_END, 0x00020000);
  unsigned budget = SPIN_CAP;
  u64 epoch;
  u32x2 tag;
  u32x2 ev;
  auto load_epoch = [&]() { ev = __builtin_amdgcn_raw_buffer_load_b64(rWS, 0, WS_EPOCH, SC1); SB(); };
  auto make_tag = [&]() {
    epoch = (u64)(unsigned)__builtin_amdgcn_readfirstlane(ev[0]) | ((u64)(unsigned)__builtin_amdgcn_readfirstlane(ev[1]) << 32);
    u64 z = epoch + 0x9E3779B97F4A7C15ull;
    z = (z ^ (z >> 30)) * 0xBF58476D1CE4E5B9ull; z = (z ^ (z >> 27)) * 0x94D049BB133111EBull; z ^= z >> 31;
    unsigned t0 = (unsigned)z, t1 = (unsigned)(z >> 32);
    asm volatile("" : "+s"(t0), "+s"(t1));
    tag = (u32x2){t0, t1};
  };
  volatile LAS unsigned* const cnt = (volatile LAS unsigned*)(lds + L_SYNC);
  auto arrive = [&](int k) { if (lane == 0) asm volatile("ds_add_u32 %0, %1" :: "v"(lds + L_SYNC + 4 * k), "v"(1u) : "memory"); };
  auto gather_all = [&](int layer, bool nap) {
    while (__builtin_amdgcn_readfirstlane(*cnt) < 8u * layer + 4u) { if (nap) __builtin_amdgcn_s_sleep(2); }
    asm volatile("" ::: "memory");
  };
  auto poll = [&](int hop, int mask) {
    int ln = lane & mask; PIN(ln);
    u32x2 v;
    for (;;) {
      v = __builtin_amdgcn_raw_buffer_load_b64(rWS, ln * 8, WS_FLAGS + hop * HOP_B + me * 128, SC1);
      asm volatile("" ::: "memory");
      if (__all(v[0] == tag[0] && ((v[1] ^ tag[1]) >> 20) == 0)) break;
      if (budget <= 1) break;
      --budget;
      if (PSLP) __builtin_amdgcn_s_sleep(PSLP);
    }
    const unsigned t = v[1] << 12;
    for (int n = 0; n < 64; ++n) {
      const unsigned now = (unsigned)__builtin_amdgcn_s_memrealtime() << 12;
      if (!__any((int)(t - now) > 0)) break;
    }
    asm volatile("" ::: "memory");
  };
  auto make_trb = [&]() {
    const int rb = (lane >> 4) & 1, q = (lane >> 2) & 3, p = lane & 3;
    const int rp = 4 * rb + p, lam = (rp & 4) | ((rp & 1) << 1) | ((rp >> 1) & 1);
    const int pc = ((odd ? 0x01327654 : 0x76543210) >> (4 * lam)) & 7;
    return lds + (8 * hh + q) * 64 + pc * 8;
  };
  auto store_x0 = [&](u32x4 f0, unsigned base) { int ln = lane; PIN(ln); st128<SC1>(f0, rWS, ln * 16, base + XOFF + (3 * pair + 2 * kh) * 1024); };
  auto store_x1 = [&](u32x4 f1, unsigned base) {
    int ln = lane; PIN(ln);
    st128<SC1>(f1, rWS, ln * 16 + __umul24(ln >> 5, 24576 - 512 - 1024 - pair * 2048), base + XOFF + (3 * pair + 1) * 1024 + kh * 512);
  };
  auto store_o = [&](u32x4 f0, u32x4 f1, unsigned base) {
    int ln = lane; PIN(ln);
    st128<SC1>(f0, rWS, ln * 16, base + TOFF + (3 * pair + 2 * odd) * 1024);
    st128<SC1>(f1, rWS, ln * 16 + (ln >> 5) * OOB, base + TOFF + (3 * pair + 1) * 1024 + odd * 512);
    __builtin_amdgcn_raw_buffer_store_b64(lo2(f1), rWS, ln * 8 + (1 - (ln >> 5)) * OOB, base + TOFF + 24576 - 256 + h * 256, SC1);
  };
  const int ht = tid & 255, sub = lane & 3;
  const int col = h * 56 - odd * 8;
  int krow[7];
  auto mkrow = [&](int i) {
    const int s = 4 * i + w;
    const int p = (s * 43) >> 7, m = s - 3 * p;
    const int c = i < 6 ? 7 * p + ((0x520 >> (4 * m)) & 15) + hh * (1 + (m & 1)) : 7 * (2 * w + hh) + 3;
    krow[i] = 8 * c + ((lane >> 2) & 7);
  };
  auto ldw = [&](rsrc_t rs, int i, int stride, int off) { int k = krow[i]; PIN(k); const u32x4 v = __builtin_amdgcn_raw_buffer_load_b128(rs, k * stride + sub * 16, col + off, 0); SB(); return v; };
  auto write_slab = [&](const u32x4* p, unsigned off) {
    lptr d = lds + off + ht * 16; PIN(d);
#pragma unroll
    for (int i = 0; i < 7; ++i) *(LAS u32x4*)(d + i * 4096) = p[i];
  };
  auto put_slab = [&](u32x4* p, int type, int part) {
    const unsigned keep01 = (odd && sub == 0) ? 0u : ~0u, keep23 = (!odd && sub == 3) ? 0u : ~0u;
#pragma unroll
    for (int i = 0; i < 7; ++i) {
      p[i][0] &= keep01; p[i][1] &= keep01; p[i][2] &= keep23; p[i][3] &= keep23;
      if (type == 0 && part == 0) { p[i][0] |= ~keep01 & 0x3f80u; p[i][2] |= ~keep23 & 0x3f80u; }
    }
    write_slab(p, part * SLAB);
  };
  auto sum_slab = [&](int type, int part, lptr trb) {
    lptr wb = trb + part * SLAB + w * 1024; PIN(wb);
    u32x4 wr[7];
#pragma unroll
    for (int i = 0; i < 7; ++i) wr[i] = trfrag(wb + i * 4096);
    f32x16 a = Z16;
#pragma unroll
    for (int i = 0; i < 7; ++i) a = mfma((u32x4){ONES, ONES, ONES, ONES}, wr[i], a);
    unsigned hi, lo; split2(a[0], hi, lo);
    if (hh == (w >> 1)) *(LAS u32x2*)(lds + L_CS + type * 3072 + part * 1024 + lane * 16 + (w & 1) * 8) = (u32x2){hi | (lo << 16), hi};
  };
  auto first_slab = [&](u32x4* p, int type, int part, lptr trb) { put_slab(p, type, part); sum_slab(type, part, trb); };
  const rsrc_t rWA = __builtin_amdgcn_make_buffer_rsrc((void*)wqkvA, 0, DM * 3 * DM * 2, 0x00020000);
  auto dma_x0 = [&](int i0, int i1) __attribute__((always_inline)) {
    const rsrc_t rX0 = __builtin_amdgcn_make_buffer_rsrc((void*)x0, 0, SEQ * DM * 2, 0x00020000);
    const int o0 = (lane >> 3) * 896 + (((lane & 7) ^ (lane >> 4)) * 16), o1 = o0 ^ 64;
#pragma unroll
    for (int i = i0; i < i1; ++i) {
      __builtin_amdgcn_raw_ptr_buffer_load_lds(rX0, (LAS void*)(lds + L_X0 + w * SLAB + i * 1024), 16, (i & 1) ? o1 : o0, (T * 32 + (i & 3) * 8) * 896 + (i >> 2) * 128, 0, 0);
      SB();
    }
  };

  if (wave < 4) {
    RSTAMP(62); STAMP(0);
    u32x4 xf[28];
    f32x16 xs, ak, av;
    float rstd;
    u32x4 mf;
    auto stats = [&](float s1, float s2) {
      const float mean = s1 * (1.0f / DM);
      rstd = __builtin_amdgcn_rsqf(fmaxf(s2 * (1.0f / DM) - mean * mean, 0.f) + 1e-5f);
      unsigned hi, lo; split2(-mean, hi, lo);
      mf = (u32x4){hi | (hi << 16), lo, hi | (hi << 16), lo};
      PIN(rstd); PIN(mf);
    };
    lptr trb;
    u32x4 id0, id1;
    float wtail;
    unsigned lomask, phoff;
    {
      __builtin_amdgcn_s_setprio(3);
#pragma unroll
      for (int i = 0; i < 7; ++i) { mkrow(i); xf[21 + i] = ldw(rWA, i, 2688, 896); }
      STAMPX(54);
      trb = make_trb();
      id0 = ident_frag(0, r, hh); id1 = ident_frag(1, r, hh);
      wtail = hh ? 0.f : 1.f; lomask = hh ? 0u : ~0u;
      PIN(trb); PIN(id0); PIN(id1); PIN(wtail); PIN(lomask);
      if (w >= 2) {
        u32x4 mk;
#pragma unroll
        for (int i = 0; i < 4; ++i) { const int k0 = ((2 * i) & 3) + 8 * (i >> 1) + 4 * hh + 16 * (w & 1); mk[i] = (k0 > r ? 0xffffu : 0u) | (k0 + 1 > r ? 0xffff0000u : 0u); }
        *(LAS u32x4*)(lds + L_MK + (w & 1) * 1024 + lane * 16) = mk;
      }
      if (w == 0) { cnt[0] = 0u; cnt[1] = 0u; cnt[2] = 0u; cnt[3] = 0u; cnt[4] = 0u; cnt[5] = 0u; cnt[6] = 0u; }
      cnt[8 + w] = 0u;
      const int lam = 4 * (w >> 1) + 2 * hh + (w & 1);
      const int pc = ((odd ? 0x01327654 : 0x76543210) >> (4 * lam)) & 7;
      unsigned off = lam == 7 ? OOB : ((h * 28 + 4 * pc - 4 * odd) * 28 + (r < 28 ? r : r - 8)) * 2;
      phoff = off; PIN(phoff);
      __builtin_amdgcn_s_setprio(0);
    }
    tag = (u32x2){0u, 0u}; epoch = 0;
    asm volatile("" : "=v"(ak), "=v"(av), "=v"(rstd), "=v"(mf), "=v"(xs));
#pragma unroll
    for (int i = 0; i < 21; ++i) asm volatile("" : "=v"(xf[i]));
    unsigned sent = SENT;
    auto release = [&](int hop) { if (w == 0) poll(hop, 15); bar(); };
    for (int layer = 0; layer < NL; ++layer) {
      const int type = layer & 1;
      const int pb = 2 + layer * 14;
      if (layer > 0) {
        unsigned base = WS_XO + (2 * layer - 1) * XO_B + TOFF;
        int v16 = lane * 16, vs = v16 + (v16 & 512);
        lptr bk = trb + L_WK;
        PIN(vs);
        release(3 * layer - 1);
        STAMP(pb + 0);
        for (;;) {
          PIN(v16); PIN(vs); PIN(bk); asm volatile("" : "+s"(base));
          u32x4 sp[8];
          auto ld = [&](int s) { xf[4 + s] = __builtin_amdgcn_raw_buffer_load_b128(rWS, v16 + (s & 3) * 1024, base + (s >> 2) * 4096, SC1); SB(); };
          unsigned m = 1;
          ak = Z16; av = Z16;
          float s1 = 0.f, s2 = 0.f, mean;
          unsigned hi;
          u32x4 ck, cv;
          gemm2(ak, av, bk, xf,
                    [&](auto gap) {
#pragma unroll
                      for (int u = 0; u < 8; ++u) { sp[u] = __builtin_amdgcn_raw_buffer_load_b128(rWS, vs + ((u >> 1) & 1) * 2048 + (u & 1) * 512, base + 24576 + (u >> 2) * 4096, SC1); SB(); gap(u); if (u < 4) PACE2(); else PACED(); }
#pragma unroll
                      for (int s = 0; s < LA - 8; ++s) { ld(s); PACED(); }
                    },
                    [&](int i) {
                      if (i + LA - 8 < 24) ld(i + LA - 8);
                      if (i < 4) { PIN(sp[2 * i]); PIN(sp[2 * i + 1]); xf[i] = cat(lo2(sp[2 * i]), lo2(sp[2 * i + 1])); }
                      if (i == 22) { av = mfma(cv, mf, av); SB(); }
                      if (i == 24) { ak = mfma(ck, mf, ak); SB(); }
                    },
                    [&](int i, int hf) {
                      if (i < 4) m = max4(m, sp[2 * i + hf]); else m = umax3(m, xf[i][2 * hf], xf[i][2 * hf + 1]);
                      PIN(m);
                      if (i < 8) { if (hf) { s2 += U2F(sp[i][3]); PIN(s2); } else { s1 += U2F(sp[i][2]); PIN(s1); } }
                      if (hf) return;
                      if (i == 13) asm volatile("ds_write_b32 %0, %1" :: "v"(lds + L_SYNC + 32 + 4 * w), "v"(layer) : "memory");
                      if (i == 9) { auto t = __builtin_amdgcn_permlane32_swap(F2U(s1), F2U(s2), false, false); s1 = U2F(t[0]) + U2F(t[1]); PIN(s1); }
                      if (i == 10) { auto t = __builtin_amdgcn_permlane32_swap(F2U(s1), F2U(s1), false, false); s1 = U2F(t[0]); s2 = U2F(t[1]); PIN(s1); PIN(s2); }
                      if (i == 14) { mean = s1 * (-1.0f / DM); s2 = s2 * (1.0f / DM); PIN(mean); PIN(s2); }
                      if (i == 15) { s2 = fmaxf(fmaf(-mean, mean, s2), 0.f); PIN(s2); }
                      if (i == 16) { rstd = __builtin_amdgcn_rsqf(s2 + 1e-5f); PIN(rstd); }
                      if (i == 17) { hi = pk2(mean, mean); s1 = mean - bfhi(hi); PIN(hi); PIN(s1); }
                      if (i == 18) { const unsigned lo = pk2(s1, 0.f); mf = (u32x4){hi, lo, hi, lo}; PIN(mf); }
                      if (i == 19) { const lptr cs = lds + L_CS + type * 3072 + v16; ck = ldl(cs); cv = ldl(cs + 1024); }
                    }, EARLYAB);
          if (__builtin_expect(!__any(m == sent), 1)) break;
          if (--budget == 0) sent = 0u;
          RET(layer * 3);
          __builtin_amdgcn_s_sleep(BACK);
          asm volatile("" ::: "memory");
        }
      }
      int first = layer; asm volatile("" : "+s"(first));
      if (first == 0) {
        int ln = lane; PIN(ln);
        const int hh = ln >> 5;
        const unsigned ra = (ln & 31) * 128 + ((ln >> 1) & 7) * 16;
        const lptr stg = lds + L_X0 + w * SLAB;
        auto kof = [](int c) { return (c >> 3) * 4096 + (c & 7) * 16; };
        lptr ad[28];
#pragma unroll
        for (int i = 0; i < 28; ++i) {
          const int k0 = kof(chunk_of(ord(i), 0)), k1 = kof(chunk_of(ord(i), 1));
          ad[i] = stg + (ra ^ k0 ^ (~lomask & (k0 ^ k1))); PIN(ad[i]);
        }
        lptr pab = lds + L_X0 + tx * SLAB + (ra ^ kof(7 * pair + 5 * kh + hh)), pc = lds + L_X0 + tx * SLAB + (ra ^ kof(7 * pair + (hh ? 3 : 2 + 2 * kh))); PIN(pab); PIN(pc);
        SB();
        first_slab(xf + 21, 0, 0, trb);
        STAMPX(53);
        bar();
        STAMPX(55);
#pragma unroll
        for (int i = 0; i < 28; ++i) xf[i] = ldl(ad[i]);
        u32x4 xa = ldl(pab), xc = ldl(pc);
        arrive(5);
        SB();
        float s2a = 0.f, s2b = 0.f;
#pragma unroll
        for (int i = 0; i < 28; ++i) {
          const u32x4 u = xf[i]; s2a = dot2(u[1], u[1], dot2(u[0], u[0], s2a)); s2b = dot2(u[3], u[3], dot2(u[2], u[2], s2b));
          PIN(s2a); PIN(s2b);
          SB();
        }
        const rsrc_t rPH = __builtin_amdgcn_make_buffer_rsrc((void*)wphi, 0, 16 * 28 * 28 * 2, 0x00020000);
        unsigned e[4];
#pragma unroll
        for (int j = 0; j < 4; ++j) e[j] = __builtin_amdgcn_raw_buffer_load_b16(rPH, phoff + j * 56, 0, 0);
        load_epoch();
        STAMP(1);
        u32x4 cc = ldl(lds + L_CS + ln * 16);
        ak = Z16;
        lptr bk = trb + L_WK; PIN(bk);
        gemm1<5>(ak, bk, xf, [](int) {}, [&](int i) {
          if (i == 4) {
            const unsigned sel = hh && kh ? 0u : ~0u;
            xc[0] = (xc[0] & sel) | (xc[2] & ~sel); xc[1] = (xc[1] & sel) | (xc[3] & ~sel); PIN(xc);
          }
          if (i >= 16 && i < 24) { const unsigned u = i < 20 ? xa[i & 3] : xc[i & 3]; xs[2 * (i - 16)] = bflo(u); xs[2 * (i - 16) + 1] = bfhi(u); PIN(xs); }
        }, NOAB);
        asm volatile("" :: "v"(ak));
        *(LAS u32x2*)(lds + L_WPH + (w >> 1) * 1024 + ln * 16 + (w & 1) * 8) = (u32x2){e[0] | (e[1] << 16), e[2] | (e[3] << 16)};
        make_tag();
        STAMPX(57);
        bar();
        STAMP(2);
        const u32x4 cv = ldl(lds + L_CS + 1024 + ln * 16);
        av = Z16;
        gemm1<5>(av, bk + SLAB, xf, [&](int i) { if (i == 8) { ak = mfma(cc, mf, ak); SB(); } if (i == 10) { av = mfma(cv, mf, av); SB(); } }, [&](int i) {
          if (i == 2) {
            float s1 = U2F(F2U(ak[12]) & ~lomask), s2 = s2a + s2b;
            red2<false>(s1, s2);
            stats(s1, s2);
          }
        }, NOAB);
      }
      asm volatile("" ::: "memory");
      arrive(0);
      STAMPX(pb + 1);
      const unsigned mid = WS_MID + (h * 4 + layer) * MID_B;
      int ln16 = lane * 16; PIN(ln16);
      u32x4 wph[2], kf0, kf1;
      if (type == 0) {
        wph[0] = ldl(lds + L_WPH + ln16); wph[1] = ldl(lds + L_WPH + 1024 + ln16);
        DUMP(0, ak, 1, rstd); DUMP(0, av, 2, rstd);
        kf0 = pack8<0>(ak); kf1 = pack8<1>(ak);
        f32x16 p = mfma(wph[0], kf0, Z16);
        p = mfma(wph[1], kf1, p);
        SB();
#pragma unroll
        for (int i = 0; i < 16; ++i) { float t = av[i] * rstd; PIN(t); av[i] = t; }
        u32x4 vb0 = pack8<0>(av), vb1 = pack8<1>(av);
        vb1[2] |= ~lomask & 0x3f80u;
        PIN(vb0); PIN(vb1);
        SB();
        u32x4 fp[2], fm[2];
        dual_softmax(p, rstd * L2E, wtail, fp, fm);
        fp[1][2] &= lomask; fp[1][3] &= lomask; fm[1][2] &= lomask; fm[1][3] &= lomask;
        PIN(fp[0]); PIN(fp[1]); PIN(fm[0]); PIN(fm[1]);
        gather_all(layer, false);
        const int rr = (ln16 >> 4) & 31, pc = (ln16 >> 9) ^ ((rr >> 1) & 3);
        const lptr d0 = lds + L_SCR + w * 2048 + rr * 64 + pc * 16, d1 = lds + L_SCR + w * 2048 + rr * 64 + (pc ^ 2) * 16;
        *(LAS u32x4*)(d0) = fp[0]; *(LAS u32x4*)(d1) = fp[1];
        *(LAS u32x4*)(d0 + 8192) = fm[0]; *(LAS u32x4*)(d1 + 8192) = fm[1];
        *(LAS u32x4*)(d0 + 16384) = vb0; *(LAS u32x4*)(d1 + 16384) = vb1;
        bar();
      } else {
        av *= rstd;
        DUMP(0, av, 2, 1.f);
        u32x4 vb1 = pack8<1>(av);
        vb1[2] |= ~lomask & 0x3f80u;
        f32x16 vt = mfma(pack8<0>(av), id0, Z16);
        vt = mfma(vb1, id1, vt);
        SB();
#pragma unroll
        for (int i = 0; i < 16; ++i) { float t = ak[i] * rstd; PIN(t); ak[i] = t; }
        DUMP(0, ak, 1, 1.f);
        kf0 = pack8<0>(ak); kf1 = pack8<1>(ak);
        const unsigned g = mid + T * 4096;
        const lptr d = lds + L_SCR + (8 + w) * 4096 + ln16;
        st128<SC1>(kf0, rWS, ln16, g);
        const u32x4 vf0 = pack8<0>(vt), vf1 = pack8<1>(vt);
        st128<SC1>(vf0, rWS, ln16 + 2048, g);
        st128<SC1>(vf1, rWS, ln16 + 3072, g);
        {
          u32x4 kc = kf1;
          kc[3] |= ~lomask & (tag[0] + layer - xhalf_addu(add4(add4(0u, kf0), kf1) + add4(add4(0u, vf0), vf1)));
          st128<SC1>(kc, rWS, ln16 + 1024, g);
          CALS(3 * layer);
        }
        gather_all(layer, false);
        *(LAS u32x4*)(d) = kf0; *(LAS u32x4*)(d + 1024) = kf1; *(LAS u32x4*)(d + 2048) = vf0; *(LAS u32x4*)(d + 3072) = vf1;
      }
      STAMPX(pb + 2);
      u32x4 qf0, qf1;
      {
        f32x16 aq = mfma(ldl(lds + L_CS + type * 3072 + 2048 + ln16), mf, Z16);
        lptr bq = trb + L_WQ; PIN(bq);
        gemm1<5>(aq, bq, xf, [](int) {}, [](int) {}, NOAB);
        aq *= type ? rstd * (0.18898223650461361f * L2E) : 1.0f;
        DUMP(0, aq, 0, type ? 1.f : rstd);
        qf0 = pack8<0>(aq); qf1 = pack8<1>(aq);
        qf1[2] |= ~lomask & 0x3f80u;
      }
      f32x16 o;
      if (type == 0) {
        u32x4 fp[2], fm[2];
        {
          f32x16 p = mfma(wph[0], qf0, Z16);
          p = mfma(wph[1], qf1, p);
          dual_softmax(p, rstd * L2E, wtail, fp, fm);
        }
        PIN(fp[0]); PIN(fp[1]); PIN(fm[0]); PIN(fm[1]);
        STAMPX(pb + 3);
        bar();
        STAMP(pb + 4);
        const lptr kvf = lds + L_KVF + ln16;
        const u32x4 a0 = ldl(kvf), a1 = ldl(kvf + 1024), a2 = ldl(kvf + 2048), a3 = ldl(kvf + 3072);
        SB();
        o = mfma(a0, fp[0], Z16);
        o = mfma(a1, fp[1], o);
        o = mfma(a2, fm[0], o);
        o = mfma(a3, fm[1], o);
        o *= __builtin_amdgcn_rcpf(upper_val(o[12]) + 1e-6f);
      } else {
        lptr kv = lds + L_SCR + w * 4096 + ln16; PIN(kv);
        auto ex4 = [&](f32x16& s, int i0) {
#pragma unroll
          for (int i = i0; i < i0 + 4; ++i) s[i] = __builtin_amdgcn_exp2f(s[i]);
          unsigned a = pk2(s[i0], s[i0 + 1]), c = pk2(s[i0 + 2], s[i0 + 3]); PIN(a); PIN(c);
          SB();
          return (u32x2){a, c};
        };
        auto tile_max = [&](int j, int side, float m) {
          f32x16 t = mfma(ldl(kv + j * 4096 + 1024), qf1, mfma(ldl(kv + j * 4096), qf0, Z16));
          int lim = ((ln16 >> 4) & 31) - ((ln16 >> 7) & 4); PIN(lim);
#pragma unroll
          for (int i = 0; i < 16; ++i) { const bool up = (i & 3) + 8 * (i >> 2) > lim; m = fmaxf(m, (side == 0 || up == (side == 1)) ? t[i] : m); }
          return m;
        };
        float m = -__builtin_inff(), l;
        for (int pass = 0;; ++pass) {
          const u32x4 mk0 = ldl(lds + L_MK + ln16), mk1 = ldl(lds + L_MK + 1024 + ln16);
          f32x16 negm;
          {
            const u32x4 v0 = ldl(kv + 8 * 4096 + 2048), v1 = ldl(kv + 8 * 4096 + 3072);
            f32x16 s = mfma(ldl(kv + 8 * 4096 + 1024), qf1, mfma(ldl(kv + 8 * 4096), qf0, Z16));
#pragma unroll
            for (int i = 0; i < 16; ++i) {
              const unsigned k = (i < 8 ? mk0 : mk1)[(i >> 1) & 3], on = (i & 1) ? (unsigned)((int)k >> 31) : (unsigned)((int)(k << 16) >> 31);
              s[i] = U2F((F2U(s[i]) & ~on) | (on & 0xff800000u));
            }
#pragma unroll
            for (int i = 0; i < 16; i += 2) m = fmax3(m, s[i], s[i + 1]);
            m = xhalf_max(m);
            s -= m;
            negm = splat(-m);
            PIN(negm);
            const u32x2 a = ex4(s, 0), c = ex4(s, 4), e = ex4(s, 8), f = ex4(s, 12);
            o = mfma(v1, cat(e, f), mfma(v0, cat(a, c), Z16));
            PIN(o);
          }
          if (pass == 0) {
            STAMPX(pb + 3);
            bar();
            STAMP(pb + 4);
          }
          u32x4 kk[2][2], vv[2][2], pa, pb2;
          f32x16 st[2];
          kk[0][0] = ldl(kv); kk[0][1] = ldl(kv + 1024); kk[1][0] = ldl(kv + 4096); kk[1][1] = ldl(kv + 4096 + 1024);
          SB();
          st[0] = mfma(kk[0][1], qf1, mfma(kk[0][0], qf0, negm));
          SB();
#pragma unroll
          for (int j = 0; j < 8; ++j) {
            f32x16& s = st[j & 1];
            vv[j & 1][0] = ldl(kv + j * 4096 + 2048); vv[j & 1][1] = ldl(kv + j * 4096 + 3072);
            if (j < 6) { kk[j & 1][0] = ldl(kv + (j + 2) * 4096); kk[j & 1][1] = ldl(kv + (j + 2) * 4096 + 1024); }
            SB();
            if (j == 0) { st[1] = mfma(kk[1][0], qf0, negm); SB(); }
            const u32x2 a = ex4(s, 0);
            if (j == 0) st[1] = mfma(kk[1][1], qf1, st[1]); else o = mfma(vv[(j - 1) & 1][0], pa, o);
            SB();
            const u32x2 c = ex4(s, 4);
            if (j == 7) o = mfma(vv[0][1], pb2, o); else if (j > 0) st[(j + 1) & 1] = mfma(kk[(j + 1) & 1][0], qf0, negm);
            SB();
            const u32x2 e = ex4(s, 8);
            if (j == 7) o = mfma(vv[1][0], cat(a, c), o); else if (j > 0) st[(j + 1) & 1] = mfma(kk[(j + 1) & 1][1], qf1, st[(j + 1) & 1]);
            SB();
            const u32x2 f = ex4(s, 12);
            if (j == 7) o = mfma(vv[1][1], cat(e, f), o); else if (j > 0) o = mfma(vv[(j - 1) & 1][1], pb2, o);
            SB();
            pa = cat(a, c); pb2 = cat(e, f);
            if (j == 0) { pa &= mk0; pb2 &= mk1; PIN(pa); PIN(pb2); }
          }
          l = upper_val(o[12]);
          if (pass > 0 || __all(l < LMAX)) break;
          RET(13);
          m = tile_max(0, 1, m);
#pragma unroll 1
          for (int j = 1; j < 8; ++j) m = tile_max(j, 0, m);
        }
        arrive(4);
        o *= __builtin_amdgcn_rcpf(l);
      }
      DUMP(1, o, 0, 1.f);
      store_o(pack8<0>(o), pack8<1>(o), WS_XO + (2 * layer) * XO_B);
      CALS(3 * layer + 1);
      STAMPX(pb + 6);
      lptr bown, both;
      {
        int ln = lane; PIN(ln);
        const int q = (ln >> 2) & 3, rp = ((ln >> 2) & 4) + (ln & 3), lam = (rp & 4) | ((rp & 1) << 1) | ((rp >> 1) & 1);
        const int pe = lam, po = 6 + ((0x01327654 >> (4 * lam)) & 7);
        const lptr rb = lds + L_WO + kh * SLAB + (8 * (ln >> 5) + q) * 128;
        const lptr ae = rb + ((pe * 8) ^ ((q >> 1) * 64)), ao = rb + ((po * 8) ^ ((q >> 1) * 64));
        bown = kh ? ao : ae; both = kh ? ae : ao;
        PIN(bown); PIN(both);
      }
      unsigned base = WS_XO + (2 * layer) * XO_B + XOFF + kh * 12288;
      int v16 = ln16, vs = (v16 >> 1) + ((v16 >> 1) & 256);
      PIN(vs);
      release(3 * layer + 1);
      STAMP(pb + 7);
      f32x16 xn, xo;
      u32x4 wa[4];
        auto tr = [](lptr p) {
          bf16x4 a0 = __builtin_amdgcn_ds_read_tr16_b64_v4bf16((LAS bf16x4*)p), a1 = __builtin_amdgcn_ds_read_tr16_b64_v4bf16((LAS bf16x4*)(p + 512));
          return __builtin_bit_cast(u32x4, __builtin_shufflevector(a0, a1, 0, 1, 2, 3, 4, 5, 6, 7));
        };
      for (;;) {
        PIN(v16); PIN(vs); asm volatile("" : "+s"(base));
        u32x2 sp[4];
        u32x4 wb[4];
#pragma unroll
        for (int u = 0; u < 4; ++u) { sp[u] = __builtin_amdgcn_raw_buffer_load_b64(rWS, vs + (u >> 1) * 1024 + (u & 1) * 256, base + 24576 - kh * 10240, SC1); SB(); if (u & 1) PACED(); }
#pragma unroll
        for (int s = 0; s < LAO; ++s) { xf[2 + s] = __builtin_amdgcn_raw_buffer_load_b128(rWS, v16 + (s & 3) * 1024, base + (s >> 2) * 4096, SC1); SB(); if (s < 3) wb[s] = tr(both + s * 2048); else if (s < 6) wa[s - 3] = tr(bown + (s - 3) * 2048); SB(); if (s < 6) PACE2(); else PACED(); }
        xf[0] = cat(sp[0], sp[1]); xf[1] = cat(sp[2], sp[3]);
        xn = xs; xo = Z16;
        unsigned m = 1;
#pragma unroll
        for (int i = 0; i < 14; ++i) {
          if (i + LAO < 12) { const int s = i + LAO; xf[2 + s] = __builtin_amdgcn_raw_buffer_load_b128(rWS, v16 + (s & 3) * 1024, base + (s >> 2) * 4096, SC1); SB(); }
          if (i + 3 < 14) wb[(i + 3) & 3] = tr(both + (i + 3) * 2048);
          SB();
          xo = mfma(wb[i & 3], xf[i], xo);
          SB();
          m = max4(m, xf[i]); PIN(m);
          if (i >= LAG) {
            wa[(i - LAG + 3) & 3] = tr(bown + (i - LAG + 3) * 2048);
            SB();
            xn = mfma(wa[(i - LAG) & 3], xf[i - LAG], xn);
          }
          SB();
        }
        if (__builtin_expect(!__any(m == sent), 1)) break;
        if (--budget == 0) sent = 0u;
        RET(layer * 3 + 2);
        __builtin_amdgcn_s_sleep(BACK);
        asm volatile("" ::: "memory");
      }
      {
        const lptr ex = lds + L_WQ + ln16;
        f32x4 t[4];
#pragma unroll
        for (int i = 14 - LAG; i < 14; ++i) {
          if (i + 3 < 14) wa[(i + 3) & 3] = tr(bown + (i + 3) * 2048);
          SB();
          xn = mfma(wa[i & 3], xf[i], xn);
          SB();
          if (i == 15 - LAG) {
#pragma unroll
            for (int g = 0; g < 4; ++g) *(LAS f32x4*)(ex + w * 4096 + g * 1024) = (f32x4){xo[4 * g], xo[4 * g + 1], xo[4 * g + 2], xo[4 * g + 3]};
          }
          if (i == 11) {
            bar();
#pragma unroll
            for (int g = 0; g < 4; ++g) t[g] = *(LAS f32x4*)(ex + (w ^ 1) * 4096 + g * 1024);
          }
          SB();
        }
#pragma unroll
        for (int g = 0; g < 4; ++g) {
#pragma unroll
          for (int j = 0; j < 4; ++j) xs[4 * g + j] = xn[4 * g + j] + t[g][j];
        }
      }
      STAMPX(pb + 8);
      if (layer == NL - 1) {
        const rsrc_t rOUT = __builtin_amdgcn_make_buffer_rsrc((void*)out, 0, SEQ * DM * 2, 0x00020000);
        const u32x4 f0 = pack8<0>(xs), f1 = pack8<1>(xs);
        int ln = lane; PIN(ln);
        const int row = (ln & 31) * 896, up = ln >> 5;
        const int ob = (tb * 4 + tx) * 32 * 896 + 7 * pair * 16;
        st128<0>(f0, rOUT, row + up * 16, ob + 5 * kh * 16);
        st128<0>(f1, rOUT, row + up * OOB, ob + (2 + 2 * kh) * 16);
        __builtin_amdgcn_raw_buffer_store_b64(lo2(f1), rOUT, row + (1 - up) * OOB, ob + 3 * 16 + kh * 8, 0);
      } else {
        const u32x4 f0 = pack8<0>(xs);
        store_x0(f0, WS_XO + (2 * layer + 1) * XO_B);
        u32x4 f1 = pack8<1>(xs);
        f1[2] &= lomask; f1[3] &= lomask;
        float s1 = 0.f, s2 = 0.f, t1 = 0.f, t2 = 0.f;
#pragma unroll
        for (int j = 0; j < 4; ++j) { const unsigned u = f0[j], v = f1[j]; s1 = dot2(u, ONES, s1); s2 = dot2(u, u, s2); t1 = dot2(v, ONES, t1); t2 = dot2(v, v, t2); }
        s1 += t1; s2 += t2;
        red2<false>(s1, s2);
        f1[2] |= ~lomask & F2U(s1); f1[3] |= ~lomask & F2U(s2);
        store_x1(f1, WS_XO + (2 * layer + 1) * XO_B);
        CALS(3 * layer + 2);
        STAMPX(pb + 9);
      }
      asm volatile("" : "=v"(ak), "=v"(av), "=v"(rstd), "=v"(mf), "=v"(phoff));
#pragma unroll
      for (int i = 14; i < 28; ++i) asm volatile("" : "=v"(xf[i]));
    }
    STAMP(61); RSTAMP(63);
    TOT1
    if (b == 0 && w == 0) __builtin_amdgcn_raw_buffer_store_b64((u32x2){(unsigned)(epoch + 1), (unsigned)((epoch + 1) >> 32)}, rWS, WS_EPOCH, 0, SC1);
  } else {
    STAMPX(53);
    u32x4 pA[21], pB[21];
    unsigned tch0, tch1;
    asm volatile("s_load_dword %0, %2, 0x0\n\ts_load_dword %1, %3, 0x0" : "=&s"(tch0), "=&s"(tch1)
                 : "s"(w == 0 ? (const void*)ws : w == 1 ? (const void*)wqkvB : w == 2 ? (const void*)woA : (const void*)wphi), "s"(w < 2 ? (const void*)woB : (const void*)out));
    dma_x0(0, 4);
    unsigned warm0;
    {
      u64 pc; asm volatile("s_getpc_b64 %0" : "=s"(pc));
      const rsrc_t rs = __builtin_amdgcn_make_buffer_rsrc(w == 3 ? (void*)(pc & ~127ull) : (void*)wqkvA, 0, w == 3 ? CODE_B : DM * 3 * DM * 2, 0x00020000);
      const int n = w == 3 ? 9 : 13;
      warm0 = __builtin_amdgcn_raw_buffer_load_b32(rs, lane < n ? (lane + (w == 3 ? (b >> 3) : b * 3 + w) * n) * 128 : OOB, 0, 0);
      SB();
    }
#pragma unroll
    for (int i = 0; i < 7; ++i) { mkrow(i); PIN(krow[i]); }
    lptr trb = make_trb(); PIN(trb);
    u32x4 id0 = ident_frag(0, r, hh); PIN(id0);
    SB();
    dma_x0(4, 28);
#pragma unroll
    for (int i = 0; i < NV0; ++i) pA[14 + i] = ldw(rWA, i, 2688, 1792);
    STAMPX(54);
    asm volatile("s_waitcnt vmcnt(%2) lgkmcnt(0)" :: "s"(tch0), "s"(tch1), "n"(NV0) : "memory");
    const u32x4 sv = {SENT, SENT, SENT, SENT};
    auto clean_kv = [&](int layer) { if (layer < NL) st128<SC1>(sv, rWS, lane * 16, WS_MID + (h * 4 + layer) * MID_B + (tb * 4 + w) * 1024); };
    auto clean_o = [&](int layer) { if (layer < NL) store_o(sv, sv, WS_XO + (2 * layer) * XO_B); };
    auto clean_x = [&](int layer) { if (layer < NL - 1) { store_x0(sv, WS_XO + (2 * layer + 1) * XO_B); store_x1(sv, WS_XO + (2 * layer + 1) * XO_B); } };
    unsigned r0 = (unsigned)__builtin_amdgcn_s_memrealtime(), c0 = (unsigned)__builtin_amdgcn_s_memtime(), tann = 0;
    auto mark = [&](int cyc) {
      const unsigned r1 = (unsigned)__builtin_amdgcn_s_memrealtime(), c1 = (unsigned)__builtin_amdgcn_s_memtime();
      tann = r1 + LAND + (unsigned)((float)cyc * (float)(r1 - r0) * __builtin_amdgcn_rcpf((float)(c1 - c0)));
      r0 = r1; c0 = c1;
    };
    auto push = [&](int hop, unsigned voff) {
      CALA(hop);
      __builtin_amdgcn_raw_buffer_store_b64((u32x2){tag[0], (tag[1] & 0xfff00000u) | (tann & 0xfffffu)}, rWS, voff, WS_FLAGS + hop * HOP_B, SC1);
    };
    auto push_grp = [&](int hop) { push(hop, lane < 16 ? (tb * 16 + lane) * 128 + h * 8 : OOB); };
    auto push_mid = [&](int layer) {
      if (layer & 1) push(3 * layer, (lane < 2 && tb + 1 + lane < 16) ? ((tb + 1 + lane) * 16 + h) * 128 + lane * 8 : OOB);
      else push(3 * layer, lane < 16 ? (lane * 16 + h) * 128 + tb * 8 : OOB);
    };
    const rsrc_t rWB = __builtin_amdgcn_make_buffer_rsrc((void*)wqkvB, 0, DM * 3 * DM * 2, 0x00020000);
    const rsrc_t rOA = __builtin_amdgcn_make_buffer_rsrc((void*)woA, 0, DM * DM * 2, 0x00020000);
    const rsrc_t rOB = __builtin_amdgcn_make_buffer_rsrc((void*)woB, 0, DM * DM * 2, 0x00020000);
    auto spin = [&](int k, unsigned n) { while (__builtin_amdgcn_readfirstlane(cnt[k]) < n) __builtin_amdgcn_s_sleep(1); asm volatile("" ::: "memory"); };
    auto wait_h = [&](int hop, int mask) {
      if (w == 0) { if (mask >= 0) poll(hop, mask); cnt[1] = hop + 1; }
      else while (__builtin_amdgcn_readfirstlane(cnt[1]) != hop + 1) { }
      asm volatile("" ::: "memory");
    };
    const unsigned pkb = WS_PK + h * 262144;
    auto ldp = [&](unsigned off) { const u32x4 v = __builtin_amdgcn_raw_buffer_load_b128(rWS, lane * 16, pkb + off + w * 1024, SC1); SB(); return v; };
    STAMPX(55);
    bar();
#pragma unroll
    for (int i = NV0; i < 7; ++i) pA[14 + i] = ldw(rWA, i, 2688, 1792);
    load_epoch();
    u32x4 pw[4];
    {
      const unsigned act = tb < 14 ? 0u : OOB;
#pragma unroll
      for (int j = 0; j < 2; ++j) {
        const int st = 2 * tb + j, p = (st * 43) >> 7, m = st - 3 * p;
        const int c = st < 24 ? 7 * p + ((0x520 >> (4 * m)) & 15) + hh * (1 + (m & 1)) : 7 * (2 * (st - 24) + hh) + 3;
        pw[j] = __builtin_amdgcn_raw_buffer_load_b128(rWB, ((8 * c + ((lane >> 2) & 7)) * 2688 + sub * 16) | (w < 3 ? act : OOB), col + (w == 2 ? 0 : 896 + 896 * w), 0);
        SB();
      }
      {
        const int n = 4 * tb + w, k2 = n >= 28, i = (n - 28 * k2) >> 1, st = i - 2, p = (st * 43) >> 7, m = st - 3 * p;
        const int c = 28 * k2 + (i < 2 ? 14 * i + 3 + 7 * (n & 1) : 7 * p + ((0x520 >> (4 * m)) & 15) + (n & 1) * (1 + (m & 1)));
        const unsigned vo = (c * 7168 + (lane >> 3) * 896 + 112 * pair + (((lane & 7) ^ ((lane >> 2) & 4)) * 16)) | act;
        pw[2] = __builtin_amdgcn_raw_buffer_load_b128(rOA, vo, 0, 0); SB();
        pw[3] = __builtin_amdgcn_raw_buffer_load_b128(rOB, vo, 0, 0); SB();
      }
    }
    __builtin_amdgcn_s_setprio(3);
    STAMPX(57);
    spin(5, 4);
    put_slab(pA + 14, 0, 1);
#pragma unroll
    for (int j = 0; j < NQ1; ++j) pA[j] = ldw(rWA, j, 2688, 0);
    sum_slab(0, 1, trb);
#pragma unroll
    for (int j = NQ1; j < NQ0; ++j) pA[j] = ldw(rWA, j, 2688, 0);
#pragma unroll
    for (int i = 0; i < 7; ++i) pA[7 + i] = ldl(lds + L_WK + i * 4096 + ht * 16);
    STAMPX(58);
    bar();
    if (w == 3) mark(TKA0);
    __builtin_amdgcn_s_setprio(0);
#pragma unroll
    for (int j = NQ0; j < 7; ++j) pA[j] = ldw(rWA, j, 2688, 0);
    {
      const unsigned keep01 = (odd && sub == 0) ? 0u : ~0u, keep23 = (!odd && sub == 3) ? 0u : ~0u, act = (tb < 14 ? 0u : OOB) | (lane * 16);
#pragma unroll
      for (int j = 0; j < 2; ++j) st128<SC1>(pw[j] & (u32x4){keep01, keep01, keep23, keep23}, rWS, w < 3 ? act : OOB, pkb + w * SLAB + (2 * tb + j) * 1024);
      st128<SC1>(pw[2], rWS, act, pkb + 3 * SLAB + (4 * tb + w) * 1024);
      st128<SC1>(pw[3], rWS, act, pkb + 5 * SLAB + (4 * tb + w) * 1024);
    }
    clean_kv(0); clean_o(0);
    make_tag();
    STAMPX(59);
    first_slab(pA, 0, 2, trb);
    drain();
    arrive(3);
    if (w == 3) { spin(3, 4); push_mid(0); }
    asm volatile("" :: "v"(warm0));
    __builtin_amdgcn_s_setprio(3);
    u32x4 po[14];
    auto swap_wo = [&](bool keep) {
      lptr d = lds + L_WO + ht * 16; PIN(d);
#pragma unroll
      for (int i = 0; i < 14; ++i) { u32x4 t; if (keep) t = ldl(d + i * 4096); *(LAS u32x4*)(d + i * 4096) = po[i]; if (keep) po[i] = t; SB(); }
    };
    STAMPX(60);
#pragma unroll
    for (int layer = 0; layer < NL; ++layer) {
      const int type = layer & 1;
      const int pb = 2 + layer * 14;
      const unsigned mid = WS_MID + (h * 4 + layer) * MID_B;
      if (type == 0) {
        lptr ta0, ta1;
        {
          const int rb = (lane >> 4) & 1, q = (lane >> 2) & 3, p = lane & 3;
          const int rp = 4 * rb + p, lam = (rp & 4) | ((rp & 1) << 1) | ((rp >> 1) & 1);
          ta0 = lds + L_SCR + (8 * hh + q) * 64 + (((lam >> 1) ^ (q >> 1)) * 16) + (lam & 1) * 8;
          ta1 = lds + L_SCR + (8 * hh + q + 4) * 64 + (((lam >> 1) ^ (q >> 1) ^ 2) * 16) + (lam & 1) * 8;
          PIN(ta0); PIN(ta1);
        }
        bar();
        STAMPX(pb + 0);
        {
          auto tr2 = [&](unsigned off) {
            bf16x4 a0 = __builtin_amdgcn_ds_read_tr16_b64_v4bf16((LAS bf16x4*)(ta0 + off));
            bf16x4 a1 = __builtin_amdgcn_ds_read_tr16_b64_v4bf16((LAS bf16x4*)(ta1 + off));
            return __builtin_bit_cast(u32x4, __builtin_shufflevector(a0, a1, 0, 1, 2, 3, 4, 5, 6, 7));
          };
          const unsigned so = (w >> 1) * 8192;
          u32x4 fa[4], fb[4];
          f32x16 kv = Z16;
#pragma unroll
          for (int i = 0; i < 3; ++i) { fa[i] = tr2(so + i * 1024); fb[i] = tr2(16384 + i * 1024); }
          SB();
#pragma unroll
          for (int i = 0; i < 8; ++i) {
            if (i + 3 < 8) { fa[(i + 3) & 3] = tr2(so + (i + 3) * 1024); fb[(i + 3) & 3] = tr2(16384 + (i + 3) * 1024); }
            SB();
            kv = mfma(fa[i & 3], fb[i & 3], kv);
            SB();
          }
          st128<SC1>((w & 1) ? pack8<1>(kv) : pack8<0>(kv), rWS, lane * 16, mid + (tb * 4 + w) * 1024);
          CALS(3 * layer);
        }
        STAMPX(pb + 1);
        if (layer == 0) clean_x(0);
        wait_h(3 * layer, 15);
        STAMPX(pb + 2);
        f32x16 sum;
        for (;;) {
          sum = Z16;
          unsigned m = 0;
          u32x4 pr[16];
          const unsigned g = mid + w * 1024;
#pragma unroll
          for (int i = 0; i < 16; ++i) {
            pr[i] = __builtin_amdgcn_raw_buffer_load_b128(rWS, lane * 16, g + i * 4096, SC1); SB();
            if (i < 15) PACED();
          }
          if (w == 3) { mark(layer ? TOA : TOA0); push_grp(3 * layer + 1); SB(); }
#pragma unroll
          for (int i = 0; i < 16; ++i) {
            if (layer == 0 && i < NPO) po[i] = ldp(3 * SLAB + i * 4096);
            sum = mfma(id0, pr[i], sum); m = max4(m, pr[i]); PIN(m); SB();
          }
          if (!__any(m == SENT) || budget == 0) break;
          --budget; RET(layer * 3 + 1);
          asm volatile("" ::: "memory");
        }
        *(LAS u32x4*)(lds + L_KVF + w * 1024 + lane * 16) = pack8<0>(sum);
        STAMPX(pb + 3);
        bar();
        if (layer == 0) {
#pragma unroll
          for (int i = NPO; i < 14; ++i) po[i] = ldp(3 * SLAB + i * 4096);
        } else __builtin_amdgcn_s_sleep(5);
      } else {
        gather_all(layer, true);
        STAMPX(pb + 0);
        u32x4 t[8];
        wait_h(3 * layer, tb >= 2 ? 1 : tb - 1);
        STAMPX(pb + 2);
        if (tb - 2 + (w >> 1) >= 0) {
          const unsigned g = mid + (tb * 4 - 8 + 2 * w) * 4096;
          for (;;) {
            unsigned c[2] = {0u, 0u};
#pragma unroll
            for (int i = 0; i < 8; ++i) { t[i] = __builtin_amdgcn_raw_buffer_load_b128(rWS, lane * 16 + (i & 3) * 1024, g + (i >> 2) * 4096, SC1); SB(); if (i < 7) PACED(); }
#pragma unroll
            for (int i = 0; i < 8; ++i) {
              c[i >> 2] = add4(c[i >> 2], t[i]); PIN(c[i >> 2]);
              if ((i & 3) == 1) t[i][3] &= (unsigned)hh - 1u;
              *(LAS u32x4*)(lds + L_SCR + 2 * w * 4096 + i * 1024 + lane * 16) = t[i];
              SB();
            }
            const auto cs = __builtin_amdgcn_permlane32_swap(c[0], c[1], false, false);
            if (__all(cs[0] + cs[1] == tag[0] + layer) || budget == 0) break;
            --budget; RET(layer * 3 + 1);
            __builtin_amdgcn_s_sleep(8);
            asm volatile("" ::: "memory");
          }
        } else {
#pragma unroll
          for (int i = 0; i < 8; ++i)
            *(LAS u32x4*)(lds + L_SCR + 2 * w * 4096 + i * 1024 + lane * 16) = (u32x4){0u, 0u, ((i & 3) == 1 && hh) ? 0xff80u : 0u, 0u};
        }
        drain();
        STAMPX(pb + 3);
        bar();
        if (w == 3) { mark(layer == 1 ? TOB1 : TOB); push_grp(3 * layer + 1); }
        __builtin_amdgcn_s_setprio(0);
        if (layer == 1) {
          clean_x(1); clean_kv(2);
#pragma unroll
          for (int i = 0; i < 14; ++i) po[i] = ldp(5 * SLAB + i * 4096);
          swap_wo(true);
          drain();
          spin(4, 4);
        }
      }
      __builtin_amdgcn_s_setprio(0);
      if (layer == 0) {
        swap_wo(false);
      } else if (layer < NL - 1) {
        write_slab((type == 0 ? pB : pA) + 7, L_WK);
      }
      STAMPX(pb + 4);
      bar();
      if (w == 3 && layer < NL - 1) { mark(layer == 2 ? TX2 : layer ? TX : TX0); push_grp(3 * layer + 2); }
      STAMPX(pb + 5);
      if (layer > 0 && layer < NL - 1) { __builtin_amdgcn_s_sleep(WSL); write_slab((type == 0 ? pB : pA) + 14, L_WV); }
      if (layer == 0) {
        __builtin_amdgcn_s_sleep(12);
#pragma unroll
        for (int i = 0; i < 21; ++i) pB[(i + 7) % 21] = ldp(i * 4096);
      }
      bar();
      if (layer < NL - 1) {
        __builtin_amdgcn_s_sleep(SWS);
        if (layer == 0) { write_slab(pB + 7, L_WK); write_slab(pB + 14, L_WV); sum_slab(1, 0, trb); sum_slab(1, 1, trb); }
        else swap_wo(layer == 1);
        bar();
        __builtin_amdgcn_s_sleep(WSL);
        write_slab(type == 0 ? pB : pA, L_WQ);
        if (layer == 0) sum_slab(1, 2, trb);
        arrive(0);
        while (!__all(cnt[8 + (lane & 3)] == layer + 1u)) __builtin_amdgcn_s_sleep(1);
        if (w == 3) { mark(layer == 0 ? TKB1 : layer == 1 ? TKA : TKB); push_mid(layer + 1); }
        __builtin_amdgcn_s_sleep(CSL);
        clean_o(layer + 1);
        if (layer == 1) { clean_x(2); drain(); }
        __builtin_amdgcn_s_setprio(3);
      }
    }
  }
}
}

extern "C" void kernel_launch(void* const* d_in, const int* in_sizes, int n_in, void* d_out, int out_size, void* d_ws, size_t ws_size, hipStream_t stream) {
  (void)hipFuncSetAttribute((const void*)fused_kernel, hipFuncAttributeMaxDynamicSharedMemorySize, L_END);
  fused_kernel<<<256, 512, L_END, stream>>>((const u16*)d_in[0], (const u16*)d_in[1], (const u16*)d_in[2], (const u16*)d_in[3],
                                            (const u16*)d_in[5], (const u16*)d_in[6], (u16*)d_out, (unsigned char*)d_ws);
}
```

```cpp
#define NL 4
#include <hip/hip_runtime.h>
#include <hip/hip_bf16.h>

typedef unsigned short u16;
typedef unsigned long long u64;
typedef __bf16 bf16x8 __attribute__((ext_vector_type(8)));
typedef __bf16 bf16x4 __attribute__((ext_vector_type(4)));
typedef __bf16 bf16x2 __attribute__((ext_vector_type(2)));
typedef float f32x16 __attribute__((ext_vector_type(16)));
typedef float f32x4 __attribute__((ext_vector_type(4)));
typedef float f32x2 __attribute__((ext_vector_type(2)));
typedef unsigned u32x4 __attribute__((ext_vector_type(4)));
typedef unsigned u32x2 __attribute__((ext_vector_type(2)));
#define LAS __attribute__((address_space(3)))
#define GAS __attribute__((address_space(1)))
#define DI __device__ __forceinline__
#define SB() __builtin_amdgcn_sched_barrier(0)
#define PIN(x) asm volatile("" : "+v"(x))
typedef __amdgpu_buffer_rsrc_t rsrc_t;
typedef LAS unsigned char* lptr;

namespace {
constexpr int SEQ = 2048, DM = 448;
constexpr int SC1 = 16;
constexpr unsigned WS_EPOCH = 0;
constexpr unsigned WS_FLAGS = 4096;
constexpr unsigned HOP_B = 256 * 128;
constexpr unsigned TILE_B = 32768;
constexpr unsigned XO_B = 4 * TILE_B;
constexpr unsigned WS_XO = 1u << 20;
constexpr unsigned MID_B = 64 * 4096;
constexpr unsigned WS_MID = WS_XO + 16 * 8 * XO_B;
constexpr unsigned WS_PK = WS_MID + 64 * MID_B;
constexpr unsigned WS_PROF = WS_PK + 16 * 262144;
constexpr unsigned WS_END = WS_PROF + 256 * 2048;
constexpr unsigned SLAB = 448 * 64;
constexpr unsigned L_WK = 0, L_WV = SLAB, L_WQ = 2 * SLAB, L_WO = 3 * SLAB;
constexpr unsigned L_SCR = 0;
constexpr unsigned L_X0 = SLAB;
constexpr unsigned L_KVF = 5 * SLAB;
constexpr unsigned L_MK = L_KVF + 4096;
constexpr unsigned L_WPH = L_MK + 2048;
constexpr unsigned L_CS = L_WPH + 2048;
constexpr unsigned L_SYNC = L_CS + 6144;
constexpr unsigned L_PROF = L_SYNC + 64;
constexpr unsigned L_END = L_PROF + 2048;
static_assert(L_END <= 163840 && L_X0 + 4 * SLAB <= L_KVF, "lds");
constexpr unsigned SPIN_CAP = 1u << 15;
constexpr unsigned SENT = 0xffffffffu;
constexpr unsigned OOB = 0x7ff00000u;
constexpr unsigned ONES = 0x3f803f80u;
constexpr unsigned CODE_B = 36 * 1024;
constexpr float L2E = 1.4426950408889634f;

DI f32x16 mfma(u32x4 a, u32x4 b, f32x16 c) {
  return __builtin_amdgcn_mfma_f32_32x32x16_bf16(__builtin_bit_cast(bf16x8, a), __builtin_bit_cast(bf16x8, b), c, 0, 0, 0);
}
DI unsigned pk2(float a, float b) { f32x2 t = {a, b}; return __builtin_bit_cast(unsigned, __builtin_convertvector(t, bf16x2)); }
template <int S_> DI u32x4 pack8(const f32x16& c) {
  return (u32x4){pk2(c[8 * S_], c[8 * S_ + 1]), pk2(c[8 * S_ + 2], c[8 * S_ + 3]), pk2(c[8 * S_ + 4], c[8 * S_ + 5]), pk2(c[8 * S_ + 6], c[8 * S_ + 7])};
}
DI u32x4 cat(u32x2 a, u32x2 b) { return (u32x4){a[0], a[1], b[0], b[1]}; }
DI u32x2 lo2(u32x4 v) { return (u32x2){v[0], v[1]}; }
DI unsigned umax3(unsigned a, unsigned b, unsigned c) { return max(max(a, b), c); }
DI unsigned max4(unsigned m, u32x4 v) { return umax3(umax3(m, v[0], v[1]), v[2], v[3]); }
DI unsigned add4(unsigned m, u32x4 v) { return (m + v[0] + v[1]) + v[2] + v[3]; }
DI unsigned xhalf_addu(unsigned v) { auto r = __builtin_amdgcn_permlane32_swap(v, v, false, false); return r[0] + r[1]; }
DI float fmax3(float a, float b, float c) { return fmaxf(fmaxf(a, b), c); }
template <int CTRL> DI float dppadd(float v) { return v + __builtin_bit_cast(float, __builtin_amdgcn_update_dpp(0, __builtin_bit_cast(int, v), CTRL, 0xf, 0xf, true)); }
DI float fmin3(float a, float b, float c) { return fminf(fminf(a, b), c); }
DI f32x16 splat(float v) { f32x16 z; _Pragma("unroll") for (int i = 0; i < 16; ++i) z[i] = v; return z; }
#define Z16 splat(0.f)
DI u32x4 trfrag(lptr p) {
  bf16x4 a0 = __builtin_amdgcn_ds_read_tr16_b64_v4bf16((LAS bf16x4*)p);
  bf16x4 a1 = __builtin_amdgcn_ds_read_tr16_b64_v4bf16((LAS bf16x4*)(p + 256));
  return __builtin_bit_cast(u32x4, __builtin_shufflevector(a0, a1, 0, 1, 2, 3, 4, 5, 6, 7));
}
DI u32x4 ldl(lptr p) { return *(LAS u32x4*)p; }
DI float U2F(unsigned u) { return __uint_as_float(u); }
DI unsigned F2U(float f) { return __float_as_uint(f); }
template <bool MX> DI void red2(float& a, float& b) {
  auto r = __builtin_amdgcn_permlane32_swap(F2U(a), F2U(b), false, false);
  const float t = MX ? fmaxf(U2F(r[0]), U2F(r[1])) : U2F(r[0]) + U2F(r[1]);
  r = __builtin_amdgcn_permlane32_swap(F2U(t), F2U(t), false, false);
  a = U2F(r[0]); b = U2F(r[1]);
}
DI float xhalf_max(float v) { auto r = __builtin_amdgcn_permlane32_swap(F2U(v), F2U(v), false, false); return fmaxf(U2F(r[0]), U2F(r[1])); }
DI float upper_val(float v) { auto r = __builtin_amdgcn_permlane32_swap(F2U(v), F2U(v), false, false); return U2F(r[1]); }
DI float dot2(unsigned u, unsigned v, float c) { return __builtin_amdgcn_fdot2_f32_bf16(__builtin_bit_cast(bf16x2, u), __builtin_bit_cast(bf16x2, v), c, false); }
DI float bflo(unsigned u) { return U2F(u << 16); }
DI float bfhi(unsigned u) { return U2F(u & 0xffff0000u); }
DI void split2(float v, unsigned& hi, unsigned& lo) { hi = pk2(v, v) & 0xffffu; lo = pk2(v - bflo(hi), 0.f) & 0xffffu; }
template <int AUX> DI void st128(u32x4 v, rsrc_t rs, unsigned voff, unsigned soff) {
  SB();
  __builtin_amdgcn_raw_buffer_store_b128(v, rs, voff, soff, AUX);
  SB();
  asm volatile("s_nop 1" :: "v"(v));
  SB();
}
DI void drain() { asm volatile("s_waitcnt vmcnt(0)" ::: "memory"); }
DI void bar() { SB(); asm volatile("s_waitcnt lgkmcnt(0)\n\ts_barrier" ::: "memory"); SB(); }
constexpr int chunk_of(int s, int hh) { return s < 24 ? 7 * (s / 3) + (s % 3 == 0 ? hh : (s % 3 == 1 ? 2 + 2 * hh : 5 + hh)) : 7 * (2 * (s - 24) + hh) + 3; }
constexpr int ord(int i) { return i < 4 ? 24 + i : i - 4; }

#define STAMP(i) do { } while (0)
#define RET(i) do { } while (0)
#define RSTAMP(i) do { } while (0)
#define STAMPX(i) STAMP(i)
#define CALS(hop) do { } while (0)
#define CALA(hop) do { } while (0)
#define TOT0
#define TOT1
#define DUMP(buf, acc, part, sc) do { } while (0)

DI void dual_softmax(const f32x16& p, float c, float wtail, u32x4 (&fp)[2], u32x4 (&fm)[2]) {
  float mx = fmax3(p[0], p[1], p[2]), mn = fmax3(-p[0], -p[1], -p[2]);
#pragma unroll
  for (int i = 3; i < 15; i += 2) { mx = fmax3(mx, p[i], p[i + 1]); mn = fmax3(mn, -p[i], -p[i + 1]); }
  mx = fmax3(mx, p[15], p[15]); mn = fmax3(mn, -p[15], -p[15]);
  red2<true>(mx, mn);
  const float a = -c * mx, b = -c * mn;
  float nc = -c; PIN(nc);
  f32x16 ep = p * c + a, em = p * nc + b;
  SB();
#pragma unroll
  for (int i = 0; i < 16; ++i) ep[i] = __builtin_amdgcn_exp2f(ep[i]);
#pragma unroll
  for (int i = 0; i < 16; ++i) em[i] = __builtin_amdgcn_exp2f(em[i]);
  f32x2 sp = {ep[0], ep[1]}, sm = {em[0], em[1]};
#pragma unroll
  for (int i = 2; i < 12; i += 2) { sp += (f32x2){ep[i], ep[i + 1]}; sm += (f32x2){em[i], em[i + 1]}; }
  sp += wtail * ((f32x2){ep[12], ep[13]} + (f32x2){ep[14], ep[15]});
  sm += wtail * ((f32x2){em[12], em[13]} + (f32x2){em[14], em[15]});
  float u = sp[0] + sp[1], v = sm[0] + sm[1];
  red2<false>(u, v);
  ep *= __builtin_amdgcn_rcpf(u); em *= __builtin_amdgcn_rcpf(v);
  fp[0] = pack8<0>(ep); fp[1] = pack8<1>(ep); fm[0] = pack8<0>(em); fm[1] = pack8<1>(em);
}
DI u32x4 ident_frag(int s, int r, int hh) {
  const unsigned val = 0x3f80u << (16 * (r & 1));
  const int idx = (((r >> 2) ^ hh) & 1) * 8 + (r >> 4) * 4 + ((r >> 2) & 2) + ((r >> 1) & 1) - 4 * s;
  return (u32x4){idx == 0 ? val : 0u, idx == 1 ? val : 0u, idx == 2 ? val : 0u, idx == 3 ? val : 0u};
}
#define CHK1 3
#define CHK2 12
template <typename S, typename A> DI void steps28(S step, A abort) {
#pragma unroll
  for (int i = 0; i <= CHK1; ++i) step(i);
  if (abort()) return;
#pragma unroll
  for (int i = CHK1 + 1; i < 28; ++i) step(i);
}
template <int PD, typename F, typename G, typename A> DI void gemm1(f32x16& acc, lptr wb, const u32x4 (&xf)[28], F pre, G post, A abort) {
  u32x4 wr[PD];
#pragma unroll
  for (int i = 0; i < PD - 1; ++i) wr[i] = trfrag(wb + ord(i) * 1024);
  SB();
  steps28([&](int i) __attribute__((always_inline)) {
    pre(i);
    if (i + PD - 1 < 28) wr[(i + PD - 1) % PD] = trfrag(wb + ord(i + PD - 1) * 1024);
    SB();
    acc = mfma(wr[i % PD], xf[i], acc);
    SB();
    post(i);
    SB();
  }, abort);
}
template <typename H, typename F, typename G, typename A>
DI void gemm2(f32x16& ak, f32x16& av, lptr bk, const u32x4 (&xf)[28], H head, F pre, G post, A abort) {
  u32x4 wk[3], wv[3];
  head([&](int u) {
    if (u == 0) wv[0] = trfrag(bk + SLAB + ord(0) * 1024);
    if (u == 1) wk[0] = trfrag(bk + ord(0) * 1024);
    if (u == 2) wv[1] = trfrag(bk + SLAB + ord(1) * 1024);
    if (u == 3) wk[1] = trfrag(bk + ord(1) * 1024);
    SB();
  });
  steps28([&](int i) __attribute__((always_inline)) {
    pre(i);
    if (i + 2 < 28) wv[(i + 2) % 3] = trfrag(bk + SLAB + ord(i + 2) * 1024);
    SB();
    av = mfma(wv[i % 3], xf[i], av);
    SB();
    if (i + 2 < 28) wk[(i + 2) % 3] = trfrag(bk + ord(i + 2) * 1024);
    post(i, 0);
    SB();
    ak = mfma(wk[i % 3], xf[i], ak);
    SB();
    post(i, 1);
    SB();
  }, abort);
}
#define NOAB [] { return false; }
#define EARLYAB [&] { return __builtin_expect(__any(m == sent), 0); }
#define LA 12
#define XH 27
#define VSLEEP 8
#define QSLEEP 28
#define LAO 8
#define XBAR 26
#define DOA 3
#define DOB 6
#define DX 3
#define KVI 6
#define KVB 3
#define NKB 5
#define XM 4
#ifndef NV0
#define NV0 7
#endif
#define NK2 8
#define NK3 9
#define PACE 8
#define LAG 6
#define PACED() do { if (PACE) asm volatile("s_nop %0" :: "n"(PACE)); SB(); } while (0)
#define PSLP 4
#define RDLY 0
#define KDLY 0
#define LAND 40
#define TKA0 4100
#define TKA 3600
#define TKB1 1950
#define TKB 2050
#define TOA0 2050
#define TOA 1850
#define TOB1 2750
#define TOB 2520
#define TX0 3800
#define TX 2850
#define BACK 8
#define NQ0 7
#define NQB 0
#define TX2 3200
#define ND0 10
#define NPO 14
#define SWS 6
#define LMAX 1.8446744e19f
#define CSL 2
#define WSL 4
#define PACE2() do { asm volatile("s_nop %0" :: "n"(PACE - 2)); SB(); } while (0)
#define NQ1 4
#define PDELAY 14

__global__ void __launch_bounds__(512, 2) fused_kernel(const u16* __restrict__ x0, const u16* __restrict__ wqkvA, const u16* __restrict__ wphi,
                                                       const u16* __restrict__ woA, const u16* __restrict__ wqkvB, const u16* __restrict__ woB,
                                                       u16* __restrict__ out, unsigned char* ws) {
  extern __shared__ __attribute__((aligned(16))) unsigned char smem[];
  u64 tstart; asm volatile("s_memrealtime %0\n\ts_waitcnt lgkmcnt(0)" : "=s"(tstart) :: "memory"); SB();
  unsigned probe = 0;
  const lptr lds = (lptr)smem;
  const int tid = threadIdx.x, lane = tid & 63, wave = __builtin_amdgcn_readfirstlane(tid >> 6);
  TOT0
  const int w = wave & 3;
  const int r = lane & 31, hh = lane >> 5;
  const int b = blockIdx.x;
  const int tb = ((b >> 1) & 3) * 4 + (b >> 6), h = (b & 1) * 8 + ((b >> 3) & 7);
  const int me = tb * 16 + h;
  const int odd = h & 1, pair = h >> 1;
  const int T = tb * 4 + w;
  const unsigned TOFF = tb * 8 * XO_B + w * TILE_B;
  const int kh = w & 1, tx = 2 * odd + (w >> 1);
  const unsigned XOFF = tb * 8 * XO_B + tx * TILE_B;
  const rsrc_t rWS = __builtin_amdgcn_make_buffer_rsrc((void*)ws, 0, WS_END, 0x00020000);
  unsigned budget = SPIN_CAP;
  u64 epoch;
  u32x2 tag;
  u32x2 ev;
  auto load_epoch = [&]() { ev = __builtin_amdgcn_raw_buffer_load_b64(rWS, 0, WS_EPOCH, SC1); SB(); };
  auto make_tag = [&]() {
    epoch = (u64)(unsigned)__builtin_amdgcn_readfirstlane(ev[0]) | ((u64)(unsigned)__builtin_amdgcn_readfirstlane(ev[1]) << 32);
    u64 z = epoch + 0x9E3779B97F4A7C15ull;
    z = (z ^ (z >> 30)) * 0xBF58476D1CE4E5B9ull; z = (z ^ (z >> 27)) * 0x94D049BB133111EBull; z ^= z >> 31;
    unsigned t0 = (unsigned)z, t1 = (unsigned)(z >> 32);
    asm volatile("" : "+s"(t0), "+s"(t1));
    tag = (u32x2){t0, t1};
  };
  volatile LAS unsigned* const cnt = (volatile LAS unsigned*)(lds + L_SYNC);
  auto arrive = [&](int k) { if (lane == 0) asm volatile("ds_add_u32 %0, %1" :: "v"(lds + L_SYNC + 4 * k), "v"(1u) : "memory"); };
  auto gather_all = [&](int layer, bool nap) {
    while (__builtin_amdgcn_readfirstlane(*cnt) < 8u * layer + 4u) { if (nap) __builtin_amdgcn_s_sleep(2); }
    asm volatile("" ::: "memory");
  };
  auto poll = [&](int hop, int mask) {
    int ln = lane & mask; PIN(ln);
    u32x2 v;
    for (;;) {
      v = __builtin_amdgcn_raw_buffer_load_b64(rWS, ln * 8, WS_FLAGS + hop * HOP_B + me * 128, SC1);
      asm volatile("" ::: "memory");
      if (__all(v[0] == tag[0] && ((v[1] ^ tag[1]) >> 20) == 0)) break;
      if (budget <= 1) break;
      --budget;
      if (PSLP) __builtin_amdgcn_s_sleep(PSLP);
    }
    const unsigned t = v[1] << 12;
    for (int n = 0; n < 64; ++n) {
      const unsigned now = (unsigned)__builtin_amdgcn_s_memrealtime() << 12;
      if (!__any((int)(t - now) > 0)) break;
    }
    asm volatile("" ::: "memory");
  };
  auto make_trb = [&]() {
    const int rb = (lane >> 4) & 1, q = (lane >> 2) & 3, p = lane & 3;
    const int rp = 4 * rb + p, lam = (rp & 4) | ((rp & 1) << 1) | ((rp >> 1) & 1);
    const int pc = ((odd ? 0x01327654 : 0x76543210) >> (4 * lam)) & 7;
    return lds + (8 * hh + q) * 64 + pc * 8;
  };
  auto store_x0 = [&](u32x4 f0, unsigned base) { int ln = lane; PIN(ln); st128<SC1>(f0, rWS, ln * 16, base + XOFF + (3 * pair + 2 * kh) * 1024); };
  auto store_x1 = [&](u32x4 f1, unsigned base) {
    int ln = lane; PIN(ln);
    st128<SC1>(f1, rWS, ln * 16 + __umul24(ln >> 5, 24576 - 512 - 1024 - pair * 2048), base + XOFF + (3 * pair + 1) * 1024 + kh * 512);
  };
  auto store_o = [&](u32x4 f0, u32x4 f1, unsigned base) {
    int ln = lane; PIN(ln);
    st128<SC1>(f0, rWS, ln * 16, base + TOFF + (3 * pair + 2 * odd) * 1024);
    st128<SC1>(f1, rWS, ln * 16 + (ln >> 5) * OOB, base + TOFF + (3 * pair + 1) * 1024 + odd * 512);
    __builtin_amdgcn_raw_buffer_store_b64(lo2(f1), rWS, ln * 8 + (1 - (ln >> 5)) * OOB, base + TOFF + 24576 - 256 + h * 256, SC1);
  };
  const int ht = tid & 255, sub = lane & 3;
  const int col = h * 56 - odd * 8;
  int krow[7];
  auto mkrow = [&](int i) {
    const int s = 4 * i + w;
    const int p = (s * 43) >> 7, m = s - 3 * p;
    const int c = i < 6 ? 7 * p + ((0x520 >> (4 * m)) & 15) + hh * (1 + (m & 1)) : 7 * (2 * w + hh) + 3;
    krow[i] = 8 * c + ((lane >> 2) & 7);
  };
  auto ldw = [&](rsrc_t rs, int i, int stride, int off) { int k = krow[i]; PIN(k); const u32x4 v = __builtin_amdgcn_raw_buffer_load_b128(rs, k * stride + sub * 16, col + off, 0); SB(); return v; };
  auto write_slab = [&](const u32x4* p, unsigned off) {
    lptr d = lds + off + ht * 16; PIN(d);
#pragma unroll
    for (int i = 0; i < 7; ++i) *(LAS u32x4*)(d + i * 4096) = p[i];
  };
  auto put_slab = [&](u32x4* p, int type, int part) {
    const unsigned keep01 = (odd && sub == 0) ? 0u : ~0u, keep23 = (!odd && sub == 3) ? 0u : ~0u;
#pragma unroll
    for (int i = 0; i < 7; ++i) {
      p[i][0] &= keep01; p[i][1] &= keep01; p[i][2] &= keep23; p[i][3] &= keep23;
      if (type == 0 && part == 0) { p[i][0] |= ~keep01 & 0x3f80u; p[i][2] |= ~keep23 & 0x3f80u; }
    }
    write_slab(p, part * SLAB);
  };
  auto sum_slab = [&](int type, int part, lptr trb) {
    lptr wb = trb + part * SLAB + w * 1024; PIN(wb);
    u32x4 wr[7];
#pragma unroll
    for (int i = 0; i < 7; ++i) wr[i] = trfrag(wb + i * 4096);
    f32x16 a = Z16;
#pragma unroll
    for (int i = 0; i < 7; ++i) a = mfma((u32x4){ONES, ONES, ONES, ONES}, wr[i], a);
    unsigned hi, lo; split2(a[0], hi, lo);
    if (hh == (w >> 1)) *(LAS u32x2*)(lds + L_CS + type * 3072 + part * 1024 + lane * 16 + (w & 1) * 8) = (u32x2){hi | (lo << 16), hi};
  };
  auto first_slab = [&](u32x4* p, int type, int part, lptr trb) { put_slab(p, type, part); sum_slab(type, part, trb); };
  const rsrc_t rWA = __builtin_amdgcn_make_buffer_rsrc((void*)wqkvA, 0, DM * 3 * DM * 2, 0x00020000);
  auto dma_x0 = [&](int i0, int i1) __attribute__((always_inline)) {
    const rsrc_t rX0 = __builtin_amdgcn_make_buffer_rsrc((void*)x0, 0, SEQ * DM * 2, 0x00020000);
    const int o0 = (lane >> 3) * 896 + (((lane & 7) ^ (lane >> 4)) * 16), o1 = o0 ^ 64;
#pragma unroll
    for (int i = i0; i < i1; ++i) {
      __builtin_amdgcn_raw_ptr_buffer_load_lds(rX0, (LAS void*)(lds + L_X0 + w * SLAB + i * 1024), 16, (i & 1) ? o1 : o0, (T * 32 + (i & 3) * 8) * 896 + (i >> 2) * 128, 0, 0);
      SB();
    }
  };

  if (wave < 4) {
    RSTAMP(62); STAMP(0);
    u32x4 xf[28];
    f32x16 xs, ak, av;
    float rstd;
    u32x4 mf;
    auto stats = [&](float s1, float s2) {
      const float mean = s1 * (1.0f / DM);
      rstd = __builtin_amdgcn_rsqf(fmaxf(s2 * (1.0f / DM) - mean * mean, 0.f) + 1e-5f);
      unsigned hi, lo; split2(-mean, hi, lo);
      mf = (u32x4){hi | (hi << 16), lo, hi | (hi << 16), lo};
      PIN(rstd); PIN(mf);
    };
    lptr trb;
    u32x4 id0, id1;
    float wtail;
    unsigned lomask, phoff;
    {
      __builtin_amdgcn_s_setprio(3);
#pragma unroll
      for (int i = 0; i < 7; ++i) { mkrow(i); xf[21 + i] = ldw(rWA, i, 2688, 896); }
      STAMPX(54);
      trb = make_trb();
      id0 = ident_frag(0, r, hh); id1 = ident_frag(1, r, hh);
      wtail = hh ? 0.f : 1.f; lomask = hh ? 0u : ~0u;
      PIN(trb); PIN(id0); PIN(id1); PIN(wtail); PIN(lomask);
      if (w >= 2) {
        u32x4 mk;
#pragma unroll
        for (int i = 0; i < 4; ++i) { const int k0 = ((2 * i) & 3) + 8 * (i >> 1) + 4 * hh + 16 * (w & 1); mk[i] = (k0 > r ? 0xffffu : 0u) | (k0 + 1 > r ? 0xffff0000u : 0u); }
        *(LAS u32x4*)(lds + L_MK + (w & 1) * 1024 + lane * 16) = mk;
      }
      if (w == 0) { cnt[0] = 0u; cnt[1] = 0u; cnt[2] = 0u; cnt[3] = 0u; cnt[4] = 0u; cnt[5] = 0u; cnt[6] = 0u; }
      cnt[8 + w] = 0u;
      const int lam = 4 * (w >> 1) + 2 * hh + (w & 1);
      const int pc = ((odd ? 0x01327654 : 0x76543210) >> (4 * lam)) & 7;
      unsigned off = lam == 7 ? OOB : ((h * 28 + 4 * pc - 4 * odd) * 28 + (r < 28 ? r : r - 8)) * 2;
      phoff = off; PIN(phoff);
      __builtin_amdgcn_s_setprio(0);
    }
    tag = (u32x2){0u, 0u}; epoch = 0;
    asm volatile("" : "=v"(ak), "=v"(av), "=v"(rstd), "=v"(mf), "=v"(xs));
#pragma unroll
    for (int i = 0; i < 21; ++i) asm volatile("" : "=v"(xf[i]));
    unsigned sent = SENT;
    auto release = [&](int hop) { if (w == 0) poll(hop, 15); bar(); };
    for (int layer = 0; layer < NL; ++layer) {
      const int type = layer & 1;
      const int pb = 2 + layer * 14;
      if (layer > 0) {
        unsigned base = WS_XO + (2 * layer - 1) * XO_B + TOFF;
        int v16 = lane * 16, vs = v16 + (v16 & 512);
        lptr bk = trb + L_WK;
        PIN(vs);
        release(3 * layer - 1);
        STAMP(pb + 0);
        for (;;) {
          PIN(v16); PIN(vs); PIN(bk); asm volatile("" : "+s"(base));
          u32x4 sp[8];
          auto ld = [&](int s) { xf[4 + s] = __builtin_amdgcn_raw_buffer_load_b128(rWS, v16 + (s & 3) * 1024, base + (s >> 2) * 4096, SC1); SB(); };
          unsigned m = 1;
          ak = Z16; av = Z16;
          float s1 = 0.f, s2 = 0.f, mean;
          unsigned hi;
          u32x4 ck, cv;
          gemm2(ak, av, bk, xf,
                    [&](auto gap) {
#pragma unroll
                      for (int u = 0; u < 8; ++u) { sp[u] = __builtin_amdgcn_raw_buffer_load_b128(rWS, vs + ((u >> 1) & 1) * 2048 + (u & 1) * 512, base + 24576 + (u >> 2) * 4096, SC1); SB(); gap(u); if (u < 4) PACE2(); else PACED(); }
#pragma unroll
                      for (int s = 0; s < LA - 8; ++s) { ld(s); PACED(); }
                    },
                    [&](int i) {
                      if (i + LA - 8 < 24) ld(i + LA - 8);
                      if (i < 4) { PIN(sp[2 * i]); PIN(sp[2 * i + 1]); xf[i] = cat(lo2(sp[2 * i]), lo2(sp[2 * i + 1])); }
                      if (i == 22) { av = mfma(cv, mf, av); SB(); }
                      if (i == 24) { ak = mfma(ck, mf, ak); SB(); }
                    },
                    [&](int i, int hf) {
                      if (i < 4) m = max4(m, sp[2 * i + hf]); else m = umax3(m, xf[i][2 * hf], xf[i][2 * hf + 1]);
                      PIN(m);
                      if (i < 8) { if (hf) { s2 += U2F(sp[i][3]); PIN(s2); } else { s1 += U2F(sp[i][2]); PIN(s1); } }
                      if (hf) return;
                      if (i == 13) asm volatile("ds_write_b32 %0, %1" :: "v"(lds + L_SYNC + 32 + 4 * w), "v"(layer) : "memory");
                      if (i == 9) { auto t = __builtin_amdgcn_permlane32_swap(F2U(s1), F2U(s2), false, false); s1 = U2F(t[0]) + U2F(t[1]); PIN(s1); }
                      if (i == 10) { auto t = __builtin_amdgcn_permlane32_swap(F2U(s1), F2U(s1), false, false); s1 = U2F(t[0]); s2 = U2F(t[1]); PIN(s1); PIN(s2); }
                      if (i == 14) { mean = s1 * (-1.0f / DM); s2 = s2 * (1.0f / DM); PIN(mean); PIN(s2); }
                      if (i == 15) { s2 = fmaxf(fmaf(-mean, mean, s2), 0.f); PIN(s2); }
                      if (i == 16) { rstd = __builtin_amdgcn_rsqf(s2 + 1e-5f); PIN(rstd); }
                      if (i == 17) { hi = pk2(mean, mean); s1 = mean - bfhi(hi); PIN(hi); PIN(s1); }
                      if (i == 18) { const unsigned lo = pk2(s1, 0.f); mf = (u32x4){hi, lo, hi, lo}; PIN(mf); }
                      if (i == 19) { const lptr cs = lds + L_CS + type * 3072 + v16; ck = ldl(cs); cv = ldl(cs + 1024); }
                    }, EARLYAB);
          if (__builtin_expect(!__any(m == sent), 1)) break;
          if (--budget == 0) sent = 0u;
          RET(layer * 3);
          __builtin_amdgcn_s_sleep(BACK);
          asm volatile("" ::: "memory");
        }
      }
      int first = layer; asm volatile("" : "+s"(first));
      if (first == 0) {
        int ln = lane; PIN(ln);
        const int hh = ln >> 5;
        const unsigned ra = (ln & 31) * 128 + ((ln >> 1) & 7) * 16;
        const lptr stg = lds + L_X0 + w * SLAB;
        auto kof = [](int c) { return (c >> 3) * 4096 + (c & 7) * 16; };
        lptr ad[28];
#pragma unroll
        for (int i = 0; i < 28; ++i) {
          const int k0 = kof(chunk_of(ord(i), 0)), k1 = kof(chunk_of(ord(i), 1));
          ad[i] = stg + (ra ^ k0 ^ (~lomask & (k0 ^ k1))); PIN(ad[i]);
        }
        lptr pab = lds + L_X0 + tx * SLAB + (ra ^ kof(7 * pair + 5 * kh + hh)), pc = lds + L_X0 + tx * SLAB + (ra ^ kof(7 * pair + (hh ? 3 : 2 + 2 * kh))); PIN(pab); PIN(pc);
        SB();
        first_slab(xf + 21, 0, 0, trb);
        STAMPX(53);
        bar();
        STAMPX(55);
#pragma unroll
        for (int i = 0; i < 28; ++i) xf[i] = ldl(ad[i]);
        u32x4 xa = ldl(pab), xc = ldl(pc);
        arrive(5);
        SB();
        float s2a = 0.f, s2b = 0.f;
#pragma unroll
        for (int i = 0; i < 28; ++i) {
          const u32x4 u = xf[i]; s2a = dot2(u[1], u[1], dot2(u[0], u[0], s2a)); s2b = dot2(u[3], u[3], dot2(u[2], u[2], s2b));
          PIN(s2a); PIN(s2b);
          SB();
        }
        const rsrc_t rPH = __builtin_amdgcn_make_buffer_rsrc((void*)wphi, 0, 16 * 28 * 28 * 2, 0x00020000);
        unsigned e[4];
#pragma unroll
        for (int j = 0; j < 4; ++j) e[j] = __builtin_amdgcn_raw_buffer_load_b16(rPH, phoff + j * 56, 0, 0);
        load_epoch();
        STAMP(1);
        u32x4 cc = ldl(lds + L_CS + ln * 16);
        ak = Z16;
        lptr bk = trb + L_WK; PIN(bk);
        gemm1<5>(ak, bk, xf, [](int) {}, [&](int i) {
          if (i == 4) {
            const unsigned sel = hh && kh ? 0u : ~0u;
            xc[0] = (xc[0] & sel) | (xc[2] & ~sel); xc[1] = (xc[1] & sel) | (xc[3] & ~sel); PIN(xc);
          }
          if (i >= 16 && i < 24) { const unsigned u = i < 20 ? xa[i & 3] : xc[i & 3]; xs[2 * (i - 16)] = bflo(u); xs[2 * (i - 16) + 1] = bfhi(u); PIN(xs); }
        }, NOAB);
        asm volatile("" :: "v"(ak));
        *(LAS u32x2*)(lds + L_WPH + (w >> 1) * 1024 + ln * 16 + (w & 1) * 8) = (u32x2){e[0] | (e[1] << 16), e[2] | (e[3] << 16)};
        make_tag();
        STAMPX(57);
        bar();
        STAMP(2);
        const u32x4 cv = ldl(lds + L_CS + 1024 + ln * 16);
        av = Z16;
        gemm1<5>(av, bk + SLAB, xf, [&](int i) { if (i == 8) { ak = mfma(cc, mf, ak); SB(); } if (i == 10) { av = mfma(cv, mf, av); SB(); } }, [&](int i) {
          if (i == 2) {
            float s1 = U2F(F2U(ak[12]) & ~lomask), s2 = s2a + s2b;
            red2<false>(s1, s2);
            stats(s1, s2);
          }
        }, NOAB);
      }
      asm volatile("" ::: "memory");
      arrive(0);
      STAMPX(pb + 1);
      const unsigned mid = WS_MID + (h * 4 + layer) * MID_B;
      int ln16 = lane * 16; PIN(ln16);
      u32x4 wph[2], kf0, kf1;
      if (type == 0) {
        wph[0] = ldl(lds + L_WPH + ln16); wph[1] = ldl(lds + L_WPH + 1024 + ln16);
        DUMP(0, ak, 1, rstd); DUMP(0, av, 2, rstd);
        kf0 = pack8<0>(ak); kf1 = pack8<1>(ak);
        f32x16 p = mfma(wph[0], kf0, Z16);
        p = mfma(wph[1], kf1, p);
        SB();
#pragma unroll
        for (int i = 0; i < 16; ++i) { float t = av[i] * rstd; PIN(t); av[i] = t; }
        u32x4 vb0 = pack8<0>(av), vb1 = pack8<1>(av);
        vb1[2] |= ~lomask & 0x3f80u;
        PIN(vb0); PIN(vb1);
        SB();
        u32x4 fp[2], fm[2];
        dual_softmax(p, rstd * L2E, wtail, fp, fm);
        fp[1][2] &= lomask; fp[1][3] &= lomask; fm[1][2] &= lomask; fm[1][3] &= lomask;
        PIN(fp[0]); PIN(fp[1]); PIN(fm[0]); PIN(fm[1]);
        gather_all(layer, false);
        const int rr = (ln16 >> 4) & 31, pc = (ln16 >> 9) ^ ((rr >> 1) & 3);
        const lptr d0 = lds + L_SCR + w * 2048 + rr * 64 + pc * 16, d1 = lds + L_SCR + w * 2048 + rr * 64 + (pc ^ 2) * 16;
        *(LAS u32x4*)(d0) = fp[0]; *(LAS u32x4*)(d1) = fp[1];
        *(LAS u32x4*)(d0 + 8192) = fm[0]; *(LAS u32x4*)(d1 + 8192) = fm[1];
        *(LAS u32x4*)(d0 + 16384) = vb0; *(LAS u32x4*)(d1 + 16384) = vb1;
        bar();
      } else {
        av *= rstd;
        DUMP(0, av, 2, 1.f);
        u32x4 vb1 = pack8<1>(av);
        vb1[2] |= ~lomask & 0x3f80u;
        f32x16 vt = mfma(pack8<0>(av), id0, Z16);
        vt = mfma(vb1, id1, vt);
        SB();
#pragma unroll
        for (int i = 0; i < 16; ++i) { float t = ak[i] * rstd; PIN(t); ak[i] = t; }
        DUMP(0, ak, 1, 1.f);
        kf0 = pack8<0>(ak); kf1 = pack8<1>(ak);
        const unsigned g = mid + T * 4096;
        const lptr d = lds + L_SCR + (8 + w) * 4096 + ln16;
        st128<SC1>(kf0, rWS, ln16, g);
        const u32x4 vf0 = pack8<0>(vt), vf1 = pack8<1>(vt);
        st128<SC1>(vf0, rWS, ln16 + 2048, g);
        st128<SC1>(vf1, rWS, ln16 + 3072, g);
        {
          u32x4 kc = kf1;
          kc[3] |= ~lomask & (tag[0] + layer - xhalf_addu(add4(add4(0u, kf0), kf1) + add4(add4(0u, vf0), vf1)));
          st128<SC1>(kc, rWS, ln16 + 1024, g);
          CALS(3 * layer);
        }
        gather_all(layer, false);
        *(LAS u32x4*)(d) = kf0; *(LAS u32x4*)(d + 1024) = kf1; *(LAS u32x4*)(d + 2048) = vf0; *(LAS u32x4*)(d + 3072) = vf1;
      }
      STAMPX(pb + 2);
      u32x4 qf0, qf1;
      {
        f32x16 aq = mfma(ldl(lds + L_CS + type * 3072 + 2048 + ln16), mf, Z16);
        lptr bq = trb + L_WQ; PIN(bq);
        gemm1<5>(aq, bq, xf, [](int) {}, [](int) {}, NOAB);
        aq *= type ? rstd * (0.18898223650461361f * L2E) : 1.0f;
        DUMP(0, aq, 0, type ? 1.f : rstd);
        qf0 = pack8<0>(aq); qf1 = pack8<1>(aq);
        qf1[2] |= ~lomask & 0x3f80u;
      }
      f32x16 o;
      if (type == 0) {
        u32x4 fp[2], fm[2];
        {
          f32x16 p = mfma(wph[0], qf0, Z16);
          p = mfma(wph[1], qf1, p);
          dual_softmax(p, rstd * L2E, wtail, fp, fm);
        }
        PIN(fp[0]); PIN(fp[1]); PIN(fm[0]); PIN(fm[1]);
        STAMPX(pb + 3);
        bar();
        STAMP(pb + 4);
        const lptr kvf = lds + L_KVF + ln16;
        const u32x4 a0 = ldl(kvf), a1 = ldl(kvf + 1024), a2 = ldl(kvf + 2048), a3 = ldl(kvf + 3072);
        SB();
        o = mfma(a0, fp[0], Z16);
        o = mfma(a1, fp[1], o);
        o = mfma(a2, fm[0], o);
        o = mfma(a3, fm[1], o);
        o *= __builtin_amdgcn_rcpf(upper_val(o[12]) + 1e-6f);
      } else {
        lptr kv = lds + L_SCR + w * 4096 + ln16; PIN(kv);
        auto ex4 = [&](f32x16& s, int i0) {
#pragma unroll
          for (int i = i0; i < i0 + 4; ++i) s[i] = __builtin_amdgcn_exp2f(s[i]);
          unsigned a = pk2(s[i0], s[i0 + 1]), c = pk2(s[i0 + 2], s[i0 + 3]); PIN(a); PIN(c);
          SB();
          return (u32x2){a, c};
        };
        auto tile_max = [&](int j, int side, float m) {
          f32x16 t = mfma(ldl(kv + j * 4096 + 1024), qf1, mfma(ldl(kv + j * 4096), qf0, Z16));
          int lim = ((ln16 >> 4) & 31) - ((ln16 >> 7) & 4); PIN(lim);
#pragma unroll
          for (int i = 0; i < 16; ++i) { const bool up = (i & 3) + 8 * (i >> 2) > lim; m = fmaxf(m, (side == 0 || up == (side == 1)) ? t[i] : m); }
          return m;
        };
        float m = -__builtin_inff(), l;
        for (int pass = 0;; ++pass) {
          const u32x4 mk0 = ldl(lds + L_MK + ln16), mk1 = ldl(lds + L_MK + 1024 + ln16);
          f32x16 negm;
          {
            const u32x4 v0 = ldl(kv + 8 * 4096 + 2048), v1 = ldl(kv + 8 * 4096 + 3072);
            f32x16 s = mfma(ldl(kv + 8 * 4096 + 1024), qf1, mfma(ldl(kv + 8 * 4096), qf0, Z16));
#pragma unroll
            for (int i = 0; i < 16; ++i) {
              const unsigned k = (i < 8 ? mk0 : mk1)[(i >> 1) & 3], on = (i & 1) ? (unsigned)((int)k >> 31) : (unsigned)((int)(k << 16) >> 31);
              s[i] = U2F((F2U(s[i]) & ~on) | (on & 0xff800000u));
            }
#pragma unroll
            for (int i = 0; i < 16; i += 2) m = fmax3(m, s[i], s[i + 1]);
            m = xhalf_max(m);
            s -= m;
            negm = splat(-m);
            PIN(negm);
            const u32x2 a = ex4(s, 0), c = ex4(s, 4), e = ex4(s, 8), f = ex4(s, 12);
            o = mfma(v1, cat(e, f), mfma(v0, cat(a, c), Z16));
            PIN(o);
          }
          if (pass == 0) {
            STAMPX(pb + 3);
            bar();
            STAMP(pb + 4);
          }
          u32x4 kk[2][2], vv[2][2], pa, pb2;
          f32x16 st[2];
          kk[0][0] = ldl(kv); kk[0][1] = ldl(kv + 1024); kk[1][0] = ldl(kv + 4096); kk[1][1] = ldl(kv + 4096 + 1024);
          SB();
          st[0] = mfma(kk[0][1], qf1, mfma(kk[0][0], qf0, negm));
          SB();
#pragma unroll
          for (int j = 0; j < 8; ++j) {
            f32x16& s = st[j & 1];
            vv[j & 1][0] = ldl(kv + j * 4096 + 2048); vv[j & 1][1] = ldl(kv + j * 4096 + 3072);
            if (j < 6) { kk[j & 1][0] = ldl(kv + (j + 2) * 4096); kk[j & 1][1] = ldl(kv + (j + 2) * 4096 + 1024); }
            SB();
            if (j == 0) { st[1] = mfma(kk[1][0], qf0, negm); SB(); }
            const u32x2 a = ex4(s, 0);
            if (j == 0) st[1] = mfma(kk[1][1], qf1, st[1]); else o = mfma(vv[(j - 1) & 1][0], pa, o);
            SB();
            const u32x2 c = ex4(s, 4);
            if (j == 7) o = mfma(vv[0][1], pb2, o); else if (j > 0) st[(j + 1) & 1] = mfma(kk[(j + 1) & 1][0], qf0, negm);
            SB();
            const u32x2 e = ex4(s, 8);
            if (j == 7) o = mfma(vv[1][0], cat(a, c), o); else if (j > 0) st[(j + 1) & 1] = mfma(kk[(j + 1) & 1][1], qf1, st[(j + 1) & 1]);
            SB();
            const u32x2 f = ex4(s, 12);
            if (j == 7) o = mfma(vv[1][1], cat(e, f), o); else if (j > 0) o = mfma(vv[(j - 1) & 1][1], pb2, o);
            SB();
            pa = cat(a, c); pb2 = cat(e, f);
            if (j == 0) { pa &= mk0; pb2 &= mk1; PIN(pa); PIN(pb2); }
          }
          l = upper_val(o[12]);
          if (pass > 0 || __all(l < LMAX)) break;
          RET(13);
          m = tile_max(0, 1, m);
#pragma unroll 1
          for (int j = 1; j < 8; ++j) m = tile_max(j, 0, m);
        }
        arrive(4);
        o *= __builtin_amdgcn_rcpf(l);
      }
      DUMP(1, o, 0, 1.f);
      store_o(pack8<0>(o), pack8<1>(o), WS_XO + (2 * layer) * XO_B);
      CALS(3 * layer + 1);
      STAMPX(pb + 6);
      lptr bown, both;
      {
        int ln = lane; PIN(ln);
        const int q = (ln >> 2) & 3, rp = ((ln >> 2) & 4) + (ln & 3), lam = (rp & 4) | ((rp & 1) << 1) | ((rp >> 1) & 1);
        const int pe = lam, po = 6 + ((0x01327654 >> (4 * lam)) & 7);
        const lptr rb = lds + L_WO + kh * SLAB + (8 * (ln >> 5) + q) * 128;
        const lptr ae = rb + ((pe * 8) ^ ((q >> 1) * 64)), ao = rb + ((po * 8) ^ ((q >> 1) * 64));
        bown = kh ? ao : ae; both = kh ? ae : ao;
        PIN(bown); PIN(both);
      }
      unsigned base = WS_XO + (2 * layer) * XO_B + XOFF + kh * 12288;
      int v16 = ln16, vs = (v16 >> 1) + ((v16 >> 1) & 256);
      PIN(vs);
      release(3 * layer + 1);
      STAMP(pb + 7);
      f32x16 xn, xo;
      u32x4 wa[4];
        auto tr = [](lptr p) {
          bf16x4 a0 = __builtin_amdgcn_ds_read_tr16_b64_v4bf16((LAS bf16x4*)p), a1 = __builtin_amdgcn_ds_read_tr16_b64_v4bf16((LAS bf16x4*)(p + 512));
          return __builtin_bit_cast(u32x4, __builtin_shufflevector(a0, a1, 0, 1, 2, 3, 4, 5, 6, 7));
        };
      for (;;) {
        PIN(v16); PIN(vs); asm volatile("" : "+s"(base));
        u32x2 sp[4];
        u32x4 wb[4];
#pragma unroll
        for (int u = 0; u < 4; ++u) { sp[u] = __builtin_amdgcn_raw_buffer_load_b64(rWS, vs + (u >> 1) * 1024 + (u & 1) * 256, base + 24576 - kh * 10240, SC1); SB(); if (u & 1) PACED(); }
#pragma unroll
        for (int s = 0; s < LAO; ++s) { xf[2 + s] = __builtin_amdgcn_raw_buffer_load_b128(rWS, v16 + (s & 3) * 1024, base + (s >> 2) * 4096, SC1); SB(); if (s < 3) wb[s] = tr(both + s * 2048); else if (s < 6) wa[s - 3] = tr(bown + (s - 3) * 2048); SB(); if (s < 6) PACE2(); else PACED(); }
        xf[0] = cat(sp[0], sp[1]); xf[1] = cat(sp[2], sp[3]);
        xn = xs; xo = Z16;
        unsigned m = 1;
#pragma unroll
        for (int i = 0; i < 14; ++i) {
          if (i + LAO < 12) { const int s = i + LAO; xf[2 + s] = __builtin_amdgcn_raw_buffer_load_b128(rWS, v16 + (s & 3) * 1024, base + (s >> 2) * 4096, SC1); SB(); }
          if (i + 3 < 14) wb[(i + 3) & 3] = tr(both + (i + 3) * 2048);
          SB();
          xo = mfma(wb[i & 3], xf[i], xo);
          SB();
          m = max4(m, xf[i]); PIN(m);
          if (i >= LAG) {
            wa[(i - LAG + 3) & 3] = tr(bown + (i - LAG + 3) * 2048);
            SB();
            xn = mfma(wa[(i - LAG) & 3], xf[i - LAG], xn);
          }
          SB();
        }
        if (__builtin_expect(!__any(m == sent), 1)) break;
        if (--budget == 0) sent = 0u;
        RET(layer * 3 + 2);
        __builtin_amdgcn_s_sleep(BACK);
        asm volatile("" ::: "memory");
      }
      {
        const lptr ex = lds + L_WQ + ln16;
        f32x4 t[4];
#pragma unroll
        for (int i = 14 - LAG; i < 14; ++i) {
          if (i + 3 < 14) wa[(i + 3) & 3] = tr(bown + (i + 3) * 2048);
          SB();
          xn = mfma(wa[i & 3], xf[i], xn);
          SB();
          if (i == 15 - LAG) {
#pragma unroll
            for (int g = 0; g < 4; ++g) *(LAS f32x4*)(ex + w * 4096 + g * 1024) = (f32x4){xo[4 * g], xo[4 * g + 1], xo[4 * g + 2], xo[4 * g + 3]};
          }
          if (i == 11) {
            bar();
#pragma unroll
            for (int g = 0; g < 4; ++g) t[g] = *(LAS f32x4*)(ex + (w ^ 1) * 4096 + g * 1024);
          }
          SB();
        }
#pragma unroll
        for (int g = 0; g < 4; ++g) {
#pragma unroll
          for (int j = 0; j < 4; ++j) xs[4 * g + j] = xn[4 * g + j] + t[g][j];
        }
      }
      STAMPX(pb + 8);
      if (layer == NL - 1) {
        const rsrc_t rOUT = __builtin_amdgcn_make_buffer_rsrc((void*)out, 0, SEQ * DM * 2, 0x00020000);
        const u32x4 f0 = pack8<0>(xs), f1 = pack8<1>(xs);
        int ln = lane; PIN(ln);
        const int row = (ln & 31) * 896, up = ln >> 5;
        const int ob = (tb * 4 + tx) * 32 * 896 + 7 * pair * 16;
        st128<0>(f0, rOUT, row + up * 16, ob + 5 * kh * 16);
        st128<0>(f1, rOUT, row + up * OOB, ob + (2 + 2 * kh) * 16);
        __builtin_amdgcn_raw_buffer_store_b64(lo2(f1), rOUT, row + (1 - up) * OOB, ob + 3 * 16 + kh * 8, 0);
      } else {
        const u32x4 f0 = pack8<0>(xs);
        store_x0(f0, WS_XO + (2 * layer + 1) * XO_B);
        u32x4 f1 = pack8<1>(xs);
        f1[2] &= lomask; f1[3] &= lomask;
        float s1 = 0.f, s2 = 0.f, t1 = 0.f, t2 = 0.f;
#pragma unroll
        for (int j = 0; j < 4; ++j) { const unsigned u = f0[j], v = f1[j]; s1 = dot2(u, ONES, s1); s2 = dot2(u, u, s2); t1 = dot2(v, ONES, t1); t2 = dot2(v, v, t2); }
        s1 += t1; s2 += t2;
        red2<false>(s1, s2);
        f1[2] |= ~lomask & F2U(s1); f1[3] |= ~lomask & F2U(s2);
        store_x1(f1, WS_XO + (2 * layer + 1) * XO_B);
        CALS(3 * layer + 2);
        STAMPX(pb + 9);
      }
      asm volatile("" : "=v"(ak), "=v"(av), "=v"(rstd), "=v"(mf), "=v"(phoff));
#pragma unroll
      for (int i = 14; i < 28; ++i) asm volatile("" : "=v"(xf[i]));
    }
    STAMP(61); RSTAMP(63);
    TOT1
    if (b == 0 && w == 0) __builtin_amdgcn_raw_buffer_store_b64((u32x2){(unsigned)(epoch + 1), (unsigned)((epoch + 1) >> 32)}, rWS, WS_EPOCH, 0, SC1);
  } else {
    STAMPX(53);
    u32x4 pA[21], pB[21];
    unsigned tch0, tch1;
    asm volatile("s_load_dword %0, %2, 0x0\n\ts_load_dword %1, %3, 0x0" : "=&s"(tch0), "=&s"(tch1)
                 : "s"(w == 0 ? (const void*)ws : w == 1 ? (const void*)wqkvB : w == 2 ? (const void*)woA : (const void*)wphi), "s"(w < 2 ? (const void*)woB : (const void*)out));
    dma_x0(0, 4);
    unsigned warm0;
    {
      u64 pc; asm volatile("s_getpc_b64 %0" : "=s"(pc));
      const rsrc_t rs = __builtin_amdgcn_make_buffer_rsrc(w == 3 ? (void*)(pc & ~127ull) : (void*)wqkvA, 0, w == 3 ? CODE_B : DM * 3 * DM * 2, 0x00020000);
      const int n = w == 3 ? 9 : 13;
      warm0 = __builtin_amdgcn_raw_buffer_load_b32(rs, lane < n ? (lane + (w == 3 ? (b >> 3) : b * 3 + w) * n) * 128 : OOB, 0, 0);
      SB();
    }
#pragma unroll
    for (int i = 0; i < 7; ++i) { mkrow(i); PIN(krow[i]); }
    lptr trb = make_trb(); PIN(trb);
    u32x4 id0 = ident_frag(0, r, hh); PIN(id0);
    SB();
    dma_x0(4, 28);
#pragma unroll
    for (int i = 0; i < NV0; ++i) pA[14 + i] = ldw(rWA, i, 2688, 1792);
    STAMPX(54);
    asm volatile("s_waitcnt vmcnt(%2) lgkmcnt(0)" :: "s"(tch0), "s"(tch1), "n"(NV0) : "memory");
    const u32x4 sv = {SENT, SENT, SENT, SENT};
    auto clean_kv = [&](int layer) { if (layer < NL) st128<SC1>(sv, rWS, lane * 16, WS_MID + (h * 4 + layer) * MID_B + (tb * 4 + w) * 1024); };
    auto clean_o = [&](int layer) { if (layer < NL) store_o(sv, sv, WS_XO + (2 * layer) * XO_B); };
    auto clean_x = [&](int layer) { if (layer < NL - 1) { store_x0(sv, WS_XO + (2 * layer + 1) * XO_B); store_x1(sv, WS_XO + (2 * layer + 1) * XO_B); } };
    unsigned r0 = (unsigned)__builtin_amdgcn_s_memrealtime(), c0 = (unsigned)__builtin_amdgcn_s_memtime(), tann = 0;
    auto mark = [&](int cyc) {
      const unsigned r1 = (unsigned)__builtin_amdgcn_s_memrealtime(), c1 = (unsigned)__builtin_amdgcn_s_memtime();
      tann = r1 + LAND + (unsigned)((float)cyc * (float)(r1 - r0) * __builtin_amdgcn_rcpf((float)(c1 - c0)));
      r0 = r1; c0 = c1;
    };
    auto push = [&](int hop, unsigned voff) {
      CALA(hop);
      __builtin_amdgcn_raw_buffer_store_b64((u32x2){tag[0], (tag[1] & 0xfff00000u) | (tann & 0xfffffu)}, rWS, voff, WS_FLAGS + hop * HOP_B, SC1);
    };
    auto push_grp = [&](int hop) { push(hop, lane < 16 ? (tb * 16 + lane) * 128 + h * 8 : OOB); };
    auto push_mid = [&](int layer) {
      if (layer & 1) push(3 * layer, (lane < 2 && tb + 1 + lane < 16) ? ((tb + 1 + lane) * 16 + h) * 128 + lane * 8 : OOB);
      else push(3 * layer, lane < 16 ? (lane * 16 + h) * 128 + tb * 8 : OOB);
    };
    const rsrc_t rWB = __builtin_amdgcn_make_buffer_rsrc((void*)wqkvB, 0, DM * 3 * DM * 2, 0x00020000);
    const rsrc_t rOA = __builtin_amdgcn_make_buffer_rsrc((void*)woA, 0, DM * DM * 2, 0x00020000);
    const rsrc_t rOB = __builtin_amdgcn_make_buffer_rsrc((void*)woB, 0, DM * DM * 2, 0x00020000);
    auto spin = [&](int k, unsigned n) { while (__builtin_amdgcn_readfirstlane(cnt[k]) < n) __builtin_amdgcn_s_sleep(1); asm volatile("" ::: "memory"); };
    auto wait_h = [&](int hop, int mask) {
      if (w == 0) { if (mask >= 0) poll(hop, mask); cnt[1] = hop + 1; }
      else while (__builtin_amdgcn_readfirstlane(cnt[1]) != hop + 1) { }
      asm volatile("" ::: "memory");
    };
    const unsigned pkb = WS_PK + h * 262144;
    auto ldp = [&](unsigned off) { const u32x4 v = __builtin_amdgcn_raw_buffer_load_b128(rWS, lane * 16, pkb + off + w * 1024, SC1); SB(); return v; };
    STAMPX(55);
    bar();
#pragma unroll
    for (int i = NV0; i < 7; ++i) pA[14 + i] = ldw(rWA, i, 2688, 1792);
    load_epoch();
    u32x4 pw[4];
    {
      const unsigned act = tb < 14 ? 0u : OOB;
#pragma unroll
      for (int j = 0; j < 2; ++j) {
        const int st = 2 * tb + j, p = (st * 43) >> 7, m = st - 3 * p;
        const int c = st < 24 ? 7 * p + ((0x520 >> (4 * m)) & 15) + hh * (1 + (m & 1)) : 7 * (2 * (st - 24) + hh) + 3;
        pw[j] = __builtin_amdgcn_raw_buffer_load_b128(rWB, ((8 * c + ((lane >> 2) & 7)) * 2688 + sub * 16) | (w < 3 ? act : OOB), col + (w == 2 ? 0 : 896 + 896 * w), 0);
        SB();
      }
      {
        const int n = 4 * tb + w, k2 = n >= 28, i = (n - 28 * k2) >> 1, st = i - 2, p = (st * 43) >> 7, m = st - 3 * p;
        const int c = 28 * k2 + (i < 2 ? 14 * i + 3 + 7 * (n & 1) : 7 * p + ((0x520 >> (4 * m)) & 15) + (n & 1) * (1 + (m & 1)));
        const unsigned vo = (c * 7168 + (lane >> 3) * 896 + 112 * pair + (((lane & 7) ^ ((lane >> 2) & 4)) * 16)) | act;
        pw[2] = __builtin_amdgcn_raw_buffer_load_b128(rOA, vo, 0, 0); SB();
        pw[3] = __builtin_amdgcn_raw_buffer_load_b128(rOB, vo, 0, 0); SB();
      }
    }
    __builtin_amdgcn_s_setprio(3);
    STAMPX(57);
    spin(5, 4);
    put_slab(pA + 14, 0, 1);
#pragma unroll
    for (int j = 0; j < NQ1; ++j) pA[j] = ldw(rWA, j, 2688, 0);
    sum_slab(0, 1, trb);
#pragma unroll
    for (int j = NQ1; j < NQ0; ++j) pA[j] = ldw(rWA, j, 2688, 0);
#pragma unroll
    for (int i = 0; i < 7; ++i) pA[7 + i] = ldl(lds + L_WK + i * 4096 + ht * 16);
    STAMPX(58);
    bar();
    if (w == 3) mark(TKA0);
    __builtin_amdgcn_s_setprio(0);
#pragma unroll
    for (int j = NQ0; j < 7; ++j) pA[j] = ldw(rWA, j, 2688, 0);
    {
      const unsigned keep01 = (odd && sub == 0) ? 0u : ~0u, keep23 = (!odd && sub == 3) ? 0u : ~0u, act = (tb < 14 ? 0u : OOB) | (lane * 16);
#pragma unroll
      for (int j = 0; j < 2; ++j) st128<SC1>(pw[j] & (u32x4){keep01, keep01, keep23, keep23}, rWS, w < 3 ? act : OOB, pkb + w * SLAB + (2 * tb + j) * 1024);
      st128<SC1>(pw[2], rWS, act, pkb + 3 * SLAB + (4 * tb + w) * 1024);
      st128<SC1>(pw[3], rWS, act, pkb + 5 * SLAB + (4 * tb + w) * 1024);
    }
    clean_kv(0); clean_o(0);
    make_tag();
    STAMPX(59);
    first_slab(pA, 0, 2, trb);
    drain();
    arrive(3);
    if (w == 3) { spin(3, 4); push_mid(0); }
    asm volatile("" :: "v"(warm0));
    __builtin_amdgcn_s_setprio(3);
    u32x4 po[14];
    auto swap_wo = [&](bool keep) {
      lptr d = lds + L_WO + ht * 16; PIN(d);
#pragma unroll
      for (int i = 0; i < 14; ++i) { u32x4 t; if (keep) t = ldl(d + i * 4096); *(LAS u32x4*)(d + i * 4096) = po[i]; if (keep) po[i] = t; SB(); }
    };
    STAMPX(60);
#pragma unroll
    for (int layer = 0; layer < NL; ++layer) {
      const int type = layer & 1;
      const int pb = 2 + layer * 14;
      const unsigned mid = WS_MID + (h * 4 + layer) * MID_B;
      if (type == 0) {
        lptr ta0, ta1;
        {
          const int rb = (lane >> 4) & 1, q = (lane >> 2) & 3, p = lane & 3;
          const int rp = 4 * rb + p, lam = (rp & 4) | ((rp & 1) << 1) | ((rp >> 1) & 1);
          ta0 = lds + L_SCR + (8 * hh + q) * 64 + (((lam >> 1) ^ (q >> 1)) * 16) + (lam & 1) * 8;
          ta1 = lds + L_SCR + (8 * hh + q + 4) * 64 + (((lam >> 1) ^ (q >> 1) ^ 2) * 16) + (lam & 1) * 8;
          PIN(ta0); PIN(ta1);
        }
        bar();
        if (layer == 2) probe = (unsigned)__builtin_amdgcn_s_memtime();
        {
          auto tr2 = [&](unsigned off) {
            bf16x4 a0 = __builtin_amdgcn_ds_read_tr16_b64_v4bf16((LAS bf16x4*)(ta0 + off));
            bf16x4 a1 = __builtin_amdgcn_ds_read_tr16_b64_v4bf16((LAS bf16x4*)(ta1 + off));
            return __builtin_bit_cast(u32x4, __builtin_shufflevector(a0, a1, 0, 1, 2, 3, 4, 5, 6, 7));
          };
          const unsigned so = (w >> 1) * 8192;
          u32x4 fa[4], fb[4];
          f32x16 kv = Z16;
#pragma unroll
          for (int i = 0; i < 3; ++i) { fa[i] = tr2(so + i * 1024); fb[i] = tr2(16384 + i * 1024); }
          SB();
#pragma unroll
          for (int i = 0; i < 8; ++i) {
            if (i + 3 < 8) { fa[(i + 3) & 3] = tr2(so + (i + 3) * 1024); fb[(i + 3) & 3] = tr2(16384 + (i + 3) * 1024); }
            SB();
            kv = mfma(fa[i & 3], fb[i & 3], kv);
            SB();
          }
          st128<SC1>((w & 1) ? pack8<1>(kv) : pack8<0>(kv), rWS, lane * 16, mid + (tb * 4 + w) * 1024);
          CALS(3 * layer);
        }
        if (layer == 2) probe = (unsigned)__builtin_amdgcn_s_memtime() - probe;
        if (layer == 0) clean_x(0);
        wait_h(3 * layer, 15);
        STAMPX(pb + 2);
        f32x16 sum;
        for (;;) {
          sum = Z16;
          unsigned m = 0;
          u32x4 pr[16];
          const unsigned g = mid + w * 1024;
#pragma unroll
          for (int i = 0; i < 16; ++i) {
            pr[i] = __builtin_amdgcn_raw_buffer_load_b128(rWS, lane * 16, g + i * 4096, SC1); SB();
            if (i < 15) PACED();
          }
          if (w == 3) { mark(layer ? TOA : TOA0); push_grp(3 * layer + 1); SB(); }
#pragma unroll
          for (int i = 0; i < 16; ++i) {
            if (layer == 0 && i < NPO) po[i] = ldp(3 * SLAB + i * 4096);
            sum = mfma(id0, pr[i], sum); m = max4(m, pr[i]); PIN(m); SB();
          }
          if (!__any(m == SENT) || budget == 0) break;
          --budget; RET(layer * 3 + 1);
          asm volatile("" ::: "memory");
        }
        *(LAS u32x4*)(lds + L_KVF + w * 1024 + lane * 16) = pack8<0>(sum);
        STAMPX(pb + 3);
        bar();
        if (layer == 0) {
#pragma unroll
          for (int i = NPO; i < 14; ++i) po[i] = ldp(3 * SLAB + i * 4096);
        } else __builtin_amdgcn_s_sleep(5);
      } else {
        gather_all(layer, true);
        if (layer == 2) probe = (unsigned)__builtin_amdgcn_s_memtime();
        u32x4 t[8];
        wait_h(3 * layer, tb >= 2 ? 1 : tb - 1);
        STAMPX(pb + 2);
        if (tb - 2 + (w >> 1) >= 0) {
          const unsigned g = mid + (tb * 4 - 8 + 2 * w) * 4096;
          for (;;) {
            unsigned c[2] = {0u, 0u};
#pragma unroll
            for (int i = 0; i < 8; ++i) { t[i] = __builtin_amdgcn_raw_buffer_load_b128(rWS, lane * 16 + (i & 3) * 1024, g + (i >> 2) * 4096, SC1); SB(); if (i < 7) PACED(); }
#pragma unroll
            for (int i = 0; i < 8; ++i) {
              c[i >> 2] = add4(c[i >> 2], t[i]); PIN(c[i >> 2]);
              if ((i & 3) == 1) t[i][3] &= (unsigned)hh - 1u;
              *(LAS u32x4*)(lds + L_SCR + 2 * w * 4096 + i * 1024 + lane * 16) = t[i];
              SB();
            }
            const auto cs = __builtin_amdgcn_permlane32_swap(c[0], c[1], false, false);
            if (__all(cs[0] + cs[1] == tag[0] + layer) || budget == 0) break;
            --budget; RET(layer * 3 + 1);
            __builtin_amdgcn_s_sleep(8);
            asm volatile("" ::: "memory");
          }
        } else {
#pragma unroll
          for (int i = 0; i < 8; ++i)
            *(LAS u32x4*)(lds + L_SCR + 2 * w * 4096 + i * 1024 + lane * 16) = (u32x4){0u, 0u, ((i & 3) == 1 && hh) ? 0xff80u : 0u, 0u};
        }
        drain();
        STAMPX(pb + 3);
        bar();
        if (w == 3) { mark(layer == 1 ? TOB1 : TOB); push_grp(3 * layer + 1); }
        __builtin_amdgcn_s_setprio(0);
        if (layer == 1) {
          clean_x(1); clean_kv(2);
#pragma unroll
          for (int i = 0; i < 14; ++i) po[i] = ldp(5 * SLAB + i * 4096);
          swap_wo(true);
          drain();
          spin(4, 4);
        }
      }
      __builtin_amdgcn_s_setprio(0);
      if (layer == 0) {
        swap_wo(false);
      } else if (layer < NL - 1) {
        write_slab((type == 0 ? pB : pA) + 7, L_WK);
      }
      STAMPX(pb + 4);
      bar();
      if (w == 3 && layer < NL - 1) { mark(layer == 2 ? TX2 : layer ? TX : TX0); push_grp(3 * layer + 2); }
      STAMPX(pb + 5);
      if (layer > 0 && layer < NL - 1) { __builtin_amdgcn_s_sleep(WSL); write_slab((type == 0 ? pB : pA) + 14, L_WV); }
      if (layer == 0) {
        __builtin_amdgcn_s_sleep(12);
#pragma unroll
        for (int i = 0; i < 21; ++i) pB[(i + 7) % 21] = ldp(i * 4096);
      }
      bar();
      if (layer < NL - 1) {
        __builtin_amdgcn_s_sleep(SWS);
        if (layer == 0) { write_slab(pB + 7, L_WK); write_slab(pB + 14, L_WV); sum_slab(1, 0, trb); sum_slab(1, 1, trb); }
        else swap_wo(layer == 1);
        bar();
        __builtin_amdgcn_s_sleep(WSL);
        write_slab(type == 0 ? pB : pA, L_WQ);
        if (layer == 0) sum_slab(1, 2, trb);
        arrive(0);
        while (!__all(cnt[8 + (lane & 3)] == layer + 1u)) __builtin_amdgcn_s_sleep(1);
        if (w == 3) { mark(layer == 0 ? TKB1 : layer == 1 ? TKA : TKB); push_mid(layer + 1); }
        __builtin_amdgcn_s_sleep(CSL);
        clean_o(layer + 1);
        if (layer == 1) { clean_x(2); drain(); }
        __builtin_amdgcn_s_setprio(3);
      }
    }
  }
  while (__builtin_amdgcn_s_memrealtime() - tstart < 6000 + probe) __builtin_amdgcn_s_sleep(8);
}
}

extern "C" void kernel_launch(void* const* d_in, const int* in_sizes, int n_in, void* d_out, int out_size, void* d_ws, size_t ws_size, hipStream_t stream) {
  (void)hipFuncSetAttribute((const void*)fused_kernel, hipFuncAttributeMaxDynamicSharedMemorySize, L_END);
  fused_kernel<<<256, 512, L_END, stream>>>((const u16*)d_in[0], (const u16*)d_in[1], (const u16*)d_in[2], (const u16*)d_in[3],
                                            (const u16*)d_in[5], (const u16*)d_in[6], (u16*)d_out, (unsigned char*)d_ws);
}
```

```cpp
#define NL 4
#include <hip/hip_runtime.h>
#include <hip/hip_bf16.h>

typedef unsigned short u16;
typedef unsigned long long u64;
typedef __bf16 bf16x8 __attribute__((ext_vector_type(8)));
typedef __bf16 bf16x4 __attribute__((ext_vector_type(4)));
typedef __bf16 bf16x2 __attribute__((ext_vector_type(2)));
typedef float f32x16 __attribute__((ext_vector_type(16)));
typedef float f32x4 __attribute__((ext_vector_type(4)));
typedef float f32x2 __attribute__((ext_vector_type(2)));
typedef unsigned u32x4 __attribute__((ext_vector_type(4)));
typedef unsigned u32x2 __attribute__((ext_vector_type(2)));
#define LAS __attribute__((address_space(3)))
#define GAS __attribute__((address_space(1)))
#define DI __device__ __forceinline__
#define SB() __builtin_amdgcn_sched_barrier(0)
#define PIN(x) asm volatile("" : "+v"(x))
typedef __amdgpu_buffer_rsrc_t rsrc_t;
typedef LAS unsigned char* lptr;

namespace {
constexpr int SEQ = 2048, DM = 448;
constexpr int SC1 = 16;
constexpr unsigned WS_EPOCH = 0;
constexpr unsigned WS_FLAGS = 4096;
constexpr unsigned HOP_B = 256 * 128;
constexpr unsigned TILE_B = 32768;
constexpr unsigned XO_B = 4 * TILE_B;
constexpr unsigned WS_XO = 1u << 20;
constexpr unsigned MID_B = 64 * 4096;
constexpr unsigned WS_MID = WS_XO + 16 * 8 * XO_B;
constexpr unsigned WS_PK = WS_MID + 64 * MID_B;
constexpr unsigned WS_PROF = WS_PK + 16 * 262144;
constexpr unsigned WS_END = WS_PROF + 256 * 2048;
constexpr unsigned SLAB = 448 * 64;
constexpr unsigned L_WK = 0, L_WV = SLAB, L_WQ = 2 * SLAB, L_WO = 3 * SLAB;
constexpr unsigned L_SCR = 0;
constexpr unsigned L_X0 = SLAB;
constexpr unsigned L_KVF = 5 * SLAB;
constexpr unsigned L_MK = L_KVF + 4096;
constexpr unsigned L_WPH = L_MK + 2048;
constexpr unsigned L_CS = L_WPH + 2048;
constexpr unsigned L_SYNC = L_CS + 6144;
constexpr unsigned L_PROF = L_SYNC + 64;
constexpr unsigned L_END = L_PROF + 2048;
static_assert(L_END <= 163840 && L_X0 + 4 * SLAB <= L_KVF, "lds");
constexpr unsigned SPIN_CAP = 1u << 15;
constexpr unsigned SENT = 0xffffffffu;
constexpr unsigned OOB = 0x7ff00000u;
constexpr unsigned ONES = 0x3f803f80u;
constexpr unsigned CODE_B = 35 * 1024;
constexpr float L2E = 1.4426950408889634f;

DI f32x16 mfma(u32x4 a, u32x4 b, f32x16 c) {
  return __builtin_amdgcn_mfma_f32_32x32x16_bf16(__builtin_bit_cast(bf16x8, a), __builtin_bit_cast(bf16x8, b), c, 0, 0, 0);
}
DI unsigned pk2(float a, float b) { f32x2 t = {a, b}; return __builtin_bit_cast(unsigned, __builtin_convertvector(t, bf16x2)); }
template <int S_> DI u32x4 pack8(const f32x16& c) {
  return (u32x4){pk2(c[8 * S_], c[8 * S_ + 1]), pk2(c[8 * S_ + 2], c[8 * S_ + 3]), pk2(c[8 * S_ + 4], c[8 * S_ + 5]), pk2(c[8 * S_ + 6], c[8 * S_ + 7])};
}
DI u32x4 cat(u32x2 a, u32x2 b) { return (u32x4){a[0], a[1], b[0], b[1]}; }
DI u32x2 lo2(u32x4 v) { return (u32x2){v[0], v[1]}; }
DI unsigned umax3(unsigned a, unsigned b, unsigned c) { return max(max(a, b), c); }
DI unsigned max4(unsigned m, u32x4 v) { return umax3(umax3(m, v[0], v[1]), v[2], v[3]); }
DI unsigned add4(unsigned m, u32x4 v) { return (m + v[0] + v[1]) + v[2] + v[3]; }
DI unsigned xhalf_addu(unsigned v) { auto r = __builtin_amdgcn_permlane32_swap(v, v, false, false); return r[0] + r[1]; }
DI float fmax3(float a, float b, float c) { return fmaxf(fmaxf(a, b), c); }
template <int CTRL> DI float dppadd(float v) { return v + __builtin_bit_cast(float, __builtin_amdgcn_update_dpp(0, __builtin_bit_cast(int, v), CTRL, 0xf, 0xf, true)); }
DI float fmin3(float a, float b, float c) { return fminf(fminf(a, b), c); }
DI f32x16 splat(float v) { f32x16 z; _Pragma("unroll") for (int i = 0; i < 16; ++i) z[i] = v; return z; }
#define Z16 splat(0.f)
DI u32x4 trfrag(lptr p) {
  bf16x4 a0 = __builtin_amdgcn_ds_read_tr16_b64_v4bf16((LAS bf16x4*)p);
  bf16x4 a1 = __builtin_amdgcn_ds_read_tr16_b64_v4bf16((LAS bf16x4*)(p + 256));
  return __builtin_bit_cast(u32x4, __builtin_shufflevector(a0, a1, 0, 1, 2, 3, 4, 5, 6, 7));
}
DI u32x4 ldl(lptr p) { return *(LAS u32x4*)p; }
DI float U2F(unsigned u) { return __uint_as_float(u); }
DI unsigned F2U(float f) { return __float_as_uint(f); }
template <bool MX> DI void red2(float& a, float& b) {
  auto r = __builtin_amdgcn_permlane32_swap(F2U(a), F2U(b), false, false);
  const float t = MX ? fmaxf(U2F(r[0]), U2F(r[1])) : U2F(r[0]) + U2F(r[1]);
  r = __builtin_amdgcn_permlane32_swap(F2U(t), F2U(t), false, false);
  a = U2F(r[0]); b = U2F(r[1]);
}
DI float xhalf_max(float v) { auto r = __builtin_amdgcn_permlane32_swap(F2U(v), F2U(v), false, false); return fmaxf(U2F(r[0]), U2F(r[1])); }
DI float upper_val(float v) { auto r = __builtin_amdgcn_permlane32_swap(F2U(v), F2U(v), false, false); return U2F(r[1]); }
DI float dot2(unsigned u, unsigned v, float c) { return __builtin_amdgcn_fdot2_f32_bf16(__builtin_bit_cast(bf16x2, u), __builtin_bit_cast(bf16x2, v), c, false); }
DI float bflo(unsigned u) { return U2F(u << 16); }
DI float bfhi(unsigned u) { return U2F(u & 0xffff0000u); }
DI void split2(float v, unsigned& hi, unsigned& lo) { hi = pk2(v, v) & 0xffffu; lo = pk2(v - bflo(hi), 0.f) & 0xffffu; }
template <int AUX> DI void st128(u32x4 v, rsrc_t rs, unsigned voff, unsigned soff) {
  SB();
  __builtin_amdgcn_raw_buffer_store_b128(v, rs, voff, soff, AUX);
  SB();
  asm volatile("s_nop 1" :: "v"(v));
  SB();
}
DI void drain() { asm volatile("s_waitcnt vmcnt(0)" ::: "memory"); }
DI void bar() { SB(); asm volatile("s_waitcnt lgkmcnt(0)\n\ts_barrier" ::: "memory"); SB(); }
constexpr int chunk_of(int s, int hh) { return s < 24 ? 7 * (s / 3) + (s % 3 == 0 ? hh : (s % 3 == 1 ? 2 + 2 * hh : 5 + hh)) : 7 * (2 * (s - 24) + hh) + 3; }
constexpr int ord(int i) { return i < 4 ? 24 + i : i - 4; }

#define STAMP(i) do { } while (0)
#define RET(i) do { } while (0)
#define RSTAMP(i) do { } while (0)
#define STAMPX(i) STAMP(i)
#define CALS(hop) do { } while (0)
#define CALA(hop) do { } while (0)
#define TOT0
#define TOT1
#define DUMP(buf, acc, part, sc) do { } while (0)

DI void dual_softmax(const f32x16& p, float c, float wtail, u32x4 (&fp)[2], u32x4 (&fm)[2]) {
  float mx = fmax3(p[0], p[1], p[2]), mn = fmax3(-p[0], -p[1], -p[2]);
#pragma unroll
  for (int i = 3; i < 15; i += 2) { mx = fmax3(mx, p[i], p[i + 1]); mn = fmax3(mn, -p[i], -p[i + 1]); }
  mx = fmax3(mx, p[15], p[15]); mn = fmax3(mn, -p[15], -p[15]);
  red2<true>(mx, mn);
  const float a = -c * mx, b = -c * mn;
  float nc = -c; PIN(nc);
  f32x16 ep = p * c + a, em = p * nc + b;
  SB();
#pragma unroll
  for (int i = 0; i < 16; ++i) ep[i] = __builtin_amdgcn_exp2f(ep[i]);
#pragma unroll
  for (int i = 0; i < 16; ++i) em[i] = __builtin_amdgcn_exp2f(em[i]);
  f32x2 sp = {ep[0], ep[1]}, sm = {em[0], em[1]};
#pragma unroll
  for (int i = 2; i < 12; i += 2) { sp += (f32x2){ep[i], ep[i + 1]}; sm += (f32x2){em[i], em[i + 1]}; }
  sp += wtail * ((f32x2){ep[12], ep[13]} + (f32x2){ep[14], ep[15]});
  sm += wtail * ((f32x2){em[12], em[13]} + (f32x2){em[14], em[15]});
  float u = sp[0] + sp[1], v = sm[0] + sm[1];
  red2<false>(u, v);
  ep *= __builtin_amdgcn_rcpf(u); em *= __builtin_amdgcn_rcpf(v);
  fp[0] = pack8<0>(ep); fp[1] = pack8<1>(ep); fm[0] = pack8<0>(em); fm[1] = pack8<1>(em);
}
DI u32x4 ident_frag(int s, int r, int hh) {
  const unsigned val = 0x3f80u << (16 * (r & 1));
  const int idx = (((r >> 2) ^ hh) & 1) * 8 + (r >> 4) * 4 + ((r >> 2) & 2) + ((r >> 1) & 1) - 4 * s;
  return (u32x4){idx == 0 ? val : 0u, idx == 1 ? val : 0u, idx == 2 ? val : 0u, idx == 3 ? val : 0u};
}
#define CHK1 3
#define CHK2 12
template <typename S, typename A> DI void steps28(S step, A abort) {
#pragma unroll
  for (int i = 0; i <= CHK1; ++i) step(i);
  if (abort()) return;
#pragma unroll
  for (int i = CHK1 + 1; i < 28; ++i) step(i);
}
template <int PD, typename F, typename G, typename A> DI void gemm1(f32x16& acc, lptr wb, const u32x4 (&xf)[28], F pre, G post, A abort) {
  u32x4 wr[PD];
#pragma unroll
  for (int i = 0; i < PD - 1; ++i) wr[i] = trfrag(wb + ord(i) * 1024);
  SB();
  steps28([&](int i) __attribute__((always_inline)) {
    pre(i);
    if (i + PD - 1 < 28) wr[(i + PD - 1) % PD] = trfrag(wb + ord(i + PD - 1) * 1024);
    SB();
    acc = mfma(wr[i % PD], xf[i], acc);
    SB();
    post(i);
    SB();
  }, abort);
}
template <typename H, typename F, typename G, typename A>
DI void gemm2(f32x16& ak, f32x16& av, lptr bk, const u32x4 (&xf)[28], H head, F pre, G post, A abort) {
  u32x4 wk[3], wv[3];
  head([&](int u) {
    if (u == 0) wv[0] = trfrag(bk + SLAB + ord(0) * 1024);
    if (u == 1) wk[0] = trfrag(bk + ord(0) * 1024);
    if (u == 2) wv[1] = trfrag(bk + SLAB + ord(1) * 1024);
    if (u == 3) wk[1] = trfrag(bk + ord(1) * 1024);
    SB();
  });
  steps28([&](int i) __attribute__((always_inline)) {
    pre(i);
    if (i + 2 < 28) wv[(i + 2) % 3] = trfrag(bk + SLAB + ord(i + 2) * 1024);
    SB();
    av = mfma(wv[i % 3], xf[i], av);
    SB();
    if (i + 2 < 28) wk[(i + 2) % 3] = trfrag(bk + ord(i + 2) * 1024);
    post(i, 0);
    SB();
    ak = mfma(wk[i % 3], xf[i], ak);
    SB();
    post(i, 1);
    SB();
  }, abort);
}
#define NOAB [] { return false; }
#define EARLYAB [&] { return __builtin_expect(__any(m == sent), 0); }
#define LA 12
#define XH 27
#define VSLEEP 8
#define QSLEEP 28
#define LAO 8
#define XBAR 26
#define DOA 3
#define DOB 6
#define DX 3
#define KVI 6
#define KVB 3
#define NKB 5
#define XM 4
#ifndef NV0
#define NV0 7
#endif
#define NK2 8
#define NK3 9
#define PACE 8
#define LAG 6
#define PACED() do { if (PACE) asm volatile("s_nop %0" :: "n"(PACE)); SB(); } while (0)
#define PSLP 4
#define RDLY 0
#define KDLY 0
#define LAND 10
#define TKA0 4100
#define TKA 3600
#define TKB1 2400
#define TKB 2500
#define TOA0 2050
#define TOA 1850
#define TOB1 2750
#define TOB 2520
#define TX0 3800
#define TX 2850
#define BACK 8
#define NQ0 7
#define NQB 0
#define TX2 3200
#define ND0 10
#define NPO 14
#define SWS 6
#define LMAX 1.8446744e19f
#define CSL 2
#define WSL 4
#define PACE2() do { asm volatile("s_nop %0" :: "n"(PACE - 2)); SB(); } while (0)
#define NQ1 4
#define REUSE 4
#define OBUF(l) ((REUSE & 1) ? 0 : 2 * (l))
#define XBUF(l) ((REUSE & 2) ? 1 + 2 * ((l) & 1) : 2 * (l) + 1)
#define MBUF(l) (((l) & 1) ? ((REUSE & 4) ? 1 : (l)) : ((REUSE & 8) ? 0 : (l)))
#define FW 1
#define PDELAY 14

__global__ void __launch_bounds__(512, 2) fused_kernel(const u16* __restrict__ x0, const u16* __restrict__ wqkvA, const u16* __restrict__ wphi,
                                                       const u16* __restrict__ woA, const u16* __restrict__ wqkvB, const u16* __restrict__ woB,
                                                       u16* __restrict__ out, unsigned char* ws) {
  extern __shared__ __attribute__((aligned(16))) unsigned char smem[];
  const lptr lds = (lptr)smem;
  const int tid = threadIdx.x, lane = tid & 63, wave = __builtin_amdgcn_readfirstlane(tid >> 6);
  TOT0
  const int w = wave & 3;
  const int r = lane & 31, hh = lane >> 5;
  const int b = blockIdx.x;
  const int tb = ((b >> 1) & 3) * 4 + (b >> 6), h = (b & 1) * 8 + ((b >> 3) & 7);
  const int me = tb * 16 + h;
  const int odd = h & 1, pair = h >> 1;
  const int T = tb * 4 + w;
  const unsigned TOFF = tb * 8 * XO_B + w * TILE_B;
  const int kh = w & 1, tx = 2 * odd + (w >> 1);
  const unsigned XOFF = tb * 8 * XO_B + tx * TILE_B;
  const rsrc_t rWS = __builtin_amdgcn_make_buffer_rsrc((void*)ws, 0, WS_END, 0x00020000);
  unsigned budget = SPIN_CAP;
  u64 epoch;
  u32x2 tag;
  u32x2 ev;
  auto load_epoch = [&]() { ev = __builtin_amdgcn_raw_buffer_load_b64(rWS, 0, WS_EPOCH, SC1); SB(); };
  auto make_tag = [&]() {
    epoch = (u64)(unsigned)__builtin_amdgcn_readfirstlane(ev[0]) | ((u64)(unsigned)__builtin_amdgcn_readfirstlane(ev[1]) << 32);
    u64 z = epoch + 0x9E3779B97F4A7C15ull;
    z = (z ^ (z >> 30)) * 0xBF58476D1CE4E5B9ull; z = (z ^ (z >> 27)) * 0x94D049BB133111EBull; z ^= z >> 31;
    unsigned t0 = (unsigned)z, t1 = (unsigned)(z >> 32);
    asm volatile("" : "+s"(t0), "+s"(t1));
    tag = (u32x2){t0, t1};
  };
  volatile LAS unsigned* const cnt = (volatile LAS unsigned*)(lds + L_SYNC);
  auto arrive = [&](int k) { if (lane == 0) asm volatile("ds_add_u32 %0, %1" :: "v"(lds + L_SYNC + 4 * k), "v"(1u) : "memory"); };
  auto gather_all = [&](int layer, bool nap) {
    while (__builtin_amdgcn_readfirstlane(*cnt) < 8u * layer + 4u) { if (nap) __builtin_amdgcn_s_sleep(2); }
    asm volatile("" ::: "memory");
  };
  auto poll = [&](int hop, int mask) {
    int ln = lane & mask; PIN(ln);
    u32x2 v;
    for (;;) {
      v = __builtin_amdgcn_raw_buffer_load_b64(rWS, ln * 8, WS_FLAGS + hop * HOP_B + me * 128, SC1);
      asm volatile("" ::: "memory");
      if (__all(v[0] == tag[0] && ((v[1] ^ tag[1]) >> 20) == 0)) break;
      if (budget <= 1) break;
      --budget;
      if (PSLP) __builtin_amdgcn_s_sleep(PSLP);
    }
    const unsigned t = v[1] << 12;
    for (int n = 0; n < 64; ++n) {
      const unsigned now = (unsigned)__builtin_amdgcn_s_memrealtime() << 12;
      if (!__any((int)(t - now) > 0)) break;
    }
    asm volatile("" ::: "memory");
  };
  auto make_trb = [&]() {
    const int rb = (lane >> 4) & 1, q = (lane >> 2) & 3, p = lane & 3;
    const int rp = 4 * rb + p, lam = (rp & 4) | ((rp & 1) << 1) | ((rp >> 1) & 1);
    const int pc = ((odd ? 0x01327654 : 0x76543210) >> (4 * lam)) & 7;
    return lds + (8 * hh + q) * 64 + pc * 8;
  };
  auto store_x0 = [&](u32x4 f0, unsigned base) { int ln = lane; PIN(ln); st128<SC1>(f0, rWS, ln * 16, base + XOFF + (3 * pair + 2 * kh) * 1024); };
  auto store_x1 = [&](u32x4 f1, unsigned base) {
    int ln = lane; PIN(ln);
    st128<SC1>(f1, rWS, ln * 16 + __umul24(ln >> 5, 24576 - 512 - 1024 - pair * 2048), base + XOFF + (3 * pair + 1) * 1024 + kh * 512);
  };
  auto store_o = [&](u32x4 f0, u32x4 f1, unsigned base) {
    int ln = lane; PIN(ln);
    st128<SC1>(f0, rWS, ln * 16, base + TOFF + (3 * pair + 2 * odd) * 1024);
    st128<SC1>(f1, rWS, ln * 16 + (ln >> 5) * OOB, base + TOFF + (3 * pair + 1) * 1024 + odd * 512);
    __builtin_amdgcn_raw_buffer_store_b64(lo2(f1), rWS, ln * 8 + (1 - (ln >> 5)) * OOB, base + TOFF + 24576 - 256 + h * 256, SC1);
  };
  const int ht = tid & 255, sub = lane & 3;
  const int col = h * 56 - odd * 8;
  int krow[7];
  auto mkrow = [&](int i) {
    const int s = 4 * i + w;
    const int p = (s * 43) >> 7, m = s - 3 * p;
    const int c = i < 6 ? 7 * p + ((0x520 >> (4 * m)) & 15) + hh * (1 + (m & 1)) : 7 * (2 * w + hh) + 3;
    krow[i] = 8 * c + ((lane >> 2) & 7);
  };
  auto ldw = [&](rsrc_t rs, int i, int stride, int off) { int k = krow[i]; PIN(k); const u32x4 v = __builtin_amdgcn_raw_buffer_load_b128(rs, k * stride + sub * 16, col + off, 0); SB(); return v; };
  auto write_slab = [&](const u32x4* p, unsigned off) {
    lptr d = lds + off + ht * 16; PIN(d);
#pragma unroll
    for (int i = 0; i < 7; ++i) *(LAS u32x4*)(d + i * 4096) = p[i];
  };
  auto put_slab = [&](u32x4* p, int type, int part) {
    const unsigned keep01 = (odd && sub == 0) ? 0u : ~0u, keep23 = (!odd && sub == 3) ? 0u : ~0u;
#pragma unroll
    for (int i = 0; i < 7; ++i) {
      p[i][0] &= keep01; p[i][1] &= keep01; p[i][2] &= keep23; p[i][3] &= keep23;
      if (type == 0 && part == 0) { p[i][0] |= ~keep01 & 0x3f80u; p[i][2] |= ~keep23 & 0x3f80u; }
    }
    write_slab(p, part * SLAB);
  };
  auto sum_slab = [&](int type, int part, lptr trb) {
    lptr wb = trb + part * SLAB + w * 1024; PIN(wb);
    u32x4 wr[7];
#pragma unroll
    for (int i = 0; i < 7; ++i) wr[i] = trfrag(wb + i * 4096);
    f32x16 a = Z16;
#pragma unroll
    for (int i = 0; i < 7; ++i) a = mfma((u32x4){ONES, ONES, ONES, ONES}, wr[i], a);
    unsigned hi, lo; split2(a[0], hi, lo);
    if (hh == (w >> 1)) *(LAS u32x2*)(lds + L_CS + type * 3072 + part * 1024 + lane * 16 + (w & 1) * 8) = (u32x2){hi | (lo << 16), hi};
  };
  auto first_slab = [&](u32x4* p, int type, int part, lptr trb) { put_slab(p, type, part); sum_slab(type, part, trb); };
  const rsrc_t rWA = __builtin_amdgcn_make_buffer_rsrc((void*)wqkvA, 0, DM * 3 * DM * 2, 0x00020000);
  auto dma_x0 = [&](int i0, int i1) __attribute__((always_inline)) {
    const rsrc_t rX0 = __builtin_amdgcn_make_buffer_rsrc((void*)x0, 0, SEQ * DM * 2, 0x00020000);
    const int o0 = (lane >> 3) * 896 + (((lane & 7) ^ (lane >> 4)) * 16), o1 = o0 ^ 64;
#pragma unroll
    for (int i = i0; i < i1; ++i) {
      __builtin_amdgcn_raw_ptr_buffer_load_lds(rX0, (LAS void*)(lds + L_X0 + w * SLAB + i * 1024), 16, (i & 1) ? o1 : o0, (T * 32 + (i & 3) * 8) * 896 + (i >> 2) * 128, 0, 0);
      SB();
    }
  };

  if (wave < 4) {
    RSTAMP(62); STAMP(0);
    u32x4 xf[28];
    f32x16 xs, ak, av;
    float rstd;
    u32x4 mf;
    auto stats = [&](float s1, float s2) {
      const float mean = s1 * (1.0f / DM);
      rstd = __builtin_amdgcn_rsqf(fmaxf(s2 * (1.0f / DM) - mean * mean, 0.f) + 1e-5f);
      unsigned hi, lo; split2(-mean, hi, lo);
      mf = (u32x4){hi | (hi << 16), lo, hi | (hi << 16), lo};
      PIN(rstd); PIN(mf);
    };
    lptr trb;
    u32x4 id0, id1;
    float wtail;
    unsigned lomask, phoff;
    {
      __builtin_amdgcn_s_setprio(3);
#pragma unroll
      for (int i = 0; i < 7; ++i) { mkrow(i); xf[21 + i] = ldw(rWA, i, 2688, 896); }
      STAMPX(54);
      trb = make_trb();
      id0 = ident_frag(0, r, hh); id1 = ident_frag(1, r, hh);
      wtail = hh ? 0.f : 1.f; lomask = hh ? 0u : ~0u;
      PIN(trb); PIN(id0); PIN(id1); PIN(wtail); PIN(lomask);
      if (w >= 2) {
        u32x4 mk;
#pragma unroll
        for (int i = 0; i < 4; ++i) { const int k0 = ((2 * i) & 3) + 8 * (i >> 1) + 4 * hh + 16 * (w & 1); mk[i] = (k0 > r ? 0xffffu : 0u) | (k0 + 1 > r ? 0xffff0000u : 0u); }
        *(LAS u32x4*)(lds + L_MK + (w & 1) * 1024 + lane * 16) = mk;
      }
      if (w == 0) { cnt[0] = 0u; cnt[1] = 0u; cnt[2] = 0u; cnt[3] = 0u; cnt[4] = 0u; cnt[5] = 0u; cnt[6] = 0u; }
      cnt[8 + w] = 0u;
      const int lam = 4 * (w >> 1) + 2 * hh + (w & 1);
      const int pc = ((odd ? 0x01327654 : 0x76543210) >> (4 * lam)) & 7;
      unsigned off = lam == 7 ? OOB : ((h * 28 + 4 * pc - 4 * odd) * 28 + (r < 28 ? r : r - 8)) * 2;
      phoff = off; PIN(phoff);
      __builtin_amdgcn_s_setprio(0);
    }
    tag = (u32x2){0u, 0u}; epoch = 0;
    asm volatile("" : "=v"(ak), "=v"(av), "=v"(rstd), "=v"(mf), "=v"(xs));
#pragma unroll
    for (int i = 0; i < 21; ++i) asm volatile("" : "=v"(xf[i]));
    unsigned sent = SENT;
    auto release = [&](int hop) { if (w == 0) poll(hop, 15); bar(); };
    for (int layer = 0; layer < NL; ++layer) {
      const int type = layer & 1;
      const int pb = 2 + layer * 14;
      if (layer > 0) {
        unsigned base = WS_XO + XBUF(layer - 1) * XO_B + TOFF;
        int v16 = lane * 16, vs = v16 + (v16 & 512);
        lptr bk = trb + L_WK;
        PIN(vs);
        release(3 * layer - 1);
        STAMP(pb + 0);
        for (;;) {
          PIN(v16); PIN(vs); PIN(bk); asm volatile("" : "+s"(base));
          u32x4 sp[8];
          auto ld = [&](int s) { xf[4 + s] = __builtin_amdgcn_raw_buffer_load_b128(rWS, v16 + (s & 3) * 1024, base + (s >> 2) * 4096, SC1); SB(); };
          unsigned m = 1;
          ak = Z16; av = Z16;
          float s1 = 0.f, s2 = 0.f, mean;
          unsigned hi;
          u32x4 ck, cv;
          gemm2(ak, av, bk, xf,
                    [&](auto gap) {
#pragma unroll
                      for (int u = 0; u < 8; ++u) { sp[u] = __builtin_amdgcn_raw_buffer_load_b128(rWS, vs + ((u >> 1) & 1) * 2048 + (u & 1) * 512, base + 24576 + (u >> 2) * 4096, SC1); SB(); gap(u); if (u < 4) PACE2(); else PACED(); }
#pragma unroll
                      for (int s = 0; s < LA - 8; ++s) { ld(s); PACED(); }
                    },
                    [&](int i) {
                      if (i + LA - 8 < 24) ld(i + LA - 8);
                      if (i < 4) { PIN(sp[2 * i]); PIN(sp[2 * i + 1]); xf[i] = cat(lo2(sp[2 * i]), lo2(sp[2 * i + 1])); }
                      if (i == 22) { av = mfma(cv, mf, av); SB(); }
                      if (i == 24) { ak = mfma(ck, mf, ak); SB(); }
                    },
                    [&](int i, int hf) {
                      if (i < 4) m = max4(m, sp[2 * i + hf]); else m = umax3(m, xf[i][2 * hf], xf[i][2 * hf + 1]);
                      PIN(m);
                      if (i < 8) { if (hf) { s2 += U2F(sp[i][3]); PIN(s2); } else { s1 += U2F(sp[i][2]); PIN(s1); } }
                      if (hf) return;
                      if (i == 13) asm volatile("ds_write_b32 %0, %1" :: "v"(lds + L_SYNC + 32 + 4 * w), "v"(layer) : "memory");
                      if (i == 9) { auto t = __builtin_amdgcn_permlane32_swap(F2U(s1), F2U(s2), false, false); s1 = U2F(t[0]) + U2F(t[1]); PIN(s1); }
                      if (i == 10) { auto t = __builtin_amdgcn_permlane32_swap(F2U(s1), F2U(s1), false, false); s1 = U2F(t[0]); s2 = U2F(t[1]); PIN(s1); PIN(s2); }
                      if (i == 14) { mean = s1 * (-1.0f / DM); s2 = s2 * (1.0f / DM); PIN(mean); PIN(s2); }
                      if (i == 15) { s2 = fmaxf(fmaf(-mean, mean, s2), 0.f); PIN(s2); }
                      if (i == 16) { rstd = __builtin_amdgcn_rsqf(s2 + 1e-5f); PIN(rstd); }
                      if (i == 17) { hi = pk2(mean, mean); s1 = mean - bfhi(hi); PIN(hi); PIN(s1); }
                      if (i == 18) { const unsigned lo = pk2(s1, 0.f); mf = (u32x4){hi, lo, hi, lo}; PIN(mf); }
                      if (i == 19) { const lptr cs = lds + L_CS + type * 3072 + v16; ck = ldl(cs); cv = ldl(cs + 1024); }
                    }, EARLYAB);
          if (__builtin_expect(!__any(m == sent), 1)) break;
          if (--budget == 0) sent = 0u;
          RET(layer * 3);
          __builtin_amdgcn_s_sleep(BACK);
          asm volatile("" ::: "memory");
        }
      }
      int first = layer; asm volatile("" : "+s"(first));
      if (first == 0) {
        int ln = lane; PIN(ln);
        const int hh = ln >> 5;
        const unsigned ra = (ln & 31) * 128 + ((ln >> 1) & 7) * 16;
        const lptr stg = lds + L_X0 + w * SLAB;
        auto kof = [](int c) { return (c >> 3) * 4096 + (c & 7) * 16; };
        lptr ad[28];
#pragma unroll
        for (int i = 0; i < 28; ++i) {
          const int k0 = kof(chunk_of(ord(i), 0)), k1 = kof(chunk_of(ord(i), 1));
          ad[i] = stg + (ra ^ k0 ^ (~lomask & (k0 ^ k1))); PIN(ad[i]);
        }
        lptr pab = lds + L_X0 + tx * SLAB + (ra ^ kof(7 * pair + 5 * kh + hh)), pc = lds + L_X0 + tx * SLAB + (ra ^ kof(7 * pair + (hh ? 3 : 2 + 2 * kh))); PIN(pab); PIN(pc);
        SB();
        first_slab(xf + 21, 0, 0, trb);
        STAMPX(53);
        bar();
        STAMPX(55);
#pragma unroll
        for (int i = 0; i < 28; ++i) xf[i] = ldl(ad[i]);
        u32x4 xa = ldl(pab), xc = ldl(pc);
        arrive(5);
        SB();
        float s2a = 0.f, s2b = 0.f;
#pragma unroll
        for (int i = 0; i < 28; ++i) {
          const u32x4 u = xf[i]; s2a = dot2(u[1], u[1], dot2(u[0], u[0], s2a)); s2b = dot2(u[3], u[3], dot2(u[2], u[2], s2b));
          PIN(s2a); PIN(s2b);
          SB();
        }
        const rsrc_t rPH = __builtin_amdgcn_make_buffer_rsrc((void*)wphi, 0, 16 * 28 * 28 * 2, 0x00020000);
        unsigned e[4];
#pragma unroll
        for (int j = 0; j < 4; ++j) e[j] = __builtin_amdgcn_raw_buffer_load_b16(rPH, phoff + j * 56, 0, 0);
        load_epoch();
        STAMP(1);
        u32x4 cc = ldl(lds + L_CS + ln * 16);
        ak = Z16;
        lptr bk = trb + L_WK; PIN(bk);
        gemm1<5>(ak, bk, xf, [](int) {}, [&](int i) {
          if (i == 4) {
            const unsigned sel = hh && kh ? 0u : ~0u;
            xc[0] = (xc[0] & sel) | (xc[2] & ~sel); xc[1] = (xc[1] & sel) | (xc[3] & ~sel); PIN(xc);
          }
          if (i >= 16 && i < 24) { const unsigned u = i < 20 ? xa[i & 3] : xc[i & 3]; xs[2 * (i - 16)] = bflo(u); xs[2 * (i - 16) + 1] = bfhi(u); PIN(xs); }
        }, NOAB);
        asm volatile("" :: "v"(ak));
        *(LAS u32x2*)(lds + L_WPH + (w >> 1) * 1024 + ln * 16 + (w & 1) * 8) = (u32x2){e[0] | (e[1] << 16), e[2] | (e[3] << 16)};
        make_tag();
        STAMPX(57);
        bar();
        STAMP(2);
        const u32x4 cv = ldl(lds + L_CS + 1024 + ln * 16);
        av = Z16;
        gemm1<5>(av, bk + SLAB, xf, [&](int i) { if (i == 8) { ak = mfma(cc, mf, ak); SB(); } if (i == 10) { av = mfma(cv, mf, av); SB(); } }, [&](int i) {
          if (i == 2) {
            float s1 = U2F(F2U(ak[12]) & ~lomask), s2 = s2a + s2b;
            red2<false>(s1, s2);
            stats(s1, s2);
          }
        }, NOAB);
      }
      asm volatile("" ::: "memory");
      arrive(0);
      STAMPX(pb + 1);
      const unsigned mid = WS_MID + (h * 4 + MBUF(layer)) * MID_B;
      int ln16 = lane * 16; PIN(ln16);
      u32x4 wph[2], kf0, kf1;
      if (type == 0) {
        wph[0] = ldl(lds + L_WPH + ln16); wph[1] = ldl(lds + L_WPH + 1024 + ln16);
        DUMP(0, ak, 1, rstd); DUMP(0, av, 2, rstd);
        kf0 = pack8<0>(ak); kf1 = pack8<1>(ak);
        f32x16 p = mfma(wph[0], kf0, Z16);
        p = mfma(wph[1], kf1, p);
        SB();
#pragma unroll
        for (int i = 0; i < 16; ++i) { float t = av[i] * rstd; PIN(t); av[i] = t; }
        u32x4 vb0 = pack8<0>(av), vb1 = pack8<1>(av);
        vb1[2] |= ~lomask & 0x3f80u;
        PIN(vb0); PIN(vb1);
        SB();
        u32x4 fp[2], fm[2];
        dual_softmax(p, rstd * L2E, wtail, fp, fm);
        fp[1][2] &= lomask; fp[1][3] &= lomask; fm[1][2] &= lomask; fm[1][3] &= lomask;
        PIN(fp[0]); PIN(fp[1]); PIN(fm[0]); PIN(fm[1]);
        gather_all(layer, false);
        const int rr = (ln16 >> 4) & 31, pc = (ln16 >> 9) ^ ((rr >> 1) & 3);
        const lptr d0 = lds + L_SCR + w * 2048 + rr * 64 + pc * 16, d1 = lds + L_SCR + w * 2048 + rr * 64 + (pc ^ 2) * 16;
        *(LAS u32x4*)(d0) = fp[0]; *(LAS u32x4*)(d1) = fp[1];
        *(LAS u32x4*)(d0 + 8192) = fm[0]; *(LAS u32x4*)(d1 + 8192) = fm[1];
        *(LAS u32x4*)(d0 + 16384) = vb0; *(LAS u32x4*)(d1 + 16384) = vb1;
        bar();
      } else {
        av *= rstd;
        DUMP(0, av, 2, 1.f);
        u32x4 vb1 = pack8<1>(av);
        vb1[2] |= ~lomask & 0x3f80u;
        f32x16 vt = mfma(pack8<0>(av), id0, Z16);
        vt = mfma(vb1, id1, vt);
        SB();
#pragma unroll
        for (int i = 0; i < 16; ++i) { float t = ak[i] * rstd; PIN(t); ak[i] = t; }
        DUMP(0, ak, 1, 1.f);
        kf0 = pack8<0>(ak); kf1 = pack8<1>(ak);
        const unsigned g = mid + T * 4096;
        const lptr d = lds + L_SCR + (8 + w) * 4096 + ln16;
        st128<SC1>(kf0, rWS, ln16, g);
        const u32x4 vf0 = pack8<0>(vt), vf1 = pack8<1>(vt);
        st128<SC1>(vf0, rWS, ln16 + 2048, g);
        st128<SC1>(vf1, rWS, ln16 + 3072, g);
        {
          u32x4 kc = kf1;
          kc[3] |= ~lomask & (tag[0] + layer - xhalf_addu(add4(add4(0u, kf0), kf1) + add4(add4(0u, vf0), vf1)));
          st128<SC1>(kc, rWS, ln16 + 1024, g);
          CALS(3 * layer);
        }
        gather_all(layer, false);
        *(LAS u32x4*)(d) = kf0; *(LAS u32x4*)(d + 1024) = kf1; *(LAS u32x4*)(d + 2048) = vf0; *(LAS u32x4*)(d + 3072) = vf1;
      }
      STAMPX(pb + 2);
      u32x4 qf0, qf1;
      {
        f32x16 aq = mfma(ldl(lds + L_CS + type * 3072 + 2048 + ln16), mf, Z16);
        lptr bq = trb + L_WQ; PIN(bq);
        gemm1<5>(aq, bq, xf, [](int) {}, [](int) {}, NOAB);
        aq *= type ? rstd * (0.18898223650461361f * L2E) : 1.0f;
        DUMP(0, aq, 0, type ? 1.f : rstd);
        qf0 = pack8<0>(aq); qf1 = pack8<1>(aq);
        qf1[2] |= ~lomask & 0x3f80u;
      }
      f32x16 o;
      if (type == 0) {
        u32x4 fp[2], fm[2];
        {
          f32x16 p = mfma(wph[0], qf0, Z16);
          p = mfma(wph[1], qf1, p);
          dual_softmax(p, rstd * L2E, wtail, fp, fm);
        }
        PIN(fp[0]); PIN(fp[1]); PIN(fm[0]); PIN(fm[1]);
        STAMPX(pb + 3);
        bar();
        STAMP(pb + 4);
        const lptr kvf = lds + L_KVF + ln16;
        const u32x4 a0 = ldl(kvf), a1 = ldl(kvf + 1024), a2 = ldl(kvf + 2048), a3 = ldl(kvf + 3072);
        SB();
        o = mfma(a0, fp[0], Z16);
        o = mfma(a1, fp[1], o);
        o = mfma(a2, fm[0], o);
        o = mfma(a3, fm[1], o);
        o *= __builtin_amdgcn_rcpf(upper_val(o[12]) + 1e-6f);
      } else {
        lptr kv = lds + L_SCR + w * 4096 + ln16; PIN(kv);
        auto ex4 = [&](f32x16& s, int i0) {
#pragma unroll
          for (int i = i0; i < i0 + 4; ++i) s[i] = __builtin_amdgcn_exp2f(s[i]);
          unsigned a = pk2(s[i0], s[i0 + 1]), c = pk2(s[i0 + 2], s[i0 + 3]); PIN(a); PIN(c);
          SB();
          return (u32x2){a, c};
        };
        auto tile_max = [&](int j, int side, float m) {
          f32x16 t = mfma(ldl(kv + j * 4096 + 1024), qf1, mfma(ldl(kv + j * 4096), qf0, Z16));
          int lim = ((ln16 >> 4) & 31) - ((ln16 >> 7) & 4); PIN(lim);
#pragma unroll
          for (int i = 0; i < 16; ++i) { const bool up = (i & 3) + 8 * (i >> 2) > lim; m = fmaxf(m, (side == 0 || up == (side == 1)) ? t[i] : m); }
          return m;
        };
        float m = -__builtin_inff(), l;
        for (int pass = 0;; ++pass) {
          const u32x4 mk0 = ldl(lds + L_MK + ln16), mk1 = ldl(lds + L_MK + 1024 + ln16);
          f32x16 negm;
          {
            const u32x4 v0 = ldl(kv + 8 * 4096 + 2048), v1 = ldl(kv + 8 * 4096 + 3072);
            f32x16 s = mfma(ldl(kv + 8 * 4096 + 1024), qf1, mfma(ldl(kv + 8 * 4096), qf0, Z16));
#pragma unroll
            for (int i = 0; i < 16; ++i) {
              const unsigned k = (i < 8 ? mk0 : mk1)[(i >> 1) & 3], on = (i & 1) ? (unsigned)((int)k >> 31) : (unsigned)((int)(k << 16) >> 31);
              s[i] = U2F((F2U(s[i]) & ~on) | (on & 0xff800000u));
            }
#pragma unroll
            for (int i = 0; i < 16; i += 2) m = fmax3(m, s[i], s[i + 1]);
            m = xhalf_max(m);
            s -= m;
            negm = splat(-m);
            PIN(negm);
            const u32x2 a = ex4(s, 0), c = ex4(s, 4), e = ex4(s, 8), f = ex4(s, 12);
            o = mfma(v1, cat(e, f), mfma(v0, cat(a, c), Z16));
            PIN(o);
          }
          if (pass == 0) {
            STAMPX(pb + 3);
            bar();
            STAMP(pb + 4);
          }
          u32x4 kk[2][2], vv[2][2], pa, pb2;
          f32x16 st[2];
          kk[0][0] = ldl(kv); kk[0][1] = ldl(kv + 1024); kk[1][0] = ldl(kv + 4096); kk[1][1] = ldl(kv + 4096 + 1024);
          SB();
          st[0] = mfma(kk[0][1], qf1, mfma(kk[0][0], qf0, negm));
          SB();
#pragma unroll
          for (int j = 0; j < 8; ++j) {
            f32x16& s = st[j & 1];
            vv[j & 1][0] = ldl(kv + j * 4096 + 2048); vv[j & 1][1] = ldl(kv + j * 4096 + 3072);
            if (j < 6) { kk[j & 1][0] = ldl(kv + (j + 2) * 4096); kk[j & 1][1] = ldl(kv + (j + 2) * 4096 + 1024); }
            SB();
            if (j == 0) { st[1] = mfma(kk[1][0], qf0, negm); SB(); }
            const u32x2 a = ex4(s, 0);
            if (j == 0) st[1] = mfma(kk[1][1], qf1, st[1]); else o = mfma(vv[(j - 1) & 1][0], pa, o);
            SB();
            const u32x2 c = ex4(s, 4);
            if (j == 7) o = mfma(vv[0][1], pb2, o); else if (j > 0) st[(j + 1) & 1] = mfma(kk[(j + 1) & 1][0], qf0, negm);
            SB();
            const u32x2 e = ex4(s, 8);
            if (j == 7) o = mfma(vv[1][0], cat(a, c), o); else if (j > 0) st[(j + 1) & 1] = mfma(kk[(j + 1) & 1][1], qf1, st[(j + 1) & 1]);
            SB();
            const u32x2 f = ex4(s, 12);
            if (j == 7) o = mfma(vv[1][1], cat(e, f), o); else if (j > 0) o = mfma(vv[(j - 1) & 1][1], pb2, o);
            SB();
            pa = cat(a, c); pb2 = cat(e, f);
            if (j == 0) { pa &= mk0; pb2 &= mk1; PIN(pa); PIN(pb2); }
          }
          l = upper_val(o[12]);
          if (pass > 0 || __all(l < LMAX)) break;
          RET(13);
          m = tile_max(0, 1, m);
#pragma unroll 1
          for (int j = 1; j < 8; ++j) m = tile_max(j, 0, m);
        }
        arrive(4);
        o *= __builtin_amdgcn_rcpf(l);
      }
      DUMP(1, o, 0, 1.f);
      store_o(pack8<0>(o), pack8<1>(o), WS_XO + OBUF(layer) * XO_B);
      CALS(3 * layer + 1);
      STAMPX(pb + 6);
      lptr bown, both;
      {
        int ln = lane; PIN(ln);
        const int q = (ln >> 2) & 3, rp = ((ln >> 2) & 4) + (ln & 3), lam = (rp & 4) | ((rp & 1) << 1) | ((rp >> 1) & 1);
        const int pe = lam, po = 6 + ((0x01327654 >> (4 * lam)) & 7);
        const lptr rb = lds + L_WO + kh * SLAB + (8 * (ln >> 5) + q) * 128;
        const lptr ae = rb + ((pe * 8) ^ ((q >> 1) * 64)), ao = rb + ((po * 8) ^ ((q >> 1) * 64));
        bown = kh ? ao : ae; both = kh ? ae : ao;
        PIN(bown); PIN(both);
      }
      unsigned base = WS_XO + OBUF(layer) * XO_B + XOFF + kh * 12288;
      int v16 = ln16, vs = (v16 >> 1) + ((v16 >> 1) & 256);
      PIN(vs);
      release(3 * layer + 1);
      STAMP(pb + 7);
      f32x16 xn, xo;
      u32x4 wa[4];
        auto tr = [](lptr p) {
          bf16x4 a0 = __builtin_amdgcn_ds_read_tr16_b64_v4bf16((LAS bf16x4*)p), a1 = __builtin_amdgcn_ds_read_tr16_b64_v4bf16((LAS bf16x4*)(p + 512));
          return __builtin_bit_cast(u32x4, __builtin_shufflevector(a0, a1, 0, 1, 2, 3, 4, 5, 6, 7));
        };
      for (;;) {
        PIN(v16); PIN(vs); asm volatile("" : "+s"(base));
        u32x2 sp[4];
        u32x4 wb[4];
#pragma unroll
        for (int u = 0; u < 4; ++u) { sp[u] = __builtin_amdgcn_raw_buffer_load_b64(rWS, vs + (u >> 1) * 1024 + (u & 1) * 256, base + 24576 - kh * 10240, SC1); SB(); if (u & 1) PACED(); }
#pragma unroll
        for (int s = 0; s < LAO; ++s) { xf[2 + s] = __builtin_amdgcn_raw_buffer_load_b128(rWS, v16 + (s & 3) * 1024, base + (s >> 2) * 4096, SC1); SB(); if (s < 3) wb[s] = tr(both + s * 2048); else if (s < 6) wa[s - 3] = tr(bown + (s - 3) * 2048); SB(); if (s < 6) PACE2(); else PACED(); }
        xf[0] = cat(sp[0], sp[1]); xf[1] = cat(sp[2], sp[3]);
        xn = xs; xo = Z16;
        unsigned m = 1;
#pragma unroll
        for (int i = 0; i < 14; ++i) {
          if (i + LAO < 12) { const int s = i + LAO; xf[2 + s] = __builtin_amdgcn_raw_buffer_load_b128(rWS, v16 + (s & 3) * 1024, base + (s >> 2) * 4096, SC1); SB(); }
          if (i + 3 < 14) wb[(i + 3) & 3] = tr(both + (i + 3) * 2048);
          SB();
          xo = mfma(wb[i & 3], xf[i], xo);
          SB();
          m = max4(m, xf[i]); PIN(m);
          if (i >= LAG) {
            wa[(i - LAG + 3) & 3] = tr(bown + (i - LAG + 3) * 2048);
            SB();
            xn = mfma(wa[(i - LAG) & 3], xf[i - LAG], xn);
          }
          SB();
        }
        if (__builtin_expect(!__any(m == sent), 1)) break;
        if (--budget == 0) sent = 0u;
        RET(layer * 3 + 2);
        __builtin_amdgcn_s_sleep(BACK);
        asm volatile("" ::: "memory");
      }
      {
        const lptr ex = lds + L_WQ + ln16;
        f32x4 t[4];
#pragma unroll
        for (int i = 14 - LAG; i < 14; ++i) {
          if (i + 3 < 14) wa[(i + 3) & 3] = tr(bown + (i + 3) * 2048);
          SB();
          xn = mfma(wa[i & 3], xf[i], xn);
          SB();
          if (i == 15 - LAG) {
#pragma unroll
            for (int g = 0; g < 4; ++g) *(LAS f32x4*)(ex + w * 4096 + g * 1024) = (f32x4){xo[4 * g], xo[4 * g + 1], xo[4 * g + 2], xo[4 * g + 3]};
          }
          if (i == 11) {
            bar();
#pragma unroll
            for (int g = 0; g < 4; ++g) t[g] = *(LAS f32x4*)(ex + (w ^ 1) * 4096 + g * 1024);
          }
          SB();
        }
#pragma unroll
        for (int g = 0; g < 4; ++g) {
#pragma unroll
          for (int j = 0; j < 4; ++j) xs[4 * g + j] = xn[4 * g + j] + t[g][j];
        }
      }
      STAMPX(pb + 8);
      if (layer == NL - 1) {
        const rsrc_t rOUT = __builtin_amdgcn_make_buffer_rsrc((void*)out, 0, SEQ * DM * 2, 0x00020000);
        const u32x4 f0 = pack8<0>(xs), f1 = pack8<1>(xs);
        int ln = lane; PIN(ln);
        const int row = (ln & 31) * 896, up = ln >> 5;
        const int ob = (tb * 4 + tx) * 32 * 896 + 7 * pair * 16;
        st128<0>(f0, rOUT, row + up * 16, ob + 5 * kh * 16);
        st128<0>(f1, rOUT, row + up * OOB, ob + (2 + 2 * kh) * 16);
        __builtin_amdgcn_raw_buffer_store_b64(lo2(f1), rOUT, row + (1 - up) * OOB, ob + 3 * 16 + kh * 8, 0);
      } else {
        const u32x4 f0 = pack8<0>(xs);
        store_x0(f0, WS_XO + XBUF(layer) * XO_B);
        u32x4 f1 = pack8<1>(xs);
        f1[2] &= lomask; f1[3] &= lomask;
        float s1 = 0.f, s2 = 0.f, t1 = 0.f, t2 = 0.f;
#pragma unroll
        for (int j = 0; j < 4; ++j) { const unsigned u = f0[j], v = f1[j]; s1 = dot2(u, ONES, s1); s2 = dot2(u, u, s2); t1 = dot2(v, ONES, t1); t2 = dot2(v, v, t2); }
        s1 += t1; s2 += t2;
        red2<false>(s1, s2);
        f1[2] |= ~lomask & F2U(s1); f1[3] |= ~lomask & F2U(s2);
        store_x1(f1, WS_XO + XBUF(layer) * XO_B);
        CALS(3 * layer + 2);
        STAMPX(pb + 9);
      }
      asm volatile("" : "=v"(ak), "=v"(av), "=v"(rstd), "=v"(mf), "=v"(phoff));
#pragma unroll
      for (int i = 14; i < 28; ++i) asm volatile("" : "=v"(xf[i]));
    }
    STAMP(61); RSTAMP(63);
    TOT1
    if (b == 0 && w == 0) __builtin_amdgcn_raw_buffer_store_b64((u32x2){(unsigned)(epoch + 1), (unsigned)((epoch + 1) >> 32)}, rWS, WS_EPOCH, 0, SC1);
  } else {
    STAMPX(53);
    u32x4 pA[21], pB[21];
    unsigned tch0, tch1;
    asm volatile("s_load_dword %0, %2, 0x0\n\ts_load_dword %1, %3, 0x0" : "=&s"(tch0), "=&s"(tch1)
                 : "s"(w == 0 ? (const void*)ws : w == 1 ? (const void*)wqkvB : w == 2 ? (const void*)woA : (const void*)wphi), "s"(w < 2 ? (const void*)woB : (const void*)out));
    dma_x0(0, 4);
    unsigned warm0;
    {
      u64 pc; asm volatile("s_getpc_b64 %0" : "=s"(pc));
      const rsrc_t rs = __builtin_amdgcn_make_buffer_rsrc(w == 3 ? (void*)(pc & ~127ull) : (void*)wqkvA, 0, w == 3 ? CODE_B : DM * 3 * DM * 2, 0x00020000);
      const int n = w == 3 ? 9 : 13;
      warm0 = __builtin_amdgcn_raw_buffer_load_b32(rs, lane < n ? (lane + (w == 3 ? (b >> 3) : b * 3 + w) * n) * 128 : OOB, 0, 0);
      SB();
    }
#pragma unroll
    for (int i = 0; i < 7; ++i) { mkrow(i); PIN(krow[i]); }
    lptr trb = make_trb(); PIN(trb);
    u32x4 id0 = ident_frag(0, r, hh); PIN(id0);
    SB();
    dma_x0(4, 28);
#pragma unroll
    for (int i = 0; i < NV0; ++i) pA[14 + i] = ldw(rWA, i, 2688, 1792);
    STAMPX(54);
    asm volatile("s_waitcnt vmcnt(%2) lgkmcnt(0)" :: "s"(tch0), "s"(tch1), "n"(NV0) : "memory");
    const u32x4 sv = {SENT, SENT, SENT, SENT};
    auto clean_kv = [&](int layer) { if (layer < NL) st128<SC1>(sv, rWS, lane * 16, WS_MID + (h * 4 + MBUF(layer)) * MID_B + (tb * 4 + w) * 1024); };
    auto clean_o = [&](int layer) { if (layer < NL) store_o(sv, sv, WS_XO + OBUF(layer) * XO_B); };
    auto clean_x = [&](int layer) { if (layer < NL - 1) { store_x0(sv, WS_XO + XBUF(layer) * XO_B); store_x1(sv, WS_XO + XBUF(layer) * XO_B); } };
    unsigned r0 = (unsigned)__builtin_amdgcn_s_memrealtime(), c0 = (unsigned)__builtin_amdgcn_s_memtime(), tann = 0;
    auto mark = [&](int cyc) {
      const unsigned r1 = (unsigned)__builtin_amdgcn_s_memrealtime(), c1 = (unsigned)__builtin_amdgcn_s_memtime();
      tann = r1 + LAND + (unsigned)((float)cyc * (float)(r1 - r0) * __builtin_amdgcn_rcpf((float)(c1 - c0)));
      r0 = r1; c0 = c1;
    };
    auto push = [&](int hop, unsigned voff) {
      CALA(hop);
      __builtin_amdgcn_raw_buffer_store_b64((u32x2){tag[0], (tag[1] & 0xfff00000u) | (tann & 0xfffffu)}, rWS, voff, WS_FLAGS + hop * HOP_B, SC1);
    };
    auto push_grp = [&](int hop) { push(hop, lane < 16 ? (tb * 16 + lane) * 128 + h * 8 : OOB); };
    auto push_mid = [&](int layer) {
      if (layer & 1) push(3 * layer, (lane < 2 && tb + 1 + lane < 16) ? ((tb + 1 + lane) * 16 + h) * 128 + lane * 8 : OOB);
      else push(3 * layer, lane < 16 ? (lane * 16 + h) * 128 + tb * 8 : OOB);
    };
    const rsrc_t rWB = __builtin_amdgcn_make_buffer_rsrc((void*)wqkvB, 0, DM * 3 * DM * 2, 0x00020000);
    const rsrc_t rOA = __builtin_amdgcn_make_buffer_rsrc((void*)woA, 0, DM * DM * 2, 0x00020000);
    const rsrc_t rOB = __builtin_amdgcn_make_buffer_rsrc((void*)woB, 0, DM * DM * 2, 0x00020000);
    auto spin = [&](int k, unsigned n) { while (__builtin_amdgcn_readfirstlane(cnt[k]) < n) __builtin_amdgcn_s_sleep(1); asm volatile("" ::: "memory"); };
    auto wait_h = [&](int hop, int mask) {
      if (w == 0) { if (mask >= 0) poll(hop, mask); cnt[1] = hop + 1; }
      else while (__builtin_amdgcn_readfirstlane(cnt[1]) != hop + 1) { }
      asm volatile("" ::: "memory");
    };
    const unsigned pkb = WS_PK + h * 262144;
    auto ldp = [&](unsigned off) { const u32x4 v = __builtin_amdgcn_raw_buffer_load_b128(rWS, lane * 16, pkb + off + w * 1024, SC1); SB(); return v; };
    STAMPX(55);
    bar();
#pragma unroll
    for (int i = NV0; i < 7; ++i) pA[14 + i] = ldw(rWA, i, 2688, 1792);
    load_epoch();
    const unsigned warm1 = __builtin_amdgcn_raw_buffer_load_b32(rWS, (FW && w == 0 && lane < 11) ? lane * HOP_B : OOB, WS_FLAGS + me * 128, SC1);
    SB();
    u32x4 pw[4];
    {
      const unsigned act = tb < 14 ? 0u : OOB;
#pragma unroll
      for (int j = 0; j < 2; ++j) {
        const int st = 2 * tb + j, p = (st * 43) >> 7, m = st - 3 * p;
        const int c = st < 24 ? 7 * p + ((0x520 >> (4 * m)) & 15) + hh * (1 + (m & 1)) : 7 * (2 * (st - 24) + hh) + 3;
        pw[j] = __builtin_amdgcn_raw_buffer_load_b128(rWB, ((8 * c + ((lane >> 2) & 7)) * 2688 + sub * 16) | (w < 3 ? act : OOB), col + (w == 2 ? 0 : 896 + 896 * w), 0);
        SB();
      }
      {
        const int n = 4 * tb + w, k2 = n >= 28, i = (n - 28 * k2) >> 1, st = i - 2, p = (st * 43) >> 7, m = st - 3 * p;
        const int c = 28 * k2 + (i < 2 ? 14 * i + 3 + 7 * (n & 1) : 7 * p + ((0x520 >> (4 * m)) & 15) + (n & 1) * (1 + (m & 1)));
        const unsigned vo = (c * 7168 + (lane >> 3) * 896 + 112 * pair + (((lane & 7) ^ ((lane >> 2) & 4)) * 16)) | act;
        pw[2] = __builtin_amdgcn_raw_buffer_load_b128(rOA, vo, 0, 0); SB();
        pw[3] = __builtin_amdgcn_raw_buffer_load_b128(rOB, vo, 0, 0); SB();
      }
    }
    __builtin_amdgcn_s_setprio(3);
    STAMPX(57);
    spin(5, 4);
    put_slab(pA + 14, 0, 1);
#pragma unroll
    for (int j = 0; j < NQ1; ++j) pA[j] = ldw(rWA, j, 2688, 0);
    sum_slab(0, 1, trb);
#pragma unroll
    for (int j = NQ1; j < NQ0; ++j) pA[j] = ldw(rWA, j, 2688, 0);
#pragma unroll
    for (int i = 0; i < 7; ++i) pA[7 + i] = ldl(lds + L_WK + i * 4096 + ht * 16);
    STAMPX(58);
    bar();
    if (w == 3) mark(TKA0);
    __builtin_amdgcn_s_setprio(0);
#pragma unroll
    for (int j = NQ0; j < 7; ++j) pA[j] = ldw(rWA, j, 2688, 0);
    {
      const unsigned keep01 = (odd && sub == 0) ? 0u : ~0u, keep23 = (!odd && sub == 3) ? 0u : ~0u, act = (tb < 14 ? 0u : OOB) | (lane * 16);
#pragma unroll
      for (int j = 0; j < 2; ++j) st128<SC1>(pw[j] & (u32x4){keep01, keep01, keep23, keep23}, rWS, w < 3 ? act : OOB, pkb + w * SLAB + (2 * tb + j) * 1024);
      st128<SC1>(pw[2], rWS, act, pkb + 3 * SLAB + (4 * tb + w) * 1024);
      st128<SC1>(pw[3], rWS, act, pkb + 5 * SLAB + (4 * tb + w) * 1024);
    }
    clean_kv(0); clean_o(0);
    make_tag();
    STAMPX(59);
    first_slab(pA, 0, 2, trb);
    drain();
    arrive(3);
    if (w == 3) { spin(3, 4); push_mid(0); }
    asm volatile("" :: "v"(warm0), "v"(warm1));
    __builtin_amdgcn_s_setprio(3);
    u32x4 po[14];
    auto swap_wo = [&](bool keep) {
      lptr d = lds + L_WO + ht * 16; PIN(d);
#pragma unroll
      for (int i = 0; i < 14; ++i) { u32x4 t; if (keep) t = ldl(d + i * 4096); *(LAS u32x4*)(d + i * 4096) = po[i]; if (keep) po[i] = t; SB(); }
    };
    STAMPX(60);
#pragma unroll
    for (int layer = 0; layer < NL; ++layer) {
      const int type = layer & 1;
      const int pb = 2 + layer * 14;
      const unsigned mid = WS_MID + (h * 4 + MBUF(layer)) * MID_B;
      if (type == 0) {
        lptr ta0, ta1;
        {
          const int rb = (lane >> 4) & 1, q = (lane >> 2) & 3, p = lane & 3;
          const int rp = 4 * rb + p, lam = (rp & 4) | ((rp & 1) << 1) | ((rp >> 1) & 1);
          ta0 = lds + L_SCR + (8 * hh + q) * 64 + (((lam >> 1) ^ (q >> 1)) * 16) + (lam & 1) * 8;
          ta1 = lds + L_SCR + (8 * hh + q + 4) * 64 + (((lam >> 1) ^ (q >> 1) ^ 2) * 16) + (lam & 1) * 8;
          PIN(ta0); PIN(ta1);
        }
        bar();
        STAMPX(pb + 0);
        {
          auto tr2 = [&](unsigned off) {
            bf16x4 a0 = __builtin_amdgcn_ds_read_tr16_b64_v4bf16((LAS bf16x4*)(ta0 + off));
            bf16x4 a1 = __builtin_amdgcn_ds_read_tr16_b64_v4bf16((LAS bf16x4*)(ta1 + off));
            return __builtin_bit_cast(u32x4, __builtin_shufflevector(a0, a1, 0, 1, 2, 3, 4, 5, 6, 7));
          };
          const unsigned so = (w >> 1) * 8192;
          u32x4 fa[4], fb[4];
          f32x16 kv = Z16;
#pragma unroll
          for (int i = 0; i < 3; ++i) { fa[i] = tr2(so + i * 1024); fb[i] = tr2(16384 + i * 1024); }
          SB();
#pragma unroll
          for (int i = 0; i < 8; ++i) {
            if (i + 3 < 8) { fa[(i + 3) & 3] = tr2(so + (i + 3) * 1024); fb[(i + 3) & 3] = tr2(16384 + (i + 3) * 1024); }
            SB();
            kv = mfma(fa[i & 3], fb[i & 3], kv);
            SB();
          }
          st128<SC1>((w & 1) ? pack8<1>(kv) : pack8<0>(kv), rWS, lane * 16, mid + (tb * 4 + w) * 1024);
          CALS(3 * layer);
        }
        STAMPX(pb + 1);
        if (layer == 0) clean_x(0);
        wait_h(3 * layer, 15);
        STAMPX(pb + 2);
        f32x16 sum;
        for (;;) {
          sum = Z16;
          unsigned m = 0;
          u32x4 pr[16];
          const unsigned g = mid + w * 1024;
#pragma unroll
          for (int i = 0; i < 16; ++i) {
            pr[i] = __builtin_amdgcn_raw_buffer_load_b128(rWS, lane * 16, g + i * 4096, SC1); SB();
            if (i < 15) PACED();
          }
          if (w == 3) { mark(layer ? TOA : TOA0); push_grp(3 * layer + 1); SB(); }
#pragma unroll
          for (int i = 0; i < 16; ++i) {
            if (layer == 0 && i < NPO) po[i] = ldp(3 * SLAB + i * 4096);
            sum = mfma(id0, pr[i], sum); m = max4(m, pr[i]); PIN(m); SB();
          }
          if (!__any(m == SENT) || budget == 0) break;
          --budget; RET(layer * 3 + 1);
          asm volatile("" ::: "memory");
        }
        *(LAS u32x4*)(lds + L_KVF + w * 1024 + lane * 16) = pack8<0>(sum);
        STAMPX(pb + 3);
        bar();
        if (layer == 0) {
#pragma unroll
          for (int i = NPO; i < 14; ++i) po[i] = ldp(3 * SLAB + i * 4096);
        } else __builtin_amdgcn_s_sleep(5);
      } else {
        gather_all(layer, true);
        STAMPX(pb + 0);
        u32x4 t[8];
        wait_h(3 * layer, tb >= 2 ? 1 : tb - 1);
        STAMPX(pb + 2);
        if (tb - 2 + (w >> 1) >= 0) {
          const unsigned g = mid + (tb * 4 - 8 + 2 * w) * 4096;
          for (;;) {
            unsigned c[2] = {0u, 0u};
#pragma unroll
            for (int i = 0; i < 8; ++i) { t[i] = __builtin_amdgcn_raw_buffer_load_b128(rWS, lane * 16 + (i & 3) * 1024, g + (i >> 2) * 4096, SC1); SB(); if (i < 7) PACED(); }
#pragma unroll
            for (int i = 0; i < 8; ++i) {
              c[i >> 2] = add4(c[i >> 2], t[i]); PIN(c[i >> 2]);
              if ((i & 3) == 1) t[i][3] &= (unsigned)hh - 1u;
              *(LAS u32x4*)(lds + L_SCR + 2 * w * 4096 + i * 1024 + lane * 16) = t[i];
              SB();
            }
            const auto cs = __builtin_amdgcn_permlane32_swap(c[0], c[1], false, false);
            if (__all(cs[0] + cs[1] == tag[0] + layer) || budget == 0) break;
            --budget; RET(layer * 3 + 1);
            __builtin_amdgcn_s_sleep(8);
            asm volatile("" ::: "memory");
          }
        } else {
#pragma unroll
          for (int i = 0; i < 8; ++i)
            *(LAS u32x4*)(lds + L_SCR + 2 * w * 4096 + i * 1024 + lane * 16) = (u32x4){0u, 0u, ((i & 3) == 1 && hh) ? 0xff80u : 0u, 0u};
        }
        drain();
        STAMPX(pb + 3);
        bar();
        if (w == 3) { mark(layer == 1 ? TOB1 : TOB); push_grp(3 * layer + 1); }
        __builtin_amdgcn_s_setprio(0);
        if (layer == 1) {
          clean_x(1); clean_kv(2);
#pragma unroll
          for (int i = 0; i < 14; ++i) po[i] = ldp(5 * SLAB + i * 4096);
          swap_wo(true);
          drain();
          spin(4, 4);
        }
      }
      __builtin_amdgcn_s_setprio(0);
      if (layer == 0) {
        swap_wo(false);
      } else if (layer < NL - 1) {
        write_slab((type == 0 ? pB : pA) + 7, L_WK);
      }
      STAMPX(pb + 4);
      bar();
      if (w == 3 && layer < NL - 1) { mark(layer == 2 ? TX2 : layer ? TX : TX0); push_grp(3 * layer + 2); }
      STAMPX(pb + 5);
      if (layer > 0 && layer < NL - 1) { __builtin_amdgcn_s_sleep(WSL); write_slab((type == 0 ? pB : pA) + 14, L_WV); }
      if (layer == 0) {
        __builtin_amdgcn_s_sleep(12);
#pragma unroll
        for (int i = 0; i < 21; ++i) pB[(i + 7) % 21] = ldp(i * 4096);
      }
      bar();
      if (layer < NL - 1) {
        __builtin_amdgcn_s_sleep(SWS);
        if (layer == 0) { write_slab(pB + 7, L_WK); write_slab(pB + 14, L_WV); sum_slab(1, 0, trb); sum_slab(1, 1, trb); }
        else swap_wo(layer == 1);
        bar();
        __builtin_amdgcn_s_sleep(WSL);
        write_slab(type == 0 ? pB : pA, L_WQ);
        if (layer == 0) sum_slab(1, 2, trb);
        arrive(0);
        while (!__all(cnt[8 + (lane & 3)] == layer + 1u)) __builtin_amdgcn_s_sleep(1);
        if (w == 3) { mark(layer == 0 ? TKB1 : layer == 1 ? TKA : TKB); push_mid(layer + 1); }
        __builtin_amdgcn_s_sleep(CSL);
        clean_o(layer + 1);
        if (layer == 1) { clean_x(2); drain(); }
        __builtin_amdgcn_s_setprio(3);
      }
    }
  }
}
}

extern "C" void kernel_launch(void* const* d_in, const int* in_sizes, int n_in, void* d_out, int out_size, void* d_ws, size_t ws_size, hipStream_t stream) {
  (void)hipFuncSetAttribute((const void*)fused_kernel, hipFuncAttributeMaxDynamicSharedMemorySize, L_END);
  fused_kernel<<<256, 512, L_END, stream>>>((const u16*)d_in[0], (const u16*)d_in[1], (const u16*)d_in[2], (const u16*)d_in[3],
                                            (const u16*)d_in[5], (const u16*)d_in[6], (u16*)d_out, (unsigned char*)d_ws);
}
```

```cpp
#define NL 4
#include <hip/hip_runtime.h>
#include <hip/hip_bf16.h>

typedef unsigned short u16;
typedef unsigned long long u64;
typedef __bf16 bf16x8 __attribute__((ext_vector_type(8)));
typedef __bf16 bf16x4 __attribute__((ext_vector_type(4)));
typedef __bf16 bf16x2 __attribute__((ext_vector_type(2)));
typedef float f32x16 __attribute__((ext_vector_type(16)));
typedef float f32x4 __attribute__((ext_vector_type(4)));
typedef float f32x2 __attribute__((ext_vector_type(2)));
typedef unsigned u32x4 __attribute__((ext_vector_type(4)));
typedef unsigned u32x2 __attribute__((ext_vector_type(2)));
#define LAS __attribute__((address_space(3)))
#define GAS __attribute__((address_space(1)))
#define DI __device__ __forceinline__
#define SB() __builtin_amdgcn_sched_barrier(0)
#define PIN(x) asm volatile("" : "+v"(x))
typedef __amdgpu_buffer_rsrc_t rsrc_t;
typedef LAS unsigned char* lptr;

namespace {
constexpr int SEQ = 2048, DM = 448;
constexpr int SC1 = 16;
constexpr unsigned WS_EPOCH = 0;
constexpr unsigned WS_FLAGS = 4096;
constexpr unsigned HOP_B = 256 * 128;
constexpr unsigned TILE_B = 32768;
constexpr unsigned XO_B = 4 * TILE_B;
constexpr unsigned WS_XO = 1u << 20;
constexpr unsigned MID_B = 64 * 4096;
constexpr unsigned WS_MID = WS_XO + 16 * 8 * XO_B;
constexpr unsigned WS_PK = WS_MID + 64 * MID_B;
constexpr unsigned WS_PROF = WS_PK + 16 * 262144;
constexpr unsigned WS_END = WS_PROF + 256 * 2048;
constexpr unsigned SLAB = 448 * 64;
constexpr unsigned L_WK = 0, L_WV = SLAB, L_WQ = 2 * SLAB, L_WO = 3 * SLAB;
constexpr unsigned L_SCR = 0;
constexpr unsigned L_X0 = SLAB;
constexpr unsigned L_KVF = 5 * SLAB;
constexpr unsigned L_MK = L_KVF + 4096;
constexpr unsigned L_WPH = L_MK + 2048;
constexpr unsigned L_CS = L_WPH + 2048;
constexpr unsigned L_SYNC = L_CS + 6144;
constexpr unsigned L_PROF = L_SYNC + 64;
constexpr unsigned L_END = L_PROF + 2048;
static_assert(L_END <= 163840 && L_X0 + 4 * SLAB <= L_KVF, "lds");
constexpr unsigned SPIN_CAP = 1u << 15;
constexpr unsigned SENT = 0xffffffffu;
constexpr unsigned OOB = 0x7ff00000u;
constexpr unsigned ONES = 0x3f803f80u;
constexpr unsigned CODE_B = 270 * 128;
constexpr float L2E = 1.4426950408889634f;

DI f32x16 mfma(u32x4 a, u32x4 b, f32x16 c) {
  return __builtin_amdgcn_mfma_f32_32x32x16_bf16(__builtin_bit_cast(bf16x8, a), __builtin_bit_cast(bf16x8, b), c, 0, 0, 0);
}
DI unsigned pk2(float a, float b) { f32x2 t = {a, b}; return __builtin_bit_cast(unsigned, __builtin_convertvector(t, bf16x2)); }
template <int S_> DI u32x4 pack8(const f32x16& c) {
  return (u32x4){pk2(c[8 * S_], c[8 * S_ + 1]), pk2(c[8 * S_ + 2], c[8 * S_ + 3]), pk2(c[8 * S_ + 4], c[8 * S_ + 5]), pk2(c[8 * S_ + 6], c[8 * S_ + 7])};
}
DI u32x4 cat(u32x2 a, u32x2 b) { return (u32x4){a[0], a[1], b[0], b[1]}; }
DI u32x2 lo2(u32x4 v) { return (u32x2){v[0], v[1]}; }
DI unsigned umax3(unsigned a, unsigned b, unsigned c) { return max(max(a, b), c); }
DI unsigned max4(unsigned m, u32x4 v) { return umax3(umax3(m, v[0], v[1]), v[2], v[3]); }
DI unsigned add4(unsigned m, u32x4 v) { return (m + v[0] + v[1]) + v[2] + v[3]; }
DI unsigned xhalf_addu(unsigned v) { auto r = __builtin_amdgcn_permlane32_swap(v, v, false, false); return r[0] + r[1]; }
DI float fmax3(float a, float b, float c) { return fmaxf(fmaxf(a, b), c); }
template <int CTRL> DI float dppadd(float v) { return v + __builtin_bit_cast(float, __builtin_amdgcn_update_dpp(0, __builtin_bit_cast(int, v), CTRL, 0xf, 0xf, true)); }
DI float fmin3(float a, float b, float c) { return fminf(fminf(a, b), c); }
DI f32x16 splat(float v) { f32x16 z; _Pragma("unroll") for (int i = 0; i < 16; ++i) z[i] = v; return z; }
#define Z16 splat(0.f)
DI u32x4 trfrag(lptr p) {
  bf16x4 a0 = __builtin_amdgcn_ds_read_tr16_b64_v4bf16((LAS bf16x4*)p);
  bf16x4 a1 = __builtin_amdgcn_ds_read_tr16_b64_v4bf16((LAS bf16x4*)(p + 256));
  return __builtin_bit_cast(u32x4, __builtin_shufflevector(a0, a1, 0, 1, 2, 3, 4, 5, 6, 7));
}
DI u32x4 ldl(lptr p) { return *(LAS u32x4*)p; }
DI float U2F(unsigned u) { return __uint_as_float(u); }
DI unsigned F2U(float f) { return __float_as_uint(f); }
template <bool MX> DI void red2(float& a, float& b) {
  auto r = __builtin_amdgcn_permlane32_swap(F2U(a), F2U(b), false, false);
  const float t = MX ? fmaxf(U2F(r[0]), U2F(r[1])) : U2F(r[0]) + U2F(r[1]);
  r = __builtin_amdgcn_permlane32_swap(F2U(t), F2U(t), false, false);
  a = U2F(r[0]); b = U2F(r[1]);
}
DI float xhalf_max(float v) { auto r = __builtin_amdgcn_permlane32_swap(F2U(v), F2U(v), false, false); return fmaxf(U2F(r[0]), U2F(r[1])); }
DI float upper_val(float v) { auto r = __builtin_amdgcn_permlane32_swap(F2U(v), F2U(v), false, false); return U2F(r[1]); }
DI float dot2(unsigned u, unsigned v, float c) { return __builtin_amdgcn_fdot2_f32_bf16(__builtin_bit_cast(bf16x2, u), __builtin_bit_cast(bf16x2, v), c, false); }
DI float bflo(unsigned u) { return U2F(u << 16); }
DI float bfhi(unsigned u) { return U2F(u & 0xffff0000u); }
DI void split2(float v, unsigned& hi, unsigned& lo) { hi = pk2(v, v) & 0xffffu; lo = pk2(v - bflo(hi), 0.f) & 0xffffu; }
template <int AUX> DI void st128(u32x4 v, rsrc_t rs, unsigned voff, unsigned soff) {
  SB();
  __builtin_amdgcn_raw_buffer_store_b128(v, rs, voff, soff, AUX);
  SB();
  asm volatile("s_nop 1" :: "v"(v));
  SB();
}
DI void drain() { asm volatile("s_waitcnt vmcnt(0)" ::: "memory"); }
DI void bar() { SB(); asm volatile("s_waitcnt lgkmcnt(0)\n\ts_barrier" ::: "memory"); SB(); }
constexpr int chunk_of(int s, int hh) { return s < 24 ? 7 * (s / 3) + (s % 3 == 0 ? hh : (s % 3 == 1 ? 2 + 2 * hh : 5 + hh)) : 7 * (2 * (s - 24) + hh) + 3; }
constexpr int ord(int i) { return i < 4 ? 24 + i : i - 4; }

#define STAMP(i) do { } while (0)
#define RET(i) do { } while (0)
#define RSTAMP(i) do { } while (0)
#define STAMPX(i) STAMP(i)
#define CALS(hop) do { } while (0)
#define CALA(hop) do { } while (0)
#define TOT0
#define TOT1
#define DUMP(buf, acc, part, sc) do { } while (0)

template <typename F> DI void dual_softmax(const f32x16& p, float c, float wtail, u32x4 (&fp)[2], u32x4 (&fm)[2], F mid) {
  float mx = fmax3(p[0], p[1], p[2]), mn = fmax3(-p[0], -p[1], -p[2]);
#pragma unroll
  for (int i = 3; i < 15; i += 2) { mx = fmax3(mx, p[i], p[i + 1]); mn = fmax3(mn, -p[i], -p[i + 1]); }
  mx = fmax3(mx, p[15], p[15]); mn = fmax3(mn, -p[15], -p[15]);
  red2<true>(mx, mn);
  const float a = -c * mx, b = -c * mn;
  float nc = -c; PIN(nc);
  f32x16 ep = p * c + a, em = p * nc + b;
  SB();
#pragma unroll
  for (int i = 0; i < 16; ++i) ep[i] = __builtin_amdgcn_exp2f(ep[i]);
  PIN(ep);
  mid();
#pragma unroll
  for (int i = 0; i < 16; ++i) em[i] = __builtin_amdgcn_exp2f(em[i]);
  f32x2 sp = {ep[0], ep[1]}, sm = {em[0], em[1]};
#pragma unroll
  for (int i = 2; i < 12; i += 2) { sp += (f32x2){ep[i], ep[i + 1]}; sm += (f32x2){em[i], em[i + 1]}; }
  sp += wtail * ((f32x2){ep[12], ep[13]} + (f32x2){ep[14], ep[15]});
  sm += wtail * ((f32x2){em[12], em[13]} + (f32x2){em[14], em[15]});
  float u = sp[0] + sp[1], v = sm[0] + sm[1];
  red2<false>(u, v);
  ep *= __builtin_amdgcn_rcpf(u); em *= __builtin_amdgcn_rcpf(v);
  fp[0] = pack8<0>(ep); fp[1] = pack8<1>(ep); fm[0] = pack8<0>(em); fm[1] = pack8<1>(em);
}
DI u32x4 ident_frag(int s, int r, int hh) {
  const unsigned val = 0x3f80u << (16 * (r & 1));
  const int idx = (((r >> 2) ^ hh) & 1) * 8 + (r >> 4) * 4 + ((r >> 2) & 2) + ((r >> 1) & 1) - 4 * s;
  return (u32x4){idx == 0 ? val : 0u, idx == 1 ? val : 0u, idx == 2 ? val : 0u, idx == 3 ? val : 0u};
}
#define CHK1 3
#define CHK2 12
template <typename S, typename A> DI void steps28(S step, A abort) {
#pragma unroll
  for (int i = 0; i <= CHK1; ++i) step(i);
  if (abort()) return;
#pragma unroll
  for (int i = CHK1 + 1; i < 28; ++i) step(i);
}
template <int PD, bool TR, typename F, typename G, typename A> DI void gemm1(f32x16& acc, lptr wb, const u32x4 (&xf)[28], F pre, G post, A abort) {
  auto frag = [](lptr p) { return TR ? trfrag(p) : ldl(p); };
  u32x4 wr[PD];
#pragma unroll
  for (int i = 0; i < PD - 1; ++i) wr[i] = frag(wb + ord(i) * 1024);
  SB();
  steps28([&](int i) __attribute__((always_inline)) {
    pre(i);
    if (i + PD - 1 < 28) wr[(i + PD - 1) % PD] = frag(wb + ord(i + PD - 1) * 1024);
    SB();
    acc = mfma(wr[i % PD], xf[i], acc);
    SB();
    post(i);
    SB();
  }, abort);
}
template <typename H, typename F, typename G, typename A>
DI void gemm2(f32x16& ak, f32x16& av, lptr bk, const u32x4 (&xf)[28], H head, F pre, G post, A abort) {
  u32x4 wk[3], wv[3];
  head([&](int u) {
    if (u == 0) wv[0] = ldl(bk + SLAB + ord(0) * 1024);
    if (u == 1) wk[0] = ldl(bk + ord(0) * 1024);
    if (u == 2) wv[1] = ldl(bk + SLAB + ord(1) * 1024);
    if (u == 3) wk[1] = ldl(bk + ord(1) * 1024);
    SB();
  });
  steps28([&](int i) __attribute__((always_inline)) {
    pre(i);
    if (i + 2 < 28) wv[(i + 2) % 3] = ldl(bk + SLAB + ord(i + 2) * 1024);
    SB();
    av = mfma(wv[i % 3], xf[i], av);
    SB();
    if (i + 2 < 28) wk[(i + 2) % 3] = ldl(bk + ord(i + 2) * 1024);
    post(i, 0);
    SB();
    ak = mfma(wk[i % 3], xf[i], ak);
    SB();
    post(i, 1);
    SB();
  }, abort);
}
#define NOAB [] { return false; }
#define EARLYAB [&] { return __builtin_expect(__any(m == sent), 0); }
#define LA 12
#define XH 27
#define VSLEEP 8
#define QSLEEP 28
#define LAO 8
#define XBAR 26
#define DOA 3
#define DOB 6
#define DX 3
#define KVI 6
#define KVB 3
#define NKB 5
#define XM 4
#ifndef NV0
#define NV0 5
#endif
#define NK2 8
#define NK3 9
#define PACE 8
#define LAG 6
#define PACED() do { if (PACE) asm volatile("s_nop %0" :: "n"(PACE)); SB(); } while (0)
#define PSLP 4
#define RDLY 0
#define KDLY 0
#define LAND 10
#define TKA0 4000
#define TKA 3500
#define TKB1 2150
#define TKB 2400
#define TOA0 2050
#define TOA 1850
#define TOB1 2750
#define TOB 2520
#define TX0 3800
#define TX 2850
#define BACK 8
#define NQ0 2
#define NQB 0
#define TX2 3200
#define ND0 10
#define NPO 14
#define SWS 6
#define LMAX 1.8446744e19f
#define CSL 2
#define WSL 4
#define PACE2() do { asm volatile("s_nop %0" :: "n"(PACE - 2)); SB(); } while (0)
#define NQ1 4
#define REUSE 4
#define OBUF(l) ((REUSE & 1) ? 0 : 2 * (l))
#define XBUF(l) ((REUSE & 2) ? 1 + 2 * ((l) & 1) : 2 * (l) + 1)
#define MBUF(l) (((l) & 1) ? ((REUSE & 4) ? 1 : (l)) : ((REUSE & 8) ? 0 : (l)))
#define FW 1
#define FLOORK 45
#ifndef NV1
#define NV1 3
#endif
#define PDELAY 14

__global__ void __launch_bounds__(512, 2) fused_kernel(const u16* __restrict__ x0, const u16* __restrict__ wqkvA, const u16* __restrict__ wphi,
                                                       const u16* __restrict__ woA, const u16* __restrict__ wqkvB, const u16* __restrict__ woB,
                                                       u16* __restrict__ out, unsigned char* ws) {
  extern __shared__ __attribute__((aligned(16))) unsigned char smem[];
  const lptr lds = (lptr)smem;
  const int tid = threadIdx.x, lane = tid & 63, wave = __builtin_amdgcn_readfirstlane(tid >> 6);
  TOT0
  const int w = wave & 3;
  const int r = lane & 31, hh = lane >> 5;
  const int b = blockIdx.x;
  const int tb = ((b >> 1) & 3) * 4 + (b >> 6), h = (b & 1) * 8 + ((b >> 3) & 7);
  const int me = tb * 16 + h;
  const int odd = h & 1, pair = h >> 1;
  const int T = tb * 4 + w;
  const unsigned TOFF = tb * 8 * XO_B + w * TILE_B;
  const int kh = w & 1, tx = 2 * odd + (w >> 1);
  const unsigned XOFF = tb * 8 * XO_B + tx * TILE_B;
  const rsrc_t rWS = __builtin_amdgcn_make_buffer_rsrc((void*)ws, 0, WS_END, 0x00020000);
  unsigned budget = SPIN_CAP;
  u64 epoch;
  u32x2 tag;
  u32x2 ev;
  auto load_epoch = [&]() { ev = __builtin_amdgcn_raw_buffer_load_b64(rWS, 0, WS_EPOCH, SC1); SB(); };
  auto make_tag = [&]() {
    epoch = (u64)(unsigned)__builtin_amdgcn_readfirstlane(ev[0]) | ((u64)(unsigned)__builtin_amdgcn_readfirstlane(ev[1]) << 32);
    u64 z = epoch + 0x9E3779B97F4A7C15ull;
    z = (z ^ (z >> 30)) * 0xBF58476D1CE4E5B9ull; z = (z ^ (z >> 27)) * 0x94D049BB133111EBull; z ^= z >> 31;
    unsigned t0 = (unsigned)z, t1 = (unsigned)(z >> 32);
    asm volatile("" : "+s"(t0), "+s"(t1));
    tag = (u32x2){t0, t1};
  };
  volatile LAS unsigned* const cnt = (volatile LAS unsigned*)(lds + L_SYNC);
  auto arrive = [&](int k) { if (lane == 0) asm volatile("ds_add_u32 %0, %1" :: "v"(lds + L_SYNC + 4 * k), "v"(1u) : "memory"); };
  auto gather_all = [&](int layer, bool nap) {
    while (__builtin_amdgcn_readfirstlane(*cnt) < 8u * layer + 4u) { if (nap) __builtin_amdgcn_s_sleep(2); }
    asm volatile("" ::: "memory");
  };
  auto poll = [&](int hop, int mask, int floor = 0) {
    const unsigned t0 = (unsigned)__builtin_amdgcn_s_memrealtime() + floor;
    int ln = lane & mask; PIN(ln);
    u32x2 v;
    for (;;) {
      v = __builtin_amdgcn_raw_buffer_load_b64(rWS, ln * 8, WS_FLAGS + hop * HOP_B + me * 128, SC1);
      asm volatile("" ::: "memory");
      if (__all(v[0] == tag[0] && ((v[1] ^ tag[1]) >> 20) == 0)) break;
      if (budget <= 1) break;
      --budget;
      if (PSLP) __builtin_amdgcn_s_sleep(PSLP);
    }
    const unsigned t = (lane < 32 ? v[1] : t0) << 12;
    for (int n = 0; n < 64; ++n) {
      const unsigned now = (unsigned)__builtin_amdgcn_s_memrealtime() << 12;
      if (!__any((int)(t - now) > 0)) break;
    }
    asm volatile("" ::: "memory");
  };
  auto make_trb = [&]() {
    const int rb = (lane >> 4) & 1, q = (lane >> 2) & 3, p = lane & 3;
    const int rp = 4 * rb + p, lam = (rp & 4) | ((rp & 1) << 1) | ((rp >> 1) & 1);
    const int pc = ((odd ? 0x01327654 : 0x76543210) >> (4 * lam)) & 7;
    return lds + (8 * hh + q) * 64 + pc * 8;
  };
  auto store_x0 = [&](u32x4 f0, unsigned base) { int ln = lane; PIN(ln); st128<SC1>(f0, rWS, ln * 16, base + XOFF + (3 * pair + 2 * kh) * 1024); };
  auto store_x1 = [&](u32x4 f1, unsigned base) {
    int ln = lane; PIN(ln);
    st128<SC1>(f1, rWS, ln * 16 + __umul24(ln >> 5, 24576 - 512 - 1024 - pair * 2048), base + XOFF + (3 * pair + 1) * 1024 + kh * 512);
  };
  auto store_o = [&](u32x4 f0, u32x4 f1, unsigned base) {
    int ln = lane; PIN(ln);
    st128<SC1>(f0, rWS, ln * 16, base + TOFF + (3 * pair + 2 * odd) * 1024);
    st128<SC1>(f1, rWS, ln * 16 + (ln >> 5) * OOB, base + TOFF + (3 * pair + 1) * 1024 + odd * 512);
    __builtin_amdgcn_raw_buffer_store_b64(lo2(f1), rWS, ln * 8 + (1 - (ln >> 5)) * OOB, base + TOFF + 24576 - 256 + h * 256, SC1);
  };
  const int ht = tid & 255, sub = lane & 3;
  const int col = h * 56 - odd * 8;
  int krow[7];
  auto mkrow = [&](int i) {
    const int s = 4 * i + w;
    const int p = (s * 43) >> 7, m = s - 3 * p;
    const int c = i < 6 ? 7 * p + ((0x520 >> (4 * m)) & 15) + hh * (1 + (m & 1)) : 7 * (2 * w + hh) + 3;
    krow[i] = __umul24(8 * c + ((lane >> 2) & 7), 2688) + sub * 16;
  };
  auto ldw = [&](rsrc_t rs, int i, int stride, int off) { const u32x4 v = __builtin_amdgcn_raw_buffer_load_b128(rs, krow[i], col + off, 0); SB(); return v; };
  auto write_slab = [&](const u32x4* p, unsigned off) {
    lptr d = lds + off + ht * 16; PIN(d);
#pragma unroll
    for (int i = 0; i < 7; ++i) *(LAS u32x4*)(d + i * 4096) = p[i];
  };
  auto patch = [&](u32x4* p, int n, bool ones) __attribute__((always_inline)) {
    unsigned k01 = (odd && sub == 0) ? 0u : ~0u, k23 = (!odd && sub == 3) ? 0u : ~0u; PIN(k01); PIN(k23);
    const unsigned o0 = ones ? ~k01 & 0x3f80u : 0u, o2 = ones ? ~k23 & 0x3f80u : 0u;
#pragma unroll
    for (int i = 0; i < n; ++i) { p[i][0] = (p[i][0] & k01) | o0; p[i][1] &= k01; p[i][2] = (p[i][2] & k23) | o2; p[i][3] &= k23; }
  };
  auto put_slab = [&](u32x4* p, int type, int part) {
    patch(p, 7, type == 0 && part == 0);
    write_slab(p, part * SLAB);
  };
  auto read_frags = [&](u32x4* p, int part, lptr trb) {
    lptr wb = trb + part * SLAB + w * 1024; PIN(wb);
#pragma unroll
    for (int i = 0; i < 7; ++i) p[i] = trfrag(wb + i * 4096);
  };
  auto sum_frags = [&](const u32x4* p, int type, int part) {
    f32x16 a = Z16;
#pragma unroll
    for (int i = 0; i < 7; ++i) a = mfma((u32x4){ONES, ONES, ONES, ONES}, p[i], a);
    unsigned hi, lo; split2(a[0], hi, lo);
    if (hh == (w >> 1)) *(LAS u32x2*)(lds + L_CS + type * 3072 + part * 1024 + lane * 16 + (w & 1) * 8) = (u32x2){hi | (lo << 16), hi};
  };
  const rsrc_t rWA = __builtin_amdgcn_make_buffer_rsrc((void*)wqkvA, 0, DM * 3 * DM * 2, 0x00020000);
  auto dma_x0 = [&](int i0, int i1) __attribute__((always_inline)) {
    const rsrc_t rX0 = __builtin_amdgcn_make_buffer_rsrc((void*)x0, 0, SEQ * DM * 2, 0x00020000);
    const int o0 = (lane >> 3) * 896 + (((lane & 7) ^ (lane >> 4)) * 16), o1 = o0 ^ 64;
#pragma unroll
    for (int i = i0; i < i1; ++i) {
      __builtin_amdgcn_raw_ptr_buffer_load_lds(rX0, (LAS void*)(lds + L_X0 + w * SLAB + i * 1024), 16, (i & 1) ? o1 : o0, (T * 32 + (i & 3) * 8) * 896 + (i >> 2) * 128, 0, 0);
      SB();
    }
  };

  if (wave < 4) {
    RSTAMP(62); STAMP(0);
    u32x4 xf[28];
    f32x16 xs, ak, av;
    float rstd;
    u32x4 mf;
    auto stats = [&](float s1, float s2) {
      const float mean = s1 * (1.0f / DM);
      rstd = __builtin_amdgcn_rsqf(fmaxf(s2 * (1.0f / DM) - mean * mean, 0.f) + 1e-5f);
      unsigned hi, lo; split2(-mean, hi, lo);
      mf = (u32x4){hi | (hi << 16), lo, hi | (hi << 16), lo};
      PIN(rstd); PIN(mf);
    };
    u32x4 id0, id1;
    float wtail;
    unsigned lomask;
    {
      __builtin_amdgcn_s_setprio(3);
      id0 = ident_frag(0, r, hh); id1 = ident_frag(1, r, hh);
      wtail = hh ? 0.f : 1.f; lomask = hh ? 0u : ~0u;
      PIN(id0); PIN(id1); PIN(wtail); PIN(lomask);
      if (w >= 2) {
        u32x4 mk;
#pragma unroll
        for (int i = 0; i < 4; ++i) { const int k0 = ((2 * i) & 3) + 8 * (i >> 1) + 4 * hh + 16 * (w & 1); mk[i] = (k0 > r ? 0xffffu : 0u) | (k0 + 1 > r ? 0xffff0000u : 0u); }
        *(LAS u32x4*)(lds + L_MK + (w & 1) * 1024 + lane * 16) = mk;
      }
      if (w == 0) { cnt[0] = 0u; cnt[1] = 0u; cnt[2] = 0u; cnt[3] = 0u; cnt[4] = 0u; cnt[5] = 0u; cnt[6] = 0u; }
      cnt[8 + w] = 0u; cnt[12 + w] = 0u;
      const int lam = 4 * (w >> 1) + 2 * hh + (w & 1);
      const int pc = ((odd ? 0x01327654 : 0x76543210) >> (4 * lam)) & 7;
      unsigned off = lam == 7 ? OOB : ((h * 28 + 4 * pc - 4 * odd) * 28 + (r < 28 ? r : r - 8)) * 2;
      const rsrc_t rPH = __builtin_amdgcn_make_buffer_rsrc((void*)wphi, 0, 16 * 28 * 28 * 2, 0x00020000);
#pragma unroll
      for (int j = 0; j < 4; ++j) xf[27][j] = __builtin_amdgcn_raw_buffer_load_b16(rPH, off + j * 56, 0, 0);
      load_epoch();
      xf[26] = cat(ev, ev);
      __builtin_amdgcn_s_setprio(0);
      STAMPX(54);
    }
    tag = (u32x2){0u, 0u}; epoch = 0;
    asm volatile("" : "=v"(ak), "=v"(av), "=v"(rstd), "=v"(mf), "=v"(xs));
#pragma unroll
    for (int i = 0; i < 26; ++i) asm volatile("" : "=v"(xf[i]));
    unsigned sent = SENT;
    auto release = [&](int hop) { if (w == 0) poll(hop, 15); bar(); };
    for (int layer = 0; layer < NL; ++layer) {
      const int type = layer & 1;
      const int pb = 2 + layer * 14;
      if (layer > 0) {
        unsigned base = WS_XO + XBUF(layer - 1) * XO_B + TOFF;
        int v16 = lane * 16, vs = v16 + (v16 & 512);
        lptr bk = lds + L_WK + v16;
        PIN(vs);
        release(3 * layer - 1);
        STAMP(pb + 0);
        for (;;) {
          PIN(v16); PIN(vs); PIN(bk); asm volatile("" : "+s"(base));
          u32x4 sp[8];
          auto ld = [&](int s) { xf[4 + s] = __builtin_amdgcn_raw_buffer_load_b128(rWS, v16 + (s & 3) * 1024, base + (s >> 2) * 4096, SC1); SB(); };
          unsigned m = 1;
          ak = Z16; av = Z16;
          float s1 = 0.f, s2 = 0.f, mean;
          unsigned hi;
          u32x4 ck, cv;
          gemm2(ak, av, bk, xf,
                    [&](auto gap) {
#pragma unroll
                      for (int u = 0; u < 8; ++u) { sp[u] = __builtin_amdgcn_raw_buffer_load_b128(rWS, vs + ((u >> 1) & 1) * 2048 + (u & 1) * 512, base + 24576 + (u >> 2) * 4096, SC1); SB(); gap(u); if (u < 4) PACE2(); else PACED(); }
#pragma unroll
                      for (int s = 0; s < LA - 8; ++s) { ld(s); PACED(); }
                    },
                    [&](int i) {
                      if (i + LA - 8 < 24) ld(i + LA - 8);
                      if (i < 4) { PIN(sp[2 * i]); PIN(sp[2 * i + 1]); xf[i] = cat(lo2(sp[2 * i]), lo2(sp[2 * i + 1])); }
                      if (i == 22) { av = mfma(cv, mf, av); SB(); }
                      if (i == 24) { ak = mfma(ck, mf, ak); SB(); }
                    },
                    [&](int i, int hf) {
                      if (i < 4) m = max4(m, sp[2 * i + hf]); else m = umax3(m, xf[i][2 * hf], xf[i][2 * hf + 1]);
                      PIN(m);
                      if (i < 8) { if (hf) { s2 += U2F(sp[i][3]); PIN(s2); } else { s1 += U2F(sp[i][2]); PIN(s1); } }
                      if (hf) return;
                      if (i == 13) asm volatile("ds_write_b32 %0, %1" :: "v"(lds + L_SYNC + 32 + 4 * w), "v"(layer) : "memory");
                      if (i == 9) { auto t = __builtin_amdgcn_permlane32_swap(F2U(s1), F2U(s2), false, false); s1 = U2F(t[0]) + U2F(t[1]); PIN(s1); }
                      if (i == 10) { auto t = __builtin_amdgcn_permlane32_swap(F2U(s1), F2U(s1), false, false); s1 = U2F(t[0]); s2 = U2F(t[1]); PIN(s1); PIN(s2); }
                      if (i == 14) { mean = s1 * (-1.0f / DM); s2 = s2 * (1.0f / DM); PIN(mean); PIN(s2); }
                      if (i == 15) { s2 = fmaxf(fmaf(-mean, mean, s2), 0.f); PIN(s2); }
                      if (i == 16) { rstd = __builtin_amdgcn_rsqf(s2 + 1e-5f); PIN(rstd); }
                      if (i == 17) { hi = pk2(mean, mean); s1 = mean - bfhi(hi); PIN(hi); PIN(s1); }
                      if (i == 18) { const unsigned lo = pk2(s1, 0.f); mf = (u32x4){hi, lo, hi, lo}; PIN(mf); }
                      if (i == 19) { const lptr cs = lds + L_CS + type * 3072 + v16; ck = ldl(cs); cv = ldl(cs + 1024); }
                    }, EARLYAB);
          if (__builtin_expect(!__any(m == sent), 1)) break;
          if (--budget == 0) sent = 0u;
          RET(layer * 3);
          __builtin_amdgcn_s_sleep(BACK);
          asm volatile("" ::: "memory");
        }
      }
      int first = layer; asm volatile("" : "+s"(first));
      if (first == 0) {
        int ln = lane; PIN(ln);
        const int hh = ln >> 5;
        const unsigned ra = (ln & 31) * 128 + ((ln >> 1) & 7) * 16;
        const lptr stg = lds + L_X0 + w * SLAB;
        auto kof = [](int c) { return (c >> 3) * 4096 + (c & 7) * 16; };
        lptr ad[28];
#pragma unroll
        for (int i = 0; i < 28; ++i) {
          const int k0 = kof(chunk_of(ord(i), 0)), k1 = kof(chunk_of(ord(i), 1));
          ad[i] = stg + (ra ^ k0 ^ (~lomask & (k0 ^ k1))); PIN(ad[i]);
        }
        lptr pab = lds + L_X0 + tx * SLAB + (ra ^ kof(7 * pair + 5 * kh + hh)), pc = lds + L_X0 + tx * SLAB + (ra ^ kof(7 * pair + (hh ? 3 : 2 + 2 * kh))); PIN(pab); PIN(pc);
        lptr trb = make_trb(); PIN(trb);
        SB();
        *(LAS u32x2*)(lds + L_WPH + (w >> 1) * 1024 + ln * 16 + (w & 1) * 8) = (u32x2){xf[27][0] | (xf[27][1] << 16), xf[27][2] | (xf[27][3] << 16)};
        ev = lo2(xf[26]);
        make_tag();
        STAMPX(53);
        bar();
        STAMPX(55);
#pragma unroll
        for (int i = 0; i < 28; ++i) xf[i] = ldl(ad[i]);
        u32x4 xa = ldl(pab), xc = ldl(pc);
        SB();
        float s2a = 0.f, s2b = 0.f;
#pragma unroll
        for (int i = 0; i < 28; ++i) {
          const u32x4 u = xf[i]; s2a = dot2(u[1], u[1], dot2(u[0], u[0], s2a)); s2b = dot2(u[3], u[3], dot2(u[2], u[2], s2b));
        }
        PIN(s2a); PIN(s2b);
        STAMPX(56);
        bar();
        STAMP(1);
        u32x4 cc = ldl(lds + L_CS + ln * 16);
        ak = Z16;
        lptr bk = trb + L_WK; PIN(bk);
        gemm1<5, true>(ak, bk, xf, [](int) {}, [&](int i) {
          if (i == 4) {
            const unsigned sel = hh && kh ? 0u : ~0u;
            xc[0] = (xc[0] & sel) | (xc[2] & ~sel); xc[1] = (xc[1] & sel) | (xc[3] & ~sel); PIN(xc);
          }
          if (i >= 16 && i < 24) { const unsigned u = i < 20 ? xa[i & 3] : xc[i & 3]; xs[2 * (i - 16)] = bflo(u); xs[2 * (i - 16) + 1] = bfhi(u); PIN(xs); }
        }, NOAB);
        asm volatile("" :: "v"(ak));
        STAMPX(57);
        bar();
        STAMP(2);
        const u32x4 cv = ldl(lds + L_CS + 1024 + ln * 16);
        av = Z16;
        gemm1<5, true>(av, bk + SLAB, xf, [&](int i) { if (i == 8) { ak = mfma(cc, mf, ak); SB(); } if (i == 10) { av = mfma(cv, mf, av); SB(); } }, [&](int i) {
          if (i == 2) {
            float s1 = U2F(F2U(ak[12]) & ~lomask), s2 = s2a + s2b;
            red2<false>(s1, s2);
            stats(s1, s2);
          }
        }, NOAB);
      }
      asm volatile("" ::: "memory");
      arrive(0);
      STAMPX(pb + 1);
      const unsigned mid = WS_MID + (h * 4 + MBUF(layer)) * MID_B;
      int ln16 = lane * 16; PIN(ln16);
      u32x4 wph[2], kf0, kf1;
      if (type == 0) {
        wph[0] = ldl(lds + L_WPH + ln16); wph[1] = ldl(lds + L_WPH + 1024 + ln16);
        DUMP(0, ak, 1, rstd); DUMP(0, av, 2, rstd);
        kf0 = pack8<0>(ak); kf1 = pack8<1>(ak);
        f32x16 p = mfma(wph[0], kf0, Z16);
        p = mfma(wph[1], kf1, p);
        SB();
#pragma unroll
        for (int i = 0; i < 16; ++i) { float t = av[i] * rstd; PIN(t); av[i] = t; }
        u32x4 vb0 = pack8<0>(av), vb1 = pack8<1>(av);
        vb1[2] |= ~lomask & 0x3f80u;
        PIN(vb0); PIN(vb1);
        lptr ta0, ta1, tb0, tb1;
        {
          const int q = (ln16 >> 6) & 3, rp = ((ln16 >> 6) & 4) + ((ln16 >> 4) & 3), lam = (rp & 4) | ((rp & 1) << 1) | ((rp >> 1) & 1);
          const int o0 = (8 * (ln16 >> 9) + q) * 64 + (((lam >> 1) ^ (q >> 1)) * 16) + (lam & 1) * 8, o1 = (o0 ^ 32) + 256;
          tb0 = lds + L_SCR + o0; tb1 = lds + L_SCR + o1; ta0 = tb0 + (w >> 1) * 8192; ta1 = tb1 + (w >> 1) * 8192;
          PIN(ta0); PIN(ta1); PIN(tb0); PIN(tb1);
        }
        const int rr = (ln16 >> 4) & 31, pc = (ln16 >> 9) ^ ((rr >> 1) & 3);
        lptr d0 = lds + L_SCR + w * 2048 + rr * 64 + pc * 16, d1 = lds + L_SCR + w * 2048 + rr * 64 + (pc ^ 2) * 16;
        lptr ma = lds + L_SYNC + ((ln16 & 64) ? 12 : 48 + ((ln16 >> 2) & 12)), mw = lds + L_SYNC + 48 + 4 * w;
        unsigned mv = (ln16 & 64) ? 4u : layer + 1u, ml = layer + 1u, got = *cnt;
        PIN(d0); PIN(d1); PIN(ma); PIN(mw); PIN(mv); PIN(ml);
        SB();
        u32x4 fp[2], fm[2];
        dual_softmax(p, rstd * L2E, wtail, fp, fm, [&] {
          if (__builtin_amdgcn_readfirstlane(got) < 8u * layer + 4u) gather_all(layer, false);
          asm volatile("" ::: "memory");
          *(LAS u32x4*)(d0 + 16384) = vb0; *(LAS u32x4*)(d1 + 16384) = vb1;
          SB();
        });
        fp[1][2] &= lomask; fp[1][3] &= lomask; fm[1][2] &= lomask; fm[1][3] &= lomask;
        *(LAS u32x4*)(d0) = fp[0]; *(LAS u32x4*)(d1) = fp[1];
        *(LAS u32x4*)(d0 + 8192) = fm[0]; *(LAS u32x4*)(d1 + 8192) = fm[1];
        asm volatile("ds_write_b32 %0, %1" :: "v"(mw), "v"(ml) : "memory");
        while (!__all(*(volatile LAS unsigned*)ma >= mv)) { }
        asm volatile("" ::: "memory");
        {
          auto tr2 = [](lptr p0, lptr p1) {
            bf16x4 a0 = __builtin_amdgcn_ds_read_tr16_b64_v4bf16((LAS bf16x4*)p0), a1 = __builtin_amdgcn_ds_read_tr16_b64_v4bf16((LAS bf16x4*)p1);
            return __builtin_bit_cast(u32x4, __builtin_shufflevector(a0, a1, 0, 1, 2, 3, 4, 5, 6, 7));
          };
          u32x4 fa[4], fb[4];
          f32x16 kv = Z16;
#pragma unroll
          for (int i = 0; i < 3; ++i) { fa[i] = tr2(ta0 + i * 1024, ta1 + i * 1024); fb[i] = tr2(tb0 + 16384 + i * 1024, tb1 + 16384 + i * 1024); SB(); }
#pragma unroll
          for (int i = 0; i < 8; ++i) {
            kv = mfma(fa[i & 3], fb[i & 3], kv);
            SB();
            if (i + 3 < 8) { fa[(i + 3) & 3] = tr2(ta0 + (i + 3) * 1024, ta1 + (i + 3) * 1024); fb[(i + 3) & 3] = tr2(tb0 + 16384 + (i + 3) * 1024, tb1 + 16384 + (i + 3) * 1024); }
            SB();
          }
          st128<SC1>((w & 1) ? pack8<1>(kv) : pack8<0>(kv), rWS, ln16, mid + (tb * 4 + w) * 1024);
          CALS(3 * layer);
        }
        while (__builtin_amdgcn_readfirstlane(cnt[6]) < 4u) __builtin_amdgcn_s_sleep(2);
        asm volatile("" ::: "memory");
      } else {
        av *= rstd;
        DUMP(0, av, 2, 1.f);
        u32x4 vb1 = pack8<1>(av);
        vb1[2] |= ~lomask & 0x3f80u;
        f32x16 vt = mfma(pack8<0>(av), id0, Z16);
        vt = mfma(vb1, id1, vt);
        SB();
#pragma unroll
        for (int i = 0; i < 16; ++i) { float t = ak[i] * rstd; PIN(t); ak[i] = t; }
        DUMP(0, ak, 1, 1.f);
        kf0 = pack8<0>(ak); kf1 = pack8<1>(ak);
        const unsigned g = mid + T * 4096;
        const lptr d = lds + L_SCR + (8 + w) * 4096 + ln16;
        st128<SC1>(kf0, rWS, ln16, g);
        const u32x4 vf0 = pack8<0>(vt), vf1 = pack8<1>(vt);
        st128<SC1>(vf0, rWS, ln16 + 2048, g);
        st128<SC1>(vf1, rWS, ln16 + 3072, g);
        {
          u32x4 kc = kf1;
          kc[3] |= ~lomask & (tag[0] + layer - xhalf_addu(add4(add4(0u, kf0), kf1) + add4(add4(0u, vf0), vf1)));
          st128<SC1>(kc, rWS, ln16 + 1024, g);
          CALS(3 * layer);
        }
        gather_all(layer, false);
        *(LAS u32x4*)(d) = kf0; *(LAS u32x4*)(d + 1024) = kf1; *(LAS u32x4*)(d + 2048) = vf0; *(LAS u32x4*)(d + 3072) = vf1;
      }
      STAMPX(pb + 2);
      u32x4 qf0, qf1;
      {
        f32x16 aq = mfma(ldl(lds + L_CS + type * 3072 + 2048 + ln16), mf, Z16);
        lptr bq = lds + L_WQ + ln16; PIN(bq);
        gemm1<5, false>(aq, bq, xf, [](int) {}, [](int) {}, NOAB);
        aq *= type ? rstd * (0.18898223650461361f * L2E) : 1.0f;
        DUMP(0, aq, 0, type ? 1.f : rstd);
        qf0 = pack8<0>(aq); qf1 = pack8<1>(aq);
        qf1[2] |= ~lomask & 0x3f80u;
      }
      f32x16 o;
      if (type == 0) {
        u32x4 fp[2], fm[2];
        {
          f32x16 p = mfma(wph[0], qf0, Z16);
          p = mfma(wph[1], qf1, p);
          dual_softmax(p, rstd * L2E, wtail, fp, fm, [] {});
        }
        PIN(fp[0]); PIN(fp[1]); PIN(fm[0]); PIN(fm[1]);
        STAMPX(pb + 3);
        bar();
        STAMP(pb + 4);
        const lptr kvf = lds + L_KVF + ln16;
        const u32x4 a0 = ldl(kvf), a1 = ldl(kvf + 1024), a2 = ldl(kvf + 2048), a3 = ldl(kvf + 3072);
        SB();
        o = mfma(a0, fp[0], Z16);
        o = mfma(a1, fp[1], o);
        o = mfma(a2, fm[0], o);
        o = mfma(a3, fm[1], o);
        o *= __builtin_amdgcn_rcpf(upper_val(o[12]) + 1e-6f);
      } else {
        lptr kv = lds + L_SCR + w * 4096 + ln16; PIN(kv);
        auto ex4 = [&](f32x16& s, int i0) {
#pragma unroll
          for (int i = i0; i < i0 + 4; ++i) s[i] = __builtin_amdgcn_exp2f(s[i]);
          unsigned a = pk2(s[i0], s[i0 + 1]), c = pk2(s[i0 + 2], s[i0 + 3]); PIN(a); PIN(c);
          SB();
          return (u32x2){a, c};
        };
        auto tile_max = [&](int j, int side, float m) {
          f32x16 t = mfma(ldl(kv + j * 4096 + 1024), qf1, mfma(ldl(kv + j * 4096), qf0, Z16));
          int lim = ((ln16 >> 4) & 31) - ((ln16 >> 7) & 4); PIN(lim);
#pragma unroll
          for (int i = 0; i < 16; ++i) { const bool up = (i & 3) + 8 * (i >> 2) > lim; m = fmaxf(m, (side == 0 || up == (side == 1)) ? t[i] : m); }
          return m;
        };
        float m = -__builtin_inff(), l;
        for (int pass = 0;; ++pass) {
          const u32x4 mk0 = ldl(lds + L_MK + ln16), mk1 = ldl(lds + L_MK + 1024 + ln16);
          f32x16 negm;
          {
            const u32x4 v0 = ldl(kv + 8 * 4096 + 2048), v1 = ldl(kv + 8 * 4096 + 3072);
            f32x16 s;
#pragma unroll
            for (int i = 0; i < 16; ++i) { unsigned k = (i < 8 ? mk0 : mk1)[(i >> 1) & 3]; if (!(i & 1)) { k <<= 16; PIN(k); } s[i] = U2F(k & 0xff800000u); }
            s = mfma(ldl(kv + 8 * 4096 + 1024), qf1, mfma(ldl(kv + 8 * 4096), qf0, s));
#pragma unroll
            for (int i = 0; i < 16; i += 2) m = fmax3(m, s[i], s[i + 1]);
            m = xhalf_max(m);
            s -= m;
            negm = splat(-m);
            PIN(negm);
            const u32x2 a = ex4(s, 0), c = ex4(s, 4), e = ex4(s, 8), f = ex4(s, 12);
            o = mfma(v1, cat(e, f), mfma(v0, cat(a, c), Z16));
            PIN(o);
          }
          if (pass == 0) {
            STAMPX(pb + 3);
            bar();
            STAMP(pb + 4);
          }
          u32x4 kk[2][2], vv[2][2], pa, pb2;
          f32x16 st[2];
          kk[0][0] = ldl(kv); kk[0][1] = ldl(kv + 1024); kk[1][0] = ldl(kv + 4096); kk[1][1] = ldl(kv + 4096 + 1024);
          SB();
          st[0] = mfma(kk[0][1], qf1, mfma(kk[0][0], qf0, negm));
          SB();
#pragma unroll
          for (int j = 0; j < 8; ++j) {
            f32x16& s = st[j & 1];
            vv[j & 1][0] = ldl(kv + j * 4096 + 2048); vv[j & 1][1] = ldl(kv + j * 4096 + 3072);
            if (j < 6) { kk[j & 1][0] = ldl(kv + (j + 2) * 4096); kk[j & 1][1] = ldl(kv + (j + 2) * 4096 + 1024); }
            SB();
            if (j == 0) { st[1] = mfma(kk[1][0], qf0, negm); SB(); }
            const u32x2 a = ex4(s, 0);
            if (j == 0) st[1] = mfma(kk[1][1], qf1, st[1]); else o = mfma(vv[(j - 1) & 1][0], pa, o);
            SB();
            const u32x2 c = ex4(s, 4);
            if (j == 7) o = mfma(vv[0][1], pb2, o); else if (j > 0) st[(j + 1) & 1] = mfma(kk[(j + 1) & 1][0], qf0, negm);
            SB();
            const u32x2 e = ex4(s, 8);
            if (j == 7) o = mfma(vv[1][0], cat(a, c), o); else if (j > 0) st[(j + 1) & 1] = mfma(kk[(j + 1) & 1][1], qf1, st[(j + 1) & 1]);
            SB();
            const u32x2 f = ex4(s, 12);
            if (j == 7) o = mfma(vv[1][1], cat(e, f), o); else if (j > 0) o = mfma(vv[(j - 1) & 1][1], pb2, o);
            SB();
            pa = cat(a, c); pb2 = cat(e, f);
            if (j == 0) { pa &= mk0; pb2 &= mk1; PIN(pa); PIN(pb2); }
          }
          l = upper_val(o[12]);
          if (pass > 0 || __all(l < LMAX)) break;
          RET(13);
          m = tile_max(0, 1, m);
#pragma unroll 1
          for (int j = 1; j < 8; ++j) m = tile_max(j, 0, m);
        }
        arrive(4);
        o *= __builtin_amdgcn_rcpf(l);
      }
      DUMP(1, o, 0, 1.f);
      store_o(pack8<0>(o), pack8<1>(o), WS_XO + OBUF(layer) * XO_B);
      CALS(3 * layer + 1);
      STAMPX(pb + 6);
      lptr bown = lds + L_WO + kh * (SLAB + 1024) + ln16, both = lds + L_WO + kh * (SLAB - 1024) + 1024 + ln16;
      PIN(bown); PIN(both);
      unsigned base = WS_XO + OBUF(layer) * XO_B + XOFF + kh * 12288;
      int v16 = ln16, vs = (v16 >> 1) + ((v16 >> 1) & 256);
      PIN(vs);
      release(3 * layer + 1);
      STAMP(pb + 7);
      f32x16 xn, xo;
      u32x4 wa[4];
      auto tr = [](lptr p) { return ldl(p); };
      for (;;) {
        PIN(v16); PIN(vs); asm volatile("" : "+s"(base));
        u32x2 sp[4];
        u32x4 wb[4];
#pragma unroll
        for (int u = 0; u < 4; ++u) { sp[u] = __builtin_amdgcn_raw_buffer_load_b64(rWS, vs + (u >> 1) * 1024 + (u & 1) * 256, base + 24576 - kh * 10240, SC1); SB(); if (u & 1) PACED(); }
#pragma unroll
        for (int s = 0; s < LAO; ++s) { xf[2 + s] = __builtin_amdgcn_raw_buffer_load_b128(rWS, v16 + (s & 3) * 1024, base + (s >> 2) * 4096, SC1); SB(); if (s < 3) wb[s] = tr(both + s * 2048); else if (s < 6) wa[s - 3] = tr(bown + (s - 3) * 2048); SB(); if (s < 6) PACE2(); else PACED(); }
        xf[0] = cat(sp[0], sp[1]); xf[1] = cat(sp[2], sp[3]);
        xn = xs; xo = Z16;
        unsigned m = 1;
#pragma unroll
        for (int i = 0; i < 14; ++i) {
          if (i + LAO < 12) { const int s = i + LAO; xf[2 + s] = __builtin_amdgcn_raw_buffer_load_b128(rWS, v16 + (s & 3) * 1024, base + (s >> 2) * 4096, SC1); SB(); }
          if (i + 3 < 14) wb[(i + 3) & 3] = tr(both + (i + 3) * 2048);
          SB();
          xo = mfma(wb[i & 3], xf[i], xo);
          SB();
          m = max4(m, xf[i]); PIN(m);
          if (i >= LAG) {
            wa[(i - LAG + 3) & 3] = tr(bown + (i - LAG + 3) * 2048);
            SB();
            xn = mfma(wa[(i - LAG) & 3], xf[i - LAG], xn);
          }
          SB();
        }
        if (__builtin_expect(!__any(m == sent), 1)) break;
        if (--budget == 0) sent = 0u;
        RET(layer * 3 + 2);
        __builtin_amdgcn_s_sleep(BACK);
        asm volatile("" ::: "memory");
      }
      {
        const lptr ex = lds + L_WQ + ln16;
        f32x4 t[4];
#pragma unroll
        for (int i = 14 - LAG; i < 14; ++i) {
          if (i + 3 < 14) wa[(i + 3) & 3] = tr(bown + (i + 3) * 2048);
          SB();
          xn = mfma(wa[i & 3], xf[i], xn);
          SB();
          if (i == 15 - LAG) {
#pragma unroll
            for (int g = 0; g < 4; ++g) *(LAS f32x4*)(ex + w * 4096 + g * 1024) = (f32x4){xo[4 * g], xo[4 * g + 1], xo[4 * g + 2], xo[4 * g + 3]};
          }
          if (i == 11) {
            bar();
#pragma unroll
            for (int g = 0; g < 4; ++g) t[g] = *(LAS f32x4*)(ex + (w ^ 1) * 4096 + g * 1024);
          }
          SB();
        }
#pragma unroll
        for (int g = 0; g < 4; ++g) {
#pragma unroll
          for (int j = 0; j < 4; ++j) xs[4 * g + j] = xn[4 * g + j] + t[g][j];
        }
      }
      STAMPX(pb + 8);
      if (layer == NL - 1) {
        const rsrc_t rOUT = __builtin_amdgcn_make_buffer_rsrc((void*)out, 0, SEQ * DM * 2, 0x00020000);
        const u32x4 f0 = pack8<0>(xs), f1 = pack8<1>(xs);
        int ln = lane; PIN(ln);
        const int row = (ln & 31) * 896, up = ln >> 5;
        const int ob = (tb * 4 + tx) * 32 * 896 + 7 * pair * 16;
        st128<0>(f0, rOUT, row + up * 16, ob + 5 * kh * 16);
        st128<0>(f1, rOUT, row + up * OOB, ob + (2 + 2 * kh) * 16);
        __builtin_amdgcn_raw_buffer_store_b64(lo2(f1), rOUT, row + (1 - up) * OOB, ob + 3 * 16 + kh * 8, 0);
      } else {
        const u32x4 f0 = pack8<0>(xs);
        store_x0(f0, WS_XO + XBUF(layer) * XO_B);
        u32x4 f1 = pack8<1>(xs);
        f1[2] &= lomask; f1[3] &= lomask;
        float s1 = 0.f, s2 = 0.f, t1 = 0.f, t2 = 0.f;
#pragma unroll
        for (int j = 0; j < 4; ++j) { const unsigned u = f0[j], v = f1[j]; s1 = dot2(u, ONES, s1); s2 = dot2(u, u, s2); t1 = dot2(v, ONES, t1); t2 = dot2(v, v, t2); }
        s1 += t1; s2 += t2;
        red2<false>(s1, s2);
        f1[2] |= ~lomask & F2U(s1); f1[3] |= ~lomask & F2U(s2);
        store_x1(f1, WS_XO + XBUF(layer) * XO_B);
        CALS(3 * layer + 2);
        STAMPX(pb + 9);
      }
      asm volatile("" : "=v"(ak), "=v"(av), "=v"(rstd), "=v"(mf));
#pragma unroll
      for (int i = 14; i < 28; ++i) asm volatile("" : "=v"(xf[i]));
    }
    STAMP(61); RSTAMP(63);
    TOT1
    if (b == 0 && w == 0) __builtin_amdgcn_raw_buffer_store_b64((u32x2){(unsigned)(epoch + 1), (unsigned)((epoch + 1) >> 32)}, rWS, WS_EPOCH, 0, SC1);
  } else {
    STAMPX(53);
    u32x4 pA[21], pB[21];
    unsigned tch0, tch1;
    asm volatile("s_load_dword %0, %2, 0x0\n\ts_load_dword %1, %3, 0x0" : "=&s"(tch0), "=&s"(tch1)
                 : "s"(w == 0 ? (const void*)ws : w == 1 ? (const void*)wqkvB : w == 2 ? (const void*)woA : (const void*)wphi), "s"(w < 2 ? (const void*)woB : (const void*)out));
    dma_x0(0, 4);
    unsigned warm0;
    {
      u64 pc; asm volatile("s_getpc_b64 %0" : "=s"(pc));
      const rsrc_t rs = __builtin_amdgcn_make_buffer_rsrc(w == 3 ? (void*)(pc & ~127ull) : (void*)wqkvA, 0, w == 3 ? CODE_B : DM * 3 * DM * 2, 0x00020000);
      const int n = w == 3 ? 9 : 13;
      warm0 = __builtin_amdgcn_raw_buffer_load_b32(rs, lane < n ? (lane + (w == 3 ? (b >> 3) : b * 3 + w) * n) * 128 : OOB, 0, 0);
      SB();
    }
#pragma unroll
    for (int i = 0; i < 7; ++i) { mkrow(i); PIN(krow[i]); }
    lptr trb = make_trb(); PIN(trb);
    u32x4 id0 = ident_frag(0, r, hh); PIN(id0);
    SB();
    dma_x0(4, 28);
#pragma unroll
    for (int i = 0; i < NV0; ++i) pA[7 + i] = ldw(rWA, i, 2688, 896);
    STAMPX(54);
    asm volatile("s_waitcnt vmcnt(%2) lgkmcnt(0)" :: "s"(tch0), "s"(tch1), "n"(NV0) : "memory");
    const u32x4 sv = {SENT, SENT, SENT, SENT};
    auto clean_kv = [&](int layer) { if (layer < NL) st128<SC1>(sv, rWS, lane * 16, WS_MID + (h * 4 + MBUF(layer)) * MID_B + (tb * 4 + w) * 1024); };
    auto clean_o = [&](int layer) { if (layer < NL) store_o(sv, sv, WS_XO + OBUF(layer) * XO_B); };
    auto clean_x = [&](int layer) { if (layer < NL - 1) { store_x0(sv, WS_XO + XBUF(layer) * XO_B); store_x1(sv, WS_XO + XBUF(layer) * XO_B); } };
    unsigned r0 = (unsigned)__builtin_amdgcn_s_memrealtime(), c0 = (unsigned)__builtin_amdgcn_s_memtime(), tann = 0;
    auto mark = [&](int cyc) {
      const unsigned r1 = (unsigned)__builtin_amdgcn_s_memrealtime(), c1 = (unsigned)__builtin_amdgcn_s_memtime();
      tann = r1 + LAND + (unsigned)((float)cyc * (float)(r1 - r0) * __builtin_amdgcn_rcpf((float)(c1 - c0)));
      r0 = r1; c0 = c1;
    };
    auto push = [&](int hop, unsigned voff) {
      CALA(hop);
      __builtin_amdgcn_raw_buffer_store_b64((u32x2){tag[0], (tag[1] & 0xfff00000u) | (tann & 0xfffffu)}, rWS, voff, WS_FLAGS + hop * HOP_B, SC1);
    };
    auto push_grp = [&](int hop) { push(hop, lane < 16 ? (tb * 16 + lane) * 128 + h * 8 : OOB); };
    auto push_mid = [&](int layer) {
      if (layer & 1) push(3 * layer, (lane < 2 && tb + 1 + lane < 16) ? ((tb + 1 + lane) * 16 + h) * 128 + lane * 8 : OOB);
      else push(3 * layer, lane < 16 ? (lane * 16 + h) * 128 + tb * 8 : OOB);
    };
    const rsrc_t rWB = __builtin_amdgcn_make_buffer_rsrc((void*)wqkvB, 0, DM * 3 * DM * 2, 0x00020000);
    auto spin = [&](int k, unsigned n) { while (__builtin_amdgcn_readfirstlane(cnt[k]) < n) __builtin_amdgcn_s_sleep(1); asm volatile("" ::: "memory"); };
    auto wait_h = [&](int hop, int mask) {
      if (w == 0) { if (mask >= 0) poll(hop, mask); cnt[1] = hop + 1; }
      else while (__builtin_amdgcn_readfirstlane(cnt[1]) != hop + 1) __builtin_amdgcn_s_sleep(1);
      asm volatile("" ::: "memory");
    };
    const unsigned pkb = WS_PK + h * 262144;
    auto ldp = [&](unsigned off) { const u32x4 v = __builtin_amdgcn_raw_buffer_load_b128(rWS, lane * 16, pkb + off + w * 1024, SC1); SB(); return v; };
    STAMPX(55);
    bar();
#pragma unroll
    for (int i = NV0; i < 7 + NV1; ++i) pA[7 + i] = ldw(rWA, i % 7, 2688, 896 + 896 * (i / 7));
    put_slab(pA + 7, 0, 0);
#pragma unroll
    for (int i = NV1; i < 7; ++i) pA[14 + i] = ldw(rWA, i, 2688, 1792);
    read_frags(pA + 7, 0, trb); sum_frags(pA + 7, 0, 0);
    __builtin_amdgcn_s_setprio(3);
    STAMPX(57);
    bar();
    load_epoch();
    const unsigned warm1 = __builtin_amdgcn_raw_buffer_load_b32(rWS, (FW && w == 0 && lane < 11) ? lane * HOP_B : OOB, WS_FLAGS + me * 128, SC1);
    SB();
    u32x4 pw[4];
    {
      const unsigned act = tb < 14 ? 0u : OOB;
#pragma unroll
      for (int j = 0; j < 2; ++j) {
        const int st = 2 * tb + j, p = (st * 43) >> 7, m = st - 3 * p;
        const int c = st < 24 ? 7 * p + ((0x520 >> (4 * m)) & 15) + hh * (1 + (m & 1)) : 7 * (2 * (st - 24) + hh) + 3;
        pw[j] = __builtin_amdgcn_raw_buffer_load_b128(rWB, ((8 * c + ((lane >> 2) & 7)) * 2688 + sub * 16) | (w < 3 ? act : OOB), col + (w == 2 ? 0 : 896 + 896 * w), 0);
        SB();
      }
      {
        const int n = 2 * tb + (w & 1), k2 = n >= 14, i = n - 14 * k2, st = i - 2, p = (st * 43) >> 7, m = st - 3 * p;
        const int c = 28 * k2 + (i < 2 ? 14 * i + 3 : 7 * p + ((0x520 >> (4 * m)) & 15)), dl = i < 2 ? 7 : 1 + (m & 1);
        const unsigned vo = (c * 7168 + (lane >> 3) * 896 + 112 * pair + (((lane & 7) ^ ((lane >> 2) & 4)) * 16)) | act;
        const rsrc_t rs = __builtin_amdgcn_make_buffer_rsrc(w >> 1 ? (void*)woB : (void*)woA, 0, DM * DM * 2, 0x00020000);
        pw[2] = __builtin_amdgcn_raw_buffer_load_b128(rs, vo, 0, 0); SB();
        pw[3] = __builtin_amdgcn_raw_buffer_load_b128(rs, vo + dl * 7168, 0, 0); SB();
      }
    }
    put_slab(pA + 14, 0, 1);
#pragma unroll
    for (int j = 0; j < NQ0; ++j) pA[j] = ldw(rWA, j, 2688, 0);
    read_frags(pA + 14, 1, trb); sum_frags(pA + 14, 0, 1);
    STAMPX(58);
    bar();
    if (w == 3) mark(TKA0);
    __builtin_amdgcn_s_setprio(0);
#pragma unroll
    for (int j = NQ0; j < 7; ++j) pA[j] = ldw(rWA, j, 2688, 0);
    {
      patch(pw, 2, false);
      const unsigned act = (tb < 14 ? 0u : OOB) | (lane * 16);
      const lptr sc = lds + L_WO + w * 4096;
#pragma unroll
      for (int j = 0; j < 4; ++j) *(LAS u32x4*)(sc + j * 1024 + lane * 16) = pw[j];
      const int q = (lane >> 2) & 3, rp = ((lane >> 2) & 4) + (lane & 3), lam = (rp & 4) | ((rp & 1) << 1) | ((rp >> 1) & 1);
      auto tr128 = [&](int pc8) {
        const lptr p = sc + 2048 + (8 * hh + q) * 128 + ((pc8 * 8) ^ ((q >> 1) * 64));
        bf16x4 a0 = __builtin_amdgcn_ds_read_tr16_b64_v4bf16((LAS bf16x4*)p), a1 = __builtin_amdgcn_ds_read_tr16_b64_v4bf16((LAS bf16x4*)(p + 512));
        return __builtin_bit_cast(u32x4, __builtin_shufflevector(a0, a1, 0, 1, 2, 3, 4, 5, 6, 7));
      };
      pw[0] = trfrag(trb + L_WO + w * 4096); pw[1] = trfrag(trb + L_WO + w * 4096 + 1024);
      pw[2] = tr128(lam); pw[3] = tr128(6 + ((0x01327654 >> (4 * lam)) & 7));
#pragma unroll
      for (int j = 0; j < 2; ++j) st128<SC1>(pw[j], rWS, w < 3 ? act : OOB, pkb + w * SLAB + (2 * tb + j) * 1024);
#pragma unroll
      for (int j = 0; j < 2; ++j) st128<SC1>(pw[2 + j], rWS, act, pkb + (3 + (w & 2)) * SLAB + (2 * tb + (w & 1)) * 2048 + j * 1024);
    }
    clean_kv(0); clean_o(0);
    make_tag();
    STAMPX(59);
    put_slab(pA, 0, 2);
    drain();
    arrive(3);
    if (w == 3) { spin(3, 4); push_mid(0); }
    clean_x(0);
    read_frags(pA, 2, trb); write_slab(pA, L_WQ); sum_frags(pA, 0, 2);
    arrive(6);
    asm volatile("" :: "v"(warm0), "v"(warm1));
    __builtin_amdgcn_s_setprio(3);
    u32x4 po[14];
    auto swap_wo = [&](bool keep) {
      lptr d = lds + L_WO + ht * 16; PIN(d);
#pragma unroll
      for (int i = 0; i < 14; ++i) { u32x4 t; if (keep) t = ldl(d + i * 4096); *(LAS u32x4*)(d + i * 4096) = po[i]; if (keep) po[i] = t; SB(); }
    };
    STAMPX(60);
#pragma unroll
    for (int layer = 0; layer < NL; ++layer) {
      const int type = layer & 1;
      const int pb = 2 + layer * 14;
      const unsigned mid = WS_MID + (h * 4 + MBUF(layer)) * MID_B;
      if (type == 0) {
        STAMPX(pb + 0);
        STAMPX(pb + 1);
        if (layer > 0) arrive(3);
        if (w == 0) {
          while (!__all(cnt[12 + (lane & 3)] == layer + 1u)) __builtin_amdgcn_s_sleep(1);
          if (layer > 0) spin(3, 8);
          poll(3 * layer, 15, FLOORK);
        }
        wait_h(3 * layer, -1);
        STAMPX(pb + 2);
        f32x16 sum;
        for (;;) {
          sum = Z16;
          unsigned m = 0;
          u32x4 pr[16];
          const unsigned g = mid + w * 1024;
#pragma unroll
          for (int i = 0; i < 16; ++i) {
            pr[i] = __builtin_amdgcn_raw_buffer_load_b128(rWS, lane * 16, g + i * 4096, SC1); SB();
            if (i < 15) PACED();
          }
          if (w == 3) { mark(layer ? TOA : TOA0); push_grp(3 * layer + 1); SB(); }
#pragma unroll
          for (int i = 0; i < 16; ++i) {
            if (layer == 0 && i < NPO) po[i] = ldp(3 * SLAB + i * 4096);
            sum = mfma(id0, pr[i], sum); m = max4(m, pr[i]); PIN(m); SB();
          }
          if (!__any(m == SENT) || budget == 0) break;
          --budget; RET(layer * 3 + 1);
          asm volatile("" ::: "memory");
        }
        *(LAS u32x4*)(lds + L_KVF + w * 1024 + lane * 16) = pack8<0>(sum);
        STAMPX(pb + 3);
        bar();
        if (layer == 0) {
#pragma unroll
          for (int i = NPO; i < 14; ++i) po[i] = ldp(3 * SLAB + i * 4096);
        } else __builtin_amdgcn_s_sleep(5);
      } else {
        gather_all(layer, true);
        STAMPX(pb + 0);
        u32x4 t[8];
        wait_h(3 * layer, tb >= 2 ? 1 : tb - 1);
        STAMPX(pb + 2);
        if (tb - 2 + (w >> 1) >= 0) {
          const unsigned g = mid + (tb * 4 - 8 + 2 * w) * 4096;
          for (;;) {
            unsigned c[2] = {0u, 0u};
#pragma unroll
            for (int i = 0; i < 8; ++i) { t[i] = __builtin_amdgcn_raw_buffer_load_b128(rWS, lane * 16 + (i & 3) * 1024, g + (i >> 2) * 4096, SC1); SB(); if (i < 7) PACED(); }
#pragma unroll
            for (int i = 0; i < 8; ++i) {
              c[i >> 2] = add4(c[i >> 2], t[i]); PIN(c[i >> 2]);
              if ((i & 3) == 1) t[i][3] &= (unsigned)hh - 1u;
              *(LAS u32x4*)(lds + L_SCR + 2 * w * 4096 + i * 1024 + lane * 16) = t[i];
              SB();
            }
            const auto cs = __builtin_amdgcn_permlane32_swap(c[0], c[1], false, false);
            if (__all(cs[0] + cs[1] == tag[0] + layer) || budget == 0) break;
            --budget; RET(layer * 3 + 1);
            __builtin_amdgcn_s_sleep(8);
            asm volatile("" ::: "memory");
          }
        } else {
#pragma unroll
          for (int i = 0; i < 8; ++i)
            *(LAS u32x4*)(lds + L_SCR + 2 * w * 4096 + i * 1024 + lane * 16) = (u32x4){0u, 0u, ((i & 3) == 1 && hh) ? 0xff80u : 0u, 0u};
        }
        drain();
        STAMPX(pb + 3);
        bar();
        if (w == 3) { mark(layer == 1 ? TOB1 : TOB); push_grp(3 * layer + 1); }
        __builtin_amdgcn_s_setprio(0);
        if (layer == 1) {
          clean_x(1); clean_kv(2);
#pragma unroll
          for (int i = 0; i < 14; ++i) po[i] = ldp(5 * SLAB + i * 4096);
          swap_wo(true);
          drain();
          spin(4, 4);
        }
      }
      __builtin_amdgcn_s_setprio(0);
      if (layer == 0) {
        swap_wo(false);
      } else if (layer < NL - 1) {
        write_slab((type == 0 ? pB : pA) + 7, L_WK);
      }
      STAMPX(pb + 4);
      bar();
      if (w == 3 && layer < NL - 1) { mark(layer == 2 ? TX2 : layer ? TX : TX0); push_grp(3 * layer + 2); }
      STAMPX(pb + 5);
      if (layer > 0 && layer < NL - 1) { __builtin_amdgcn_s_sleep(WSL); write_slab((type == 0 ? pB : pA) + 14, L_WV); }
      if (layer == 0) {
        __builtin_amdgcn_s_sleep(12);
#pragma unroll
        for (int i = 0; i < 21; ++i) pB[(i + 7) % 21] = ldp(i * 4096);
      }
      bar();
      if (layer < NL - 1) {
        __builtin_amdgcn_s_sleep(SWS);
        if (layer == 0) { write_slab(pB + 7, L_WK); write_slab(pB + 14, L_WV); sum_frags(pB + 7, 1, 0); sum_frags(pB + 14, 1, 1); }
        else swap_wo(layer == 1);
        bar();
        __builtin_amdgcn_s_sleep(WSL);
        write_slab(type == 0 ? pB : pA, L_WQ);
        if (layer == 0) sum_frags(pB, 1, 2);
        arrive(0);
        while (!__all(cnt[8 + (lane & 3)] == layer + 1u)) __builtin_amdgcn_s_sleep(1);
        if (w == 3) { mark(layer == 0 ? TKB1 : layer == 1 ? TKA : TKB); push_mid(layer + 1); }
        __builtin_amdgcn_s_sleep(CSL);
        clean_o(layer + 1);
        if (layer == 1) { clean_x(2); drain(); }
        __builtin_amdgcn_s_setprio(3);
      }
    }
  }
}
}

extern "C" void kernel_launch(void* const* d_in, const int* in_sizes, int n_in, void* d_out, int out_size, void* d_ws, size_t ws_size, hipStream_t stream) {
  (void)hipFuncSetAttribute((const void*)fused_kernel, hipFuncAttributeMaxDynamicSharedMemorySize, L_END);
  fused_kernel<<<256, 512, L_END, stream>>>((const u16*)d_in[0], (const u16*)d_in[1], (const u16*)d_in[2], (const u16*)d_in[3],
                                            (const u16*)d_in[5], (const u16*)d_in[6], (u16*)d_out, (unsigned char*)d_ws);
}
```

```cpp
#define NL 4
#include <hip/hip_runtime.h>
#include <hip/hip_bf16.h>

typedef unsigned short u16;
typedef unsigned long long u64;
typedef __bf16 bf16x8 __attribute__((ext_vector_type(8)));
typedef __bf16 bf16x4 __attribute__((ext_vector_type(4)));
typedef __bf16 bf16x2 __attribute__((ext_vector_type(2)));
typedef float f32x16 __attribute__((ext_vector_type(16)));
typedef float f32x4 __attribute__((ext_vector_type(4)));
typedef float f32x2 __attribute__((ext_vector_type(2)));
typedef unsigned u32x4 __attribute__((ext_vector_type(4)));
typedef unsigned u32x2 __attribute__((ext_vector_type(2)));
#define LAS __attribute__((address_space(3)))
#define GAS __attribute__((address_space(1)))
#define DI __device__ __forceinline__
#define SB() __builtin_amdgcn_sched_barrier(0)
#define PIN(x) asm volatile("" : "+v"(x))
typedef __amdgpu_buffer_rsrc_t rsrc_t;
typedef LAS unsigned char* lptr;

namespace {
constexpr int SEQ = 2048, DM = 448;
constexpr int SC1 = 16;
constexpr unsigned WS_EPOCH = 0;
constexpr unsigned WS_FLAGS = 4096;
constexpr unsigned HOP_B = 256 * 128;
constexpr unsigned TILE_B = 32768;
constexpr unsigned XO_B = 4 * TILE_B;
constexpr unsigned WS_XO = 1u << 20;
constexpr unsigned MID_B = 64 * 4096;
constexpr unsigned WS_MID = WS_XO + 16 * 8 * XO_B;
constexpr unsigned WS_PK = WS_MID + 64 * MID_B;
constexpr unsigned WS_PROF = WS_PK + 16 * 262144;
constexpr unsigned WS_END = WS_PROF + 256 * 2048;
constexpr unsigned SLAB = 448 * 64;
constexpr unsigned L_WK = 0, L_WV = SLAB, L_WQ = 2 * SLAB, L_WO = 3 * SLAB;
constexpr unsigned L_SCR = 0;
constexpr unsigned L_X0 = SLAB;
constexpr unsigned L_KVF = 5 * SLAB;
constexpr unsigned L_MK = L_KVF + 4096;
constexpr unsigned L_WPH = L_MK + 2048;
constexpr unsigned L_CS = L_WPH + 2048;
constexpr unsigned L_SYNC = L_CS + 6144;
constexpr unsigned L_PROF = L_SYNC + 64;
constexpr unsigned L_END = L_PROF + 2048;
static_assert(L_END <= 163840 && L_X0 + 4 * SLAB <= L_KVF, "lds");
constexpr unsigned SPIN_CAP = 1u << 15;
constexpr unsigned SENT = 0xffffffffu;
constexpr unsigned OOB = 0x7ff00000u;
constexpr unsigned ONES = 0x3f803f80u;
constexpr unsigned CODE_B = 270 * 128;
constexpr float L2E = 1.4426950408889634f;

DI f32x16 mfma(u32x4 a, u32x4 b, f32x16 c) {
  return __builtin_amdgcn_mfma_f32_32x32x16_bf16(__builtin_bit_cast(bf16x8, a), __builtin_bit_cast(bf16x8, b), c, 0, 0, 0);
}
DI unsigned pk2(float a, float b) { f32x2 t = {a, b}; return __builtin_bit_cast(unsigned, __builtin_convertvector(t, bf16x2)); }
template <int S_> DI u32x4 pack8(const f32x16& c) {
  return (u32x4){pk2(c[8 * S_], c[8 * S_ + 1]), pk2(c[8 * S_ + 2], c[8 * S_ + 3]), pk2(c[8 * S_ + 4], c[8 * S_ + 5]), pk2(c[8 * S_ + 6], c[8 * S_ + 7])};
}
DI u32x4 cat(u32x2 a, u32x2 b) { return (u32x4){a[0], a[1], b[0], b[1]}; }
DI u32x2 lo2(u32x4 v) { return (u32x2){v[0], v[1]}; }
DI unsigned umax3(unsigned a, unsigned b, unsigned c) { return max(max(a, b), c); }
DI unsigned max4(unsigned m, u32x4 v) { return umax3(umax3(m, v[0], v[1]), v[2], v[3]); }
DI unsigned add4(unsigned m, u32x4 v) { return (m + v[0] + v[1]) + v[2] + v[3]; }
DI unsigned xhalf_addu(unsigned v) { auto r = __builtin_amdgcn_permlane32_swap(v, v, false, false); return r[0] + r[1]; }
DI float fmax3(float a, float b, float c) { return fmaxf(fmaxf(a, b), c); }
template <int CTRL> DI float dppadd(float v) { return v + __builtin_bit_cast(float, __builtin_amdgcn_update_dpp(0, __builtin_bit_cast(int, v), CTRL, 0xf, 0xf, true)); }
DI float fmin3(float a, float b, float c) { return fminf(fminf(a, b), c); }
DI f32x16 splat(float v) { f32x16 z; _Pragma("unroll") for (int i = 0; i < 16; ++i) z[i] = v; return z; }
#define Z16 splat(0.f)
DI u32x4 trfrag(lptr p) {
  bf16x4 a0 = __builtin_amdgcn_ds_read_tr16_b64_v4bf16((LAS bf16x4*)p);
  bf16x4 a1 = __builtin_amdgcn_ds_read_tr16_b64_v4bf16((LAS bf16x4*)(p + 256));
  return __builtin_bit_cast(u32x4, __builtin_shufflevector(a0, a1, 0, 1, 2, 3, 4, 5, 6, 7));
}
DI u32x4 ldl(lptr p) { return *(LAS u32x4*)p; }
DI float U2F(unsigned u) { return __uint_as_float(u); }
DI unsigned F2U(float f) { return __float_as_uint(f); }
template <bool MX> DI void red2(float& a, float& b) {
  auto r = __builtin_amdgcn_permlane32_swap(F2U(a), F2U(b), false, false);
  const float t = MX ? fmaxf(U2F(r[0]), U2F(r[1])) : U2F(r[0]) + U2F(r[1]);
  r = __builtin_amdgcn_permlane32_swap(F2U(t), F2U(t), false, false);
  a = U2F(r[0]); b = U2F(r[1]);
}
DI float xhalf_max(float v) { auto r = __builtin_amdgcn_permlane32_swap(F2U(v), F2U(v), false, false); return fmaxf(U2F(r[0]), U2F(r[1])); }
DI float upper_val(float v) { auto r = __builtin_amdgcn_permlane32_swap(F2U(v), F2U(v), false, false); return U2F(r[1]); }
DI float dot2(unsigned u, unsigned v, float c) { return __builtin_amdgcn_fdot2_f32_bf16(__builtin_bit_cast(bf16x2, u), __builtin_bit_cast(bf16x2, v), c, false); }
DI float bflo(unsigned u) { return U2F(u << 16); }
DI float bfhi(unsigned u) { return U2F(u & 0xffff0000u); }
DI void split2(float v, unsigned& hi, unsigned& lo) { hi = pk2(v, v) & 0xffffu; lo = pk2(v - bflo(hi), 0.f) & 0xffffu; }
template <int AUX> DI void st128(u32x4 v, rsrc_t rs, unsigned voff, unsigned soff) {
  SB();
  __builtin_amdgcn_raw_buffer_store_b128(v, rs, voff, soff, AUX);
  SB();
  asm volatile("s_nop 1" :: "v"(v));
  SB();
}
DI void drain() { asm volatile("s_waitcnt vmcnt(0)" ::: "memory"); }
DI void bar() { SB(); asm volatile("s_waitcnt lgkmcnt(0)\n\ts_barrier" ::: "memory"); SB(); }
constexpr int chunk_of(int s, int hh) { return s < 24 ? 7 * (s / 3) + (s % 3 == 0 ? hh : (s % 3 == 1 ? 2 + 2 * hh : 5 + hh)) : 7 * (2 * (s - 24) + hh) + 3; }
constexpr int ord(int i) { return i < 4 ? 24 + i : i - 4; }

#define STAMP(i) do { } while (0)
#define RET(i) do { } while (0)
#define RSTAMP(i) do { } while (0)
#define STAMPX(i) STAMP(i)
#define CALS(hop) do { } while (0)
#define CALA(hop) do { } while (0)
#define TOT0
#define TOT1
#define DUMP(buf, acc, part, sc) do { } while (0)

template <typename F> DI void dual_softmax(const f32x16& p, float c, float wtail, u32x4 (&fp)[2], u32x4 (&fm)[2], F mid) {
  float mx = fmax3(p[0], p[1], p[2]), mn = fmax3(-p[0], -p[1], -p[2]);
#pragma unroll
  for (int i = 3; i < 15; i += 2) { mx = fmax3(mx, p[i], p[i + 1]); mn = fmax3(mn, -p[i], -p[i + 1]); }
  mx = fmax3(mx, p[15], p[15]); mn = fmax3(mn, -p[15], -p[15]);
  red2<true>(mx, mn);
  const float a = -c * mx, b = -c * mn;
  float nc = -c; PIN(nc);
  f32x16 ep = p * c + a, em = p * nc + b;
  SB();
#pragma unroll
  for (int i = 0; i < 16; ++i) ep[i] = __builtin_amdgcn_exp2f(ep[i]);
  PIN(ep);
  mid();
#pragma unroll
  for (int i = 0; i < 16; ++i) em[i] = __builtin_amdgcn_exp2f(em[i]);
  f32x2 sp = {ep[0], ep[1]}, sm = {em[0], em[1]};
#pragma unroll
  for (int i = 2; i < 12; i += 2) { sp += (f32x2){ep[i], ep[i + 1]}; sm += (f32x2){em[i], em[i + 1]}; }
  sp += wtail * ((f32x2){ep[12], ep[13]} + (f32x2){ep[14], ep[15]});
  sm += wtail * ((f32x2){em[12], em[13]} + (f32x2){em[14], em[15]});
  float u = sp[0] + sp[1], v = sm[0] + sm[1];
  red2<false>(u, v);
  ep *= __builtin_amdgcn_rcpf(u); em *= __builtin_amdgcn_rcpf(v);
  fp[0] = pack8<0>(ep); fp[1] = pack8<1>(ep); fm[0] = pack8<0>(em); fm[1] = pack8<1>(em);
}
DI u32x4 ident_frag(int s, int r, int hh) {
  const unsigned val = 0x3f80u << (16 * (r & 1));
  const int idx = (((r >> 2) ^ hh) & 1) * 8 + (r >> 4) * 4 + ((r >> 2) & 2) + ((r >> 1) & 1) - 4 * s;
  return (u32x4){idx == 0 ? val : 0u, idx == 1 ? val : 0u, idx == 2 ? val : 0u, idx == 3 ? val : 0u};
}
#define CHK1 3
#define CHK2 12
template <typename S, typename A> DI void steps28(S step, A abort) {
#pragma unroll
  for (int i = 0; i <= CHK1; ++i) step(i);
  if (abort()) return;
#pragma unroll
  for (int i = CHK1 + 1; i < 28; ++i) step(i);
}
template <int PD, bool TR, typename F, typename G, typename A> DI void gemm1(f32x16& acc, lptr wb, const u32x4 (&xf)[28], F pre, G post, A abort) {
  auto frag = [](lptr p) { return TR ? trfrag(p) : ldl(p); };
  u32x4 wr[PD];
#pragma unroll
  for (int i = 0; i < PD - 1; ++i) wr[i] = frag(wb + ord(i) * 1024);
  SB();
  steps28([&](int i) __attribute__((always_inline)) {
    pre(i);
    if (i + PD - 1 < 28) wr[(i + PD - 1) % PD] = frag(wb + ord(i + PD - 1) * 1024);
    SB();
    acc = mfma(wr[i % PD], xf[i], acc);
    SB();
    post(i);
    SB();
  }, abort);
}
template <typename H, typename F, typename G, typename A>
DI void gemm2(f32x16& ak, f32x16& av, lptr bk, const u32x4 (&xf)[28], H head, F pre, G post, A abort) {
  u32x4 wk[3], wv[3];
  head([&](int u) {
    if (u == 0) wv[0] = ldl(bk + SLAB + ord(0) * 1024);
    if (u == 1) wk[0] = ldl(bk + ord(0) * 1024);
    if (u == 2) wv[1] = ldl(bk + SLAB + ord(1) * 1024);
    if (u == 3) wk[1] = ldl(bk + ord(1) * 1024);
    SB();
  });
  steps28([&](int i) __attribute__((always_inline)) {
    pre(i);
    if (i + 2 < 28) wv[(i + 2) % 3] = ldl(bk + SLAB + ord(i + 2) * 1024);
    SB();
    av = mfma(wv[i % 3], xf[i], av);
    SB();
    if (i + 2 < 28) wk[(i + 2) % 3] = ldl(bk + ord(i + 2) * 1024);
    post(i, 0);
    SB();
    ak = mfma(wk[i % 3], xf[i], ak);
    SB();
    post(i, 1);
    SB();
  }, abort);
}
#define NOAB [] { return false; }
#define EARLYAB [&] { return __builtin_expect(__any(m == sent), 0); }
#define LA 12
#define XH 27
#define VSLEEP 8
#define QSLEEP 28
#define LAO 8
#define XBAR 26
#define DOA 3
#define DOB 6
#define DX 3
#define KVI 6
#define KVB 3
#define NKB 5
#define XM 4
#ifndef NV0
#define NV0 5
#endif
#define NK2 8
#define NK3 9
#define PACE 8
#define LAG 6
#define PACED() do { if (PACE) asm volatile("s_nop %0" :: "n"(PACE)); SB(); } while (0)
#define PSLP 4
#define RDLY 0
#define KDLY 0
#define LAND 10
#define TKA0 4000
#define TKA 3500
#define TKB1 2150
#define TKB 2400
#define TOA0 2050
#define TOA 1850
#define TOB1 2750
#define TOB 2520
#define TX0 3800
#define TX 2850
#define BACK 8
#define NQ0 2
#define NQB 0
#define TX2 3200
#define ND0 10
#define NPO 14
#define SWS 6
#define LMAX 1.8446744e19f
#define CSL 2
#define WSL 4
#define PACE2() do { asm volatile("s_nop %0" :: "n"(PACE - 2)); SB(); } while (0)
#define NQ1 4
#define REUSE 4
#define OBUF(l) ((REUSE & 1) ? 0 : 2 * (l))
#define XBUF(l) ((REUSE & 2) ? 1 + 2 * ((l) & 1) : 2 * (l) + 1)
#define MBUF(l) (((l) & 1) ? ((REUSE & 4) ? 1 : (l)) : ((REUSE & 8) ? 0 : (l)))
#define FW 1
#define FLOORK 45
#ifndef NV1
#define NV1 3
#endif
#define PDELAY 14

__global__ void __launch_bounds__(512, 2) fused_kernel(const u16* __restrict__ x0, const u16* __restrict__ wqkvA, const u16* __restrict__ wphi,
                                                       const u16* __restrict__ woA, const u16* __restrict__ wqkvB, const u16* __restrict__ woB,
                                                       u16* __restrict__ out, unsigned char* ws) {
  extern __shared__ __attribute__((aligned(16))) unsigned char smem[];
  u64 tstart; asm volatile("s_memrealtime %0\n\ts_waitcnt lgkmcnt(0)" : "=s"(tstart) :: "memory"); SB();
  unsigned probe = 0;
  const lptr lds = (lptr)smem;
  const int tid = threadIdx.x, lane = tid & 63, wave = __builtin_amdgcn_readfirstlane(tid >> 6);
  TOT0
  const int w = wave & 3;
  const int r = lane & 31, hh = lane >> 5;
  const int b = blockIdx.x;
  const int tb = ((b >> 1) & 3) * 4 + (b >> 6), h = (b & 1) * 8 + ((b >> 3) & 7);
  const int me = tb * 16 + h;
  const int odd = h & 1, pair = h >> 1;
  const int T = tb * 4 + w;
  const unsigned TOFF = tb * 8 * XO_B + w * TILE_B;
  const int kh = w & 1, tx = 2 * odd + (w >> 1);
  const unsigned XOFF = tb * 8 * XO_B + tx * TILE_B;
  const rsrc_t rWS = __builtin_amdgcn_make_buffer_rsrc((void*)ws, 0, WS_END, 0x00020000);
  unsigned budget = SPIN_CAP;
  u64 epoch;
  u32x2 tag;
  u32x2 ev;
  auto load_epoch = [&]() { ev = __builtin_amdgcn_raw_buffer_load_b64(rWS, 0, WS_EPOCH, SC1); SB(); };
  auto make_tag = [&]() {
    epoch = (u64)(unsigned)__builtin_amdgcn_readfirstlane(ev[0]) | ((u64)(unsigned)__builtin_amdgcn_readfirstlane(ev[1]) << 32);
    u64 z = epoch + 0x9E3779B97F4A7C15ull;
    z = (z ^ (z >> 30)) * 0xBF58476D1CE4E5B9ull; z = (z ^ (z >> 27)) * 0x94D049BB133111EBull; z ^= z >> 31;
    unsigned t0 = (unsigned)z, t1 = (unsigned)(z >> 32);
    asm volatile("" : "+s"(t0), "+s"(t1));
    tag = (u32x2){t0, t1};
  };
  volatile LAS unsigned* const cnt = (volatile LAS unsigned*)(lds + L_SYNC);
  auto arrive = [&](int k) { if (lane == 0) asm volatile("ds_add_u32 %0, %1" :: "v"(lds + L_SYNC + 4 * k), "v"(1u) : "memory"); };
  auto gather_all = [&](int layer, bool nap) {
    while (__builtin_amdgcn_readfirstlane(*cnt) < 8u * layer + 4u) { if (nap) __builtin_amdgcn_s_sleep(2); }
    asm volatile("" ::: "memory");
  };
  auto poll = [&](int hop, int mask, int floor = 0) {
    const unsigned t0 = (unsigned)__builtin_amdgcn_s_memrealtime() + floor;
    int ln = lane & mask; PIN(ln);
    u32x2 v;
    for (;;) {
      v = __builtin_amdgcn_raw_buffer_load_b64(rWS, ln * 8, WS_FLAGS + hop * HOP_B + me * 128, SC1);
      asm volatile("" ::: "memory");
      if (__all(v[0] == tag[0] && ((v[1] ^ tag[1]) >> 20) == 0)) break;
      if (budget <= 1) break;
      --budget;
      if (PSLP) __builtin_amdgcn_s_sleep(PSLP);
    }
    const unsigned t = (lane < 32 ? v[1] : t0) << 12;
    for (int n = 0; n < 64; ++n) {
      const unsigned now = (unsigned)__builtin_amdgcn_s_memrealtime() << 12;
      if (!__any((int)(t - now) > 0)) break;
    }
    asm volatile("" ::: "memory");
  };
  auto make_trb = [&]() {
    const int rb = (lane >> 4) & 1, q = (lane >> 2) & 3, p = lane & 3;
    const int rp = 4 * rb + p, lam = (rp & 4) | ((rp & 1) << 1) | ((rp >> 1) & 1);
    const int pc = ((odd ? 0x01327654 : 0x76543210) >> (4 * lam)) & 7;
    return lds + (8 * hh + q) * 64 + pc * 8;
  };
  auto store_x0 = [&](u32x4 f0, unsigned base) { int ln = lane; PIN(ln); st128<SC1>(f0, rWS, ln * 16, base + XOFF + (3 * pair + 2 * kh) * 1024); };
  auto store_x1 = [&](u32x4 f1, unsigned base) {
    int ln = lane; PIN(ln);
    st128<SC1>(f1, rWS, ln * 16 + __umul24(ln >> 5, 24576 - 512 - 1024 - pair * 2048), base + XOFF + (3 * pair + 1) * 1024 + kh * 512);
  };
  auto store_o = [&](u32x4 f0, u32x4 f1, unsigned base) {
    int ln = lane; PIN(ln);
    st128<SC1>(f0, rWS, ln * 16, base + TOFF + (3 * pair + 2 * odd) * 1024);
    st128<SC1>(f1, rWS, ln * 16 + (ln >> 5) * OOB, base + TOFF + (3 * pair + 1) * 1024 + odd * 512);
    __builtin_amdgcn_raw_buffer_store_b64(lo2(f1), rWS, ln * 8 + (1 - (ln >> 5)) * OOB, base + TOFF + 24576 - 256 + h * 256, SC1);
  };
  const int ht = tid & 255, sub = lane & 3;
  const int col = h * 56 - odd * 8;
  int krow[7];
  auto mkrow = [&](int i) {
    const int s = 4 * i + w;
    const int p = (s * 43) >> 7, m = s - 3 * p;
    const int c = i < 6 ? 7 * p + ((0x520 >> (4 * m)) & 15) + hh * (1 + (m & 1)) : 7 * (2 * w + hh) + 3;
    krow[i] = __umul24(8 * c + ((lane >> 2) & 7), 2688) + sub * 16;
  };
  auto ldw = [&](rsrc_t rs, int i, int stride, int off) { const u32x4 v = __builtin_amdgcn_raw_buffer_load_b128(rs, krow[i], col + off, 0); SB(); return v; };
  auto write_slab = [&](const u32x4* p, unsigned off) {
    lptr d = lds + off + ht * 16; PIN(d);
#pragma unroll
    for (int i = 0; i < 7; ++i) *(LAS u32x4*)(d + i * 4096) = p[i];
  };
  auto patch = [&](u32x4* p, int n, bool ones) __attribute__((always_inline)) {
    unsigned k01 = (odd && sub == 0) ? 0u : ~0u, k23 = (!odd && sub == 3) ? 0u : ~0u; PIN(k01); PIN(k23);
    const unsigned o0 = ones ? ~k01 & 0x3f80u : 0u, o2 = ones ? ~k23 & 0x3f80u : 0u;
#pragma unroll
    for (int i = 0; i < n; ++i) { p[i][0] = (p[i][0] & k01) | o0; p[i][1] &= k01; p[i][2] = (p[i][2] & k23) | o2; p[i][3] &= k23; }
  };
  auto put_slab = [&](u32x4* p, int type, int part) {
    patch(p, 7, type == 0 && part == 0);
    write_slab(p, part * SLAB);
  };
  auto read_frags = [&](u32x4* p, int part, lptr trb) {
    lptr wb = trb + part * SLAB + w * 1024; PIN(wb);
#pragma unroll
    for (int i = 0; i < 7; ++i) p[i] = trfrag(wb + i * 4096);
  };
  auto sum_frags = [&](const u32x4* p, int type, int part) {
    f32x16 a = Z16;
#pragma unroll
    for (int i = 0; i < 7; ++i) a = mfma((u32x4){ONES, ONES, ONES, ONES}, p[i], a);
    unsigned hi, lo; split2(a[0], hi, lo);
    if (hh == (w >> 1)) *(LAS u32x2*)(lds + L_CS + type * 3072 + part * 1024 + lane * 16 + (w & 1) * 8) = (u32x2){hi | (lo << 16), hi};
  };
  const rsrc_t rWA = __builtin_amdgcn_make_buffer_rsrc((void*)wqkvA, 0, DM * 3 * DM * 2, 0x00020000);
  auto dma_x0 = [&](int i0, int i1) __attribute__((always_inline)) {
    const rsrc_t rX0 = __builtin_amdgcn_make_buffer_rsrc((void*)x0, 0, SEQ * DM * 2, 0x00020000);
    const int o0 = (lane >> 3) * 896 + (((lane & 7) ^ (lane >> 4)) * 16), o1 = o0 ^ 64;
#pragma unroll
    for (int i = i0; i < i1; ++i) {
      __builtin_amdgcn_raw_ptr_buffer_load_lds(rX0, (LAS void*)(lds + L_X0 + w * SLAB + i * 1024), 16, (i & 1) ? o1 : o0, (T * 32 + (i & 3) * 8) * 896 + (i >> 2) * 128, 0, 0);
      SB();
    }
  };

  if (wave < 4) {
    RSTAMP(62); STAMP(0);
    u32x4 xf[28];
    f32x16 xs, ak, av;
    float rstd;
    u32x4 mf;
    auto stats = [&](float s1, float s2) {
      const float mean = s1 * (1.0f / DM);
      rstd = __builtin_amdgcn_rsqf(fmaxf(s2 * (1.0f / DM) - mean * mean, 0.f) + 1e-5f);
      unsigned hi, lo; split2(-mean, hi, lo);
      mf = (u32x4){hi | (hi << 16), lo, hi | (hi << 16), lo};
      PIN(rstd); PIN(mf);
    };
    u32x4 id0, id1;
    float wtail;
    unsigned lomask;
    {
      __builtin_amdgcn_s_setprio(3);
      id0 = ident_frag(0, r, hh); id1 = ident_frag(1, r, hh);
      wtail = hh ? 0.f : 1.f; lomask = hh ? 0u : ~0u;
      PIN(id0); PIN(id1); PIN(wtail); PIN(lomask);
      if (w >= 2) {
        u32x4 mk;
#pragma unroll
        for (int i = 0; i < 4; ++i) { const int k0 = ((2 * i) & 3) + 8 * (i >> 1) + 4 * hh + 16 * (w & 1); mk[i] = (k0 > r ? 0xffffu : 0u) | (k0 + 1 > r ? 0xffff0000u : 0u); }
        *(LAS u32x4*)(lds + L_MK + (w & 1) * 1024 + lane * 16) = mk;
      }
      if (w == 0) { cnt[0] = 0u; cnt[1] = 0u; cnt[2] = 0u; cnt[3] = 0u; cnt[4] = 0u; cnt[5] = 0u; cnt[6] = 0u; }
      cnt[8 + w] = 0u; cnt[12 + w] = 0u;
      const int lam = 4 * (w >> 1) + 2 * hh + (w & 1);
      const int pc = ((odd ? 0x01327654 : 0x76543210) >> (4 * lam)) & 7;
      unsigned off = lam == 7 ? OOB : ((h * 28 + 4 * pc - 4 * odd) * 28 + (r < 28 ? r : r - 8)) * 2;
      const rsrc_t rPH = __builtin_amdgcn_make_buffer_rsrc((void*)wphi, 0, 16 * 28 * 28 * 2, 0x00020000);
#pragma unroll
      for (int j = 0; j < 4; ++j) xf[27][j] = __builtin_amdgcn_raw_buffer_load_b16(rPH, off + j * 56, 0, 0);
      load_epoch();
      xf[26] = cat(ev, ev);
      __builtin_amdgcn_s_setprio(0);
      STAMPX(54);
    }
    tag = (u32x2){0u, 0u}; epoch = 0;
    asm volatile("" : "=v"(ak), "=v"(av), "=v"(rstd), "=v"(mf), "=v"(xs));
#pragma unroll
    for (int i = 0; i < 26; ++i) asm volatile("" : "=v"(xf[i]));
    unsigned sent = SENT;
    auto release = [&](int hop) { if (w == 0) poll(hop, 15); bar(); };
    for (int layer = 0; layer < NL; ++layer) {
      const int type = layer & 1;
      const int pb = 2 + layer * 14;
      if (layer > 0) {
        unsigned base = WS_XO + XBUF(layer - 1) * XO_B + TOFF;
        int v16 = lane * 16, vs = v16 + (v16 & 512);
        lptr bk = lds + L_WK + v16;
        PIN(vs);
        release(3 * layer - 1);
        STAMP(pb + 0);
        for (;;) {
          PIN(v16); PIN(vs); PIN(bk); asm volatile("" : "+s"(base));
          u32x4 sp[8];
          auto ld = [&](int s) { xf[4 + s] = __builtin_amdgcn_raw_buffer_load_b128(rWS, v16 + (s & 3) * 1024, base + (s >> 2) * 4096, SC1); SB(); };
          unsigned m = 1;
          ak = Z16; av = Z16;
          float s1 = 0.f, s2 = 0.f, mean;
          unsigned hi;
          u32x4 ck, cv;
          gemm2(ak, av, bk, xf,
                    [&](auto gap) {
#pragma unroll
                      for (int u = 0; u < 8; ++u) { sp[u] = __builtin_amdgcn_raw_buffer_load_b128(rWS, vs + ((u >> 1) & 1) * 2048 + (u & 1) * 512, base + 24576 + (u >> 2) * 4096, SC1); SB(); gap(u); if (u < 4) PACE2(); else PACED(); }
#pragma unroll
                      for (int s = 0; s < LA - 8; ++s) { ld(s); PACED(); }
                    },
                    [&](int i) {
                      if (i + LA - 8 < 24) ld(i + LA - 8);
                      if (i < 4) { PIN(sp[2 * i]); PIN(sp[2 * i + 1]); xf[i] = cat(lo2(sp[2 * i]), lo2(sp[2 * i + 1])); }
                      if (i == 22) { av = mfma(cv, mf, av); SB(); }
                      if (i == 24) { ak = mfma(ck, mf, ak); SB(); }
                    },
                    [&](int i, int hf) {
                      if (i < 4) m = max4(m, sp[2 * i + hf]); else m = umax3(m, xf[i][2 * hf], xf[i][2 * hf + 1]);
                      PIN(m);
                      if (i < 8) { if (hf) { s2 += U2F(sp[i][3]); PIN(s2); } else { s1 += U2F(sp[i][2]); PIN(s1); } }
                      if (hf) return;
                      if (i == 13) asm volatile("ds_write_b32 %0, %1" :: "v"(lds + L_SYNC + 32 + 4 * w), "v"(layer) : "memory");
                      if (i == 9) { auto t = __builtin_amdgcn_permlane32_swap(F2U(s1), F2U(s2), false, false); s1 = U2F(t[0]) + U2F(t[1]); PIN(s1); }
                      if (i == 10) { auto t = __builtin_amdgcn_permlane32_swap(F2U(s1), F2U(s1), false, false); s1 = U2F(t[0]); s2 = U2F(t[1]); PIN(s1); PIN(s2); }
                      if (i == 14) { mean = s1 * (-1.0f / DM); s2 = s2 * (1.0f / DM); PIN(mean); PIN(s2); }
                      if (i == 15) { s2 = fmaxf(fmaf(-mean, mean, s2), 0.f); PIN(s2); }
                      if (i == 16) { rstd = __builtin_amdgcn_rsqf(s2 + 1e-5f); PIN(rstd); }
                      if (i == 17) { hi = pk2(mean, mean); s1 = mean - bfhi(hi); PIN(hi); PIN(s1); }
                      if (i == 18) { const unsigned lo = pk2(s1, 0.f); mf = (u32x4){hi, lo, hi, lo}; PIN(mf); }
                      if (i == 19) { const lptr cs = lds + L_CS + type * 3072 + v16; ck = ldl(cs); cv = ldl(cs + 1024); }
                    }, EARLYAB);
          if (__builtin_expect(!__any(m == sent), 1)) break;
          if (--budget == 0) sent = 0u;
          RET(layer * 3);
          __builtin_amdgcn_s_sleep(BACK);
          asm volatile("" ::: "memory");
        }
      }
      int first = layer; asm volatile("" : "+s"(first));
      if (first == 0) {
        int ln = lane; PIN(ln);
        const int hh = ln >> 5;
        const unsigned ra = (ln & 31) * 128 + ((ln >> 1) & 7) * 16;
        const lptr stg = lds + L_X0 + w * SLAB;
        auto kof = [](int c) { return (c >> 3) * 4096 + (c & 7) * 16; };
        lptr ad[28];
#pragma unroll
        for (int i = 0; i < 28; ++i) {
          const int k0 = kof(chunk_of(ord(i), 0)), k1 = kof(chunk_of(ord(i), 1));
          ad[i] = stg + (ra ^ k0 ^ (~lomask & (k0 ^ k1))); PIN(ad[i]);
        }
        lptr pab = lds + L_X0 + tx * SLAB + (ra ^ kof(7 * pair + 5 * kh + hh)), pc = lds + L_X0 + tx * SLAB + (ra ^ kof(7 * pair + (hh ? 3 : 2 + 2 * kh))); PIN(pab); PIN(pc);
        lptr trb = make_trb(); PIN(trb);
        SB();
        *(LAS u32x2*)(lds + L_WPH + (w >> 1) * 1024 + ln * 16 + (w & 1) * 8) = (u32x2){xf[27][0] | (xf[27][1] << 16), xf[27][2] | (xf[27][3] << 16)};
        ev = lo2(xf[26]);
        make_tag();
        STAMPX(53);
        bar();
probe = (unsigned)(__builtin_amdgcn_s_memrealtime() - tstart);
#pragma unroll
        for (int i = 0; i < 28; ++i) xf[i] = ldl(ad[i]);
        u32x4 xa = ldl(pab), xc = ldl(pc);
        SB();
        float s2a = 0.f, s2b = 0.f;
#pragma unroll
        for (int i = 0; i < 28; ++i) {
          const u32x4 u = xf[i]; s2a = dot2(u[1], u[1], dot2(u[0], u[0], s2a)); s2b = dot2(u[3], u[3], dot2(u[2], u[2], s2b));
        }
        PIN(s2a); PIN(s2b);
        STAMPX(56);
        bar();
        STAMP(1);
        u32x4 cc = ldl(lds + L_CS + ln * 16);
        ak = Z16;
        lptr bk = trb + L_WK; PIN(bk);
        gemm1<5, true>(ak, bk, xf, [](int) {}, [&](int i) {
          if (i == 4) {
            const unsigned sel = hh && kh ? 0u : ~0u;
            xc[0] = (xc[0] & sel) | (xc[2] & ~sel); xc[1] = (xc[1] & sel) | (xc[3] & ~sel); PIN(xc);
          }
          if (i >= 16 && i < 24) { const unsigned u = i < 20 ? xa[i & 3] : xc[i & 3]; xs[2 * (i - 16)] = bflo(u); xs[2 * (i - 16) + 1] = bfhi(u); PIN(xs); }
        }, NOAB);
        asm volatile("" :: "v"(ak));
        STAMPX(57);
        bar();
        STAMP(2);
        const u32x4 cv = ldl(lds + L_CS + 1024 + ln * 16);
        av = Z16;
        gemm1<5, true>(av, bk + SLAB, xf, [&](int i) { if (i == 8) { ak = mfma(cc, mf, ak); SB(); } if (i == 10) { av = mfma(cv, mf, av); SB(); } }, [&](int i) {
          if (i == 2) {
            float s1 = U2F(F2U(ak[12]) & ~lomask), s2 = s2a + s2b;
            red2<false>(s1, s2);
            stats(s1, s2);
          }
        }, NOAB);
      }
      asm volatile("" ::: "memory");
      arrive(0);
      STAMPX(pb + 1);
      const unsigned mid = WS_MID + (h * 4 + MBUF(layer)) * MID_B;
      int ln16 = lane * 16; PIN(ln16);
      u32x4 wph[2], kf0, kf1;
      if (type == 0) {
        wph[0] = ldl(lds + L_WPH + ln16); wph[1] = ldl(lds + L_WPH + 1024 + ln16);
        DUMP(0, ak, 1, rstd); DUMP(0, av, 2, rstd);
        kf0 = pack8<0>(ak); kf1 = pack8<1>(ak);
        f32x16 p = mfma(wph[0], kf0, Z16);
        p = mfma(wph[1], kf1, p);
        SB();
#pragma unroll
        for (int i = 0; i < 16; ++i) { float t = av[i] * rstd; PIN(t); av[i] = t; }
        u32x4 vb0 = pack8<0>(av), vb1 = pack8<1>(av);
        vb1[2] |= ~lomask & 0x3f80u;
        PIN(vb0); PIN(vb1);
        lptr ta0, ta1, tb0, tb1;
        {
          const int q = (ln16 >> 6) & 3, rp = ((ln16 >> 6) & 4) + ((ln16 >> 4) & 3), lam = (rp & 4) | ((rp & 1) << 1) | ((rp >> 1) & 1);
          const int o0 = (8 * (ln16 >> 9) + q) * 64 + (((lam >> 1) ^ (q >> 1)) * 16) + (lam & 1) * 8, o1 = (o0 ^ 32) + 256;
          tb0 = lds + L_SCR + o0; tb1 = lds + L_SCR + o1; ta0 = tb0 + (w >> 1) * 8192; ta1 = tb1 + (w >> 1) * 8192;
          PIN(ta0); PIN(ta1); PIN(tb0); PIN(tb1);
        }
        const int rr = (ln16 >> 4) & 31, pc = (ln16 >> 9) ^ ((rr >> 1) & 3);
        lptr d0 = lds + L_SCR + w * 2048 + rr * 64 + pc * 16, d1 = lds + L_SCR + w * 2048 + rr * 64 + (pc ^ 2) * 16;
        lptr ma = lds + L_SYNC + ((ln16 & 64) ? 12 : 48 + ((ln16 >> 2) & 12)), mw = lds + L_SYNC + 48 + 4 * w;
        unsigned mv = (ln16 & 64) ? 4u : layer + 1u, ml = layer + 1u, got = *cnt;
        PIN(d0); PIN(d1); PIN(ma); PIN(mw); PIN(mv); PIN(ml);
        SB();
        u32x4 fp[2], fm[2];
        dual_softmax(p, rstd * L2E, wtail, fp, fm, [&] {
          if (__builtin_amdgcn_readfirstlane(got) < 8u * layer + 4u) gather_all(layer, false);
          asm volatile("" ::: "memory");
          *(LAS u32x4*)(d0 + 16384) = vb0; *(LAS u32x4*)(d1 + 16384) = vb1;
          SB();
        });
        fp[1][2] &= lomask; fp[1][3] &= lomask; fm[1][2] &= lomask; fm[1][3] &= lomask;
        *(LAS u32x4*)(d0) = fp[0]; *(LAS u32x4*)(d1) = fp[1];
        *(LAS u32x4*)(d0 + 8192) = fm[0]; *(LAS u32x4*)(d1 + 8192) = fm[1];
        asm volatile("ds_write_b32 %0, %1" :: "v"(mw), "v"(ml) : "memory");
        while (!__all(*(volatile LAS unsigned*)ma >= mv)) { }
        asm volatile("" ::: "memory");
        {
          auto tr2 = [](lptr p0, lptr p1) {
            bf16x4 a0 = __builtin_amdgcn_ds_read_tr16_b64_v4bf16((LAS bf16x4*)p0), a1 = __builtin_amdgcn_ds_read_tr16_b64_v4bf16((LAS bf16x4*)p1);
            return __builtin_bit_cast(u32x4, __builtin_shufflevector(a0, a1, 0, 1, 2, 3, 4, 5, 6, 7));
          };
          u32x4 fa[4], fb[4];
          f32x16 kv = Z16;
#pragma unroll
          for (int i = 0; i < 3; ++i) { fa[i] = tr2(ta0 + i * 1024, ta1 + i * 1024); fb[i] = tr2(tb0 + 16384 + i * 1024, tb1 + 16384 + i * 1024); SB(); }
#pragma unroll
          for (int i = 0; i < 8; ++i) {
            kv = mfma(fa[i & 3], fb[i & 3], kv);
            SB();
            if (i + 3 < 8) { fa[(i + 3) & 3] = tr2(ta0 + (i + 3) * 1024, ta1 + (i + 3) * 1024); fb[(i + 3) & 3] = tr2(tb0 + 16384 + (i + 3) * 1024, tb1 + 16384 + (i + 3) * 1024); }
            SB();
          }
          st128<SC1>((w & 1) ? pack8<1>(kv) : pack8<0>(kv), rWS, ln16, mid + (tb * 4 + w) * 1024);
          CALS(3 * layer);
        }
        while (__builtin_amdgcn_readfirstlane(cnt[6]) < 4u) __builtin_amdgcn_s_sleep(2);
        asm volatile("" ::: "memory");
      } else {
        av *= rstd;
        DUMP(0, av, 2, 1.f);
        u32x4 vb1 = pack8<1>(av);
        vb1[2] |= ~lomask & 0x3f80u;
        f32x16 vt = mfma(pack8<0>(av), id0, Z16);
        vt = mfma(vb1, id1, vt);
        SB();
#pragma unroll
        for (int i = 0; i < 16; ++i) { float t = ak[i] * rstd; PIN(t); ak[i] = t; }
        DUMP(0, ak, 1, 1.f);
        kf0 = pack8<0>(ak); kf1 = pack8<1>(ak);
        const unsigned g = mid + T * 4096;
        const lptr d = lds + L_SCR + (8 + w) * 4096 + ln16;
        st128<SC1>(kf0, rWS, ln16, g);
        const u32x4 vf0 = pack8<0>(vt), vf1 = pack8<1>(vt);
        st128<SC1>(vf0, rWS, ln16 + 2048, g);
        st128<SC1>(vf1, rWS, ln16 + 3072, g);
        {
          u32x4 kc = kf1;
          kc[3] |= ~lomask & (tag[0] + layer - xhalf_addu(add4(add4(0u, kf0), kf1) + add4(add4(0u, vf0), vf1)));
          st128<SC1>(kc, rWS, ln16 + 1024, g);
          CALS(3 * layer);
        }
        gather_all(layer, false);
        *(LAS u32x4*)(d) = kf0; *(LAS u32x4*)(d + 1024) = kf1; *(LAS u32x4*)(d + 2048) = vf0; *(LAS u32x4*)(d + 3072) = vf1;
      }
      STAMPX(pb + 2);
      u32x4 qf0, qf1;
      {
        f32x16 aq = mfma(ldl(lds + L_CS + type * 3072 + 2048 + ln16), mf, Z16);
        lptr bq = lds + L_WQ + ln16; PIN(bq);
        gemm1<5, false>(aq, bq, xf, [](int) {}, [](int) {}, NOAB);
        aq *= type ? rstd * (0.18898223650461361f * L2E) : 1.0f;
        DUMP(0, aq, 0, type ? 1.f : rstd);
        qf0 = pack8<0>(aq); qf1 = pack8<1>(aq);
        qf1[2] |= ~lomask & 0x3f80u;
      }
      f32x16 o;
      if (type == 0) {
        u32x4 fp[2], fm[2];
        {
          f32x16 p = mfma(wph[0], qf0, Z16);
          p = mfma(wph[1], qf1, p);
          dual_softmax(p, rstd * L2E, wtail, fp, fm, [] {});
        }
        PIN(fp[0]); PIN(fp[1]); PIN(fm[0]); PIN(fm[1]);
        STAMPX(pb + 3);
        bar();
        STAMP(pb + 4);
        const lptr kvf = lds + L_KVF + ln16;
        const u32x4 a0 = ldl(kvf), a1 = ldl(kvf + 1024), a2 = ldl(kvf + 2048), a3 = ldl(kvf + 3072);
        SB();
        o = mfma(a0, fp[0], Z16);
        o = mfma(a1, fp[1], o);
        o = mfma(a2, fm[0], o);
        o = mfma(a3, fm[1], o);
        o *= __builtin_amdgcn_rcpf(upper_val(o[12]) + 1e-6f);
      } else {
        lptr kv = lds + L_SCR + w * 4096 + ln16; PIN(kv);
        auto ex4 = [&](f32x16& s, int i0) {
#pragma unroll
          for (int i = i0; i < i0 + 4; ++i) s[i] = __builtin_amdgcn_exp2f(s[i]);
          unsigned a = pk2(s[i0], s[i0 + 1]), c = pk2(s[i0 + 2], s[i0 + 3]); PIN(a); PIN(c);
          SB();
          return (u32x2){a, c};
        };
        auto tile_max = [&](int j, int side, float m) {
          f32x16 t = mfma(ldl(kv + j * 4096 + 1024), qf1, mfma(ldl(kv + j * 4096), qf0, Z16));
          int lim = ((ln16 >> 4) & 31) - ((ln16 >> 7) & 4); PIN(lim);
#pragma unroll
          for (int i = 0; i < 16; ++i) { const bool up = (i & 3) + 8 * (i >> 2) > lim; m = fmaxf(m, (side == 0 || up == (side == 1)) ? t[i] : m); }
          return m;
        };
        float m = -__builtin_inff(), l;
        for (int pass = 0;; ++pass) {
          const u32x4 mk0 = ldl(lds + L_MK + ln16), mk1 = ldl(lds + L_MK + 1024 + ln16);
          f32x16 negm;
          {
            const u32x4 v0 = ldl(kv + 8 * 4096 + 2048), v1 = ldl(kv + 8 * 4096 + 3072);
            f32x16 s;
#pragma unroll
            for (int i = 0; i < 16; ++i) { unsigned k = (i < 8 ? mk0 : mk1)[(i >> 1) & 3]; if (!(i & 1)) { k <<= 16; PIN(k); } s[i] = U2F(k & 0xff800000u); }
            s = mfma(ldl(kv + 8 * 4096 + 1024), qf1, mfma(ldl(kv + 8 * 4096), qf0, s));
#pragma unroll
            for (int i = 0; i < 16; i += 2) m = fmax3(m, s[i], s[i + 1]);
            m = xhalf_max(m);
            s -= m;
            negm = splat(-m);
            PIN(negm);
            const u32x2 a = ex4(s, 0), c = ex4(s, 4), e = ex4(s, 8), f = ex4(s, 12);
            o = mfma(v1, cat(e, f), mfma(v0, cat(a, c), Z16));
            PIN(o);
          }
          if (pass == 0) {
            STAMPX(pb + 3);
            bar();
            STAMP(pb + 4);
          }
          u32x4 kk[2][2], vv[2][2], pa, pb2;
          f32x16 st[2];
          kk[0][0] = ldl(kv); kk[0][1] = ldl(kv + 1024); kk[1][0] = ldl(kv + 4096); kk[1][1] = ldl(kv + 4096 + 1024);
          SB();
          st[0] = mfma(kk[0][1], qf1, mfma(kk[0][0], qf0, negm));
          SB();
#pragma unroll
          for (int j = 0; j < 8; ++j) {
            f32x16& s = st[j & 1];
            vv[j & 1][0] = ldl(kv + j * 4096 + 2048); vv[j & 1][1] = ldl(kv + j * 4096 + 3072);
            if (j < 6) { kk[j & 1][0] = ldl(kv + (j + 2) * 4096); kk[j & 1][1] = ldl(kv + (j + 2) * 4096 + 1024); }
            SB();
            if (j == 0) { st[1] = mfma(kk[1][0], qf0, negm); SB(); }
            const u32x2 a = ex4(s, 0);
            if (j == 0) st[1] = mfma(kk[1][1], qf1, st[1]); else o = mfma(vv[(j - 1) & 1][0], pa, o);
            SB();
            const u32x2 c = ex4(s, 4);
            if (j == 7) o = mfma(vv[0][1], pb2, o); else if (j > 0) st[(j + 1) & 1] = mfma(kk[(j + 1) & 1][0], qf0, negm);
            SB();
            const u32x2 e = ex4(s, 8);
            if (j == 7) o = mfma(vv[1][0], cat(a, c), o); else if (j > 0) st[(j + 1) & 1] = mfma(kk[(j + 1) & 1][1], qf1, st[(j + 1) & 1]);
            SB();
            const u32x2 f = ex4(s, 12);
            if (j == 7) o = mfma(vv[1][1], cat(e, f), o); else if (j > 0) o = mfma(vv[(j - 1) & 1][1], pb2, o);
            SB();
            pa = cat(a, c); pb2 = cat(e, f);
            if (j == 0) { pa &= mk0; pb2 &= mk1; PIN(pa); PIN(pb2); }
          }
          l = upper_val(o[12]);
          if (pass > 0 || __all(l < LMAX)) break;
          RET(13);
          m = tile_max(0, 1, m);
#pragma unroll 1
          for (int j = 1; j < 8; ++j) m = tile_max(j, 0, m);
        }
        arrive(4);
        o *= __builtin_amdgcn_rcpf(l);
      }
      DUMP(1, o, 0, 1.f);
      store_o(pack8<0>(o), pack8<1>(o), WS_XO + OBUF(layer) * XO_B);
      CALS(3 * layer + 1);
      STAMPX(pb + 6);
      lptr bown = lds + L_WO + kh * (SLAB + 1024) + ln16, both = lds + L_WO + kh * (SLAB - 1024) + 1024 + ln16;
      PIN(bown); PIN(both);
      unsigned base = WS_XO + OBUF(layer) * XO_B + XOFF + kh * 12288;
      int v16 = ln16, vs = (v16 >> 1) + ((v16 >> 1) & 256);
      PIN(vs);
      release(3 * layer + 1);
      STAMP(pb + 7);
      f32x16 xn, xo;
      u32x4 wa[4];
      auto tr = [](lptr p) { return ldl(p); };
      for (;;) {
        PIN(v16); PIN(vs); asm volatile("" : "+s"(base));
        u32x2 sp[4];
        u32x4 wb[4];
#pragma unroll
        for (int u = 0; u < 4; ++u) { sp[u] = __builtin_amdgcn_raw_buffer_load_b64(rWS, vs + (u >> 1) * 1024 + (u & 1) * 256, base + 24576 - kh * 10240, SC1); SB(); if (u & 1) PACED(); }
#pragma unroll
        for (int s = 0; s < LAO; ++s) { xf[2 + s] = __builtin_amdgcn_raw_buffer_load_b128(rWS, v16 + (s & 3) * 1024, base + (s >> 2) * 4096, SC1); SB(); if (s < 3) wb[s] = tr(both + s * 2048); else if (s < 6) wa[s - 3] = tr(bown + (s - 3) * 2048); SB(); if (s < 6) PACE2(); else PACED(); }
        xf[0] = cat(sp[0], sp[1]); xf[1] = cat(sp[2], sp[3]);
        xn = xs; xo = Z16;
        unsigned m = 1;
#pragma unroll
        for (int i = 0; i < 14; ++i) {
          if (i + LAO < 12) { const int s = i + LAO; xf[2 + s] = __builtin_amdgcn_raw_buffer_load_b128(rWS, v16 + (s & 3) * 1024, base + (s >> 2) * 4096, SC1); SB(); }
          if (i + 3 < 14) wb[(i + 3) & 3] = tr(both + (i + 3) * 2048);
          SB();
          xo = mfma(wb[i & 3], xf[i], xo);
          SB();
          m = max4(m, xf[i]); PIN(m);
          if (i >= LAG) {
            wa[(i - LAG + 3) & 3] = tr(bown + (i - LAG + 3) * 2048);
            SB();
            xn = mfma(wa[(i - LAG) & 3], xf[i - LAG], xn);
          }
          SB();
        }
        if (__builtin_expect(!__any(m == sent), 1)) break;
        if (--budget == 0) sent = 0u;
        RET(layer * 3 + 2);
        __builtin_amdgcn_s_sleep(BACK);
        asm volatile("" ::: "memory");
      }
      {
        const lptr ex = lds + L_WQ + ln16;
        f32x4 t[4];
#pragma unroll
        for (int i = 14 - LAG; i < 14; ++i) {
          if (i + 3 < 14) wa[(i + 3) & 3] = tr(bown + (i + 3) * 2048);
          SB();
          xn = mfma(wa[i & 3], xf[i], xn);
          SB();
          if (i == 15 - LAG) {
#pragma unroll
            for (int g = 0; g < 4; ++g) *(LAS f32x4*)(ex + w * 4096 + g * 1024) = (f32x4){xo[4 * g], xo[4 * g + 1], xo[4 * g + 2], xo[4 * g + 3]};
          }
          if (i == 11) {
            bar();
#pragma unroll
            for (int g = 0; g < 4; ++g) t[g] = *(LAS f32x4*)(ex + (w ^ 1) * 4096 + g * 1024);
          }
          SB();
        }
#pragma unroll
        for (int g = 0; g < 4; ++g) {
#pragma unroll
          for (int j = 0; j < 4; ++j) xs[4 * g + j] = xn[4 * g + j] + t[g][j];
        }
      }
      STAMPX(pb + 8);
      if (layer == NL - 1) {
        const rsrc_t rOUT = __builtin_amdgcn_make_buffer_rsrc((void*)out, 0, SEQ * DM * 2, 0x00020000);
        const u32x4 f0 = pack8<0>(xs), f1 = pack8<1>(xs);
        int ln = lane; PIN(ln);
        const int row = (ln & 31) * 896, up = ln >> 5;
        const int ob = (tb * 4 + tx) * 32 * 896 + 7 * pair * 16;
        st128<0>(f0, rOUT, row + up * 16, ob + 5 * kh * 16);
        st128<0>(f1, rOUT, row + up * OOB, ob + (2 + 2 * kh) * 16);
        __builtin_amdgcn_raw_buffer_store_b64(lo2(f1), rOUT, row + (1 - up) * OOB, ob + 3 * 16 + kh * 8, 0);
      } else {
        const u32x4 f0 = pack8<0>(xs);
        store_x0(f0, WS_XO + XBUF(layer) * XO_B);
        u32x4 f1 = pack8<1>(xs);
        f1[2] &= lomask; f1[3] &= lomask;
        float s1 = 0.f, s2 = 0.f, t1 = 0.f, t2 = 0.f;
#pragma unroll
        for (int j = 0; j < 4; ++j) { const unsigned u = f0[j], v = f1[j]; s1 = dot2(u, ONES, s1); s2 = dot2(u, u, s2); t1 = dot2(v, ONES, t1); t2 = dot2(v, v, t2); }
        s1 += t1; s2 += t2;
        red2<false>(s1, s2);
        f1[2] |= ~lomask & F2U(s1); f1[3] |= ~lomask & F2U(s2);
        store_x1(f1, WS_XO + XBUF(layer) * XO_B);
        CALS(3 * layer + 2);
        STAMPX(pb + 9);
      }
      asm volatile("" : "=v"(ak), "=v"(av), "=v"(rstd), "=v"(mf));
#pragma unroll
      for (int i = 14; i < 28; ++i) asm volatile("" : "=v"(xf[i]));
    }
    STAMP(61); RSTAMP(63);
    if (w == 0) while (__builtin_amdgcn_s_memrealtime() - tstart < 6000 + probe) __builtin_amdgcn_s_sleep(4);
    if (b == 0 && w == 0) __builtin_amdgcn_raw_buffer_store_b64((u32x2){(unsigned)(epoch + 1), (unsigned)((epoch + 1) >> 32)}, rWS, WS_EPOCH, 0, SC1);
  } else {
    STAMPX(53);
    u32x4 pA[21], pB[21];
    unsigned tch0, tch1;
    asm volatile("s_load_dword %0, %2, 0x0\n\ts_load_dword %1, %3, 0x0" : "=&s"(tch0), "=&s"(tch1)
                 : "s"(w == 0 ? (const void*)ws : w == 1 ? (const void*)wqkvB : w == 2 ? (const void*)woA : (const void*)wphi), "s"(w < 2 ? (const void*)woB : (const void*)out));
    dma_x0(0, 4);
    unsigned warm0;
    {
      u64 pc; asm volatile("s_getpc_b64 %0" : "=s"(pc));
      const rsrc_t rs = __builtin_amdgcn_make_buffer_rsrc(w == 3 ? (void*)(pc & ~127ull) : (void*)wqkvA, 0, w == 3 ? CODE_B : DM * 3 * DM * 2, 0x00020000);
      const int n = w == 3 ? 9 : 13;
      warm0 = __builtin_amdgcn_raw_buffer_load_b32(rs, lane < n ? (lane + (w == 3 ? (b >> 3) : b * 3 + w) * n) * 128 : OOB, 0, 0);
      SB();
    }
#pragma unroll
    for (int i = 0; i < 7; ++i) { mkrow(i); PIN(krow[i]); }
    lptr trb = make_trb(); PIN(trb);
    u32x4 id0 = ident_frag(0, r, hh); PIN(id0);
    SB();
    dma_x0(4, 28);
#pragma unroll
    for (int i = 0; i < NV0; ++i) pA[7 + i] = ldw(rWA, i, 2688, 896);
    STAMPX(54);
    asm volatile("s_waitcnt vmcnt(%2) lgkmcnt(0)" :: "s"(tch0), "s"(tch1), "n"(NV0) : "memory");
    const u32x4 sv = {SENT, SENT, SENT, SENT};
    auto clean_kv = [&](int layer) { if (layer < NL) st128<SC1>(sv, rWS, lane * 16, WS_MID + (h * 4 + MBUF(layer)) * MID_B + (tb * 4 + w) * 1024); };
    auto clean_o = [&](int layer) { if (layer < NL) store_o(sv, sv, WS_XO + OBUF(layer) * XO_B); };
    auto clean_x = [&](int layer) { if (layer < NL - 1) { store_x0(sv, WS_XO + XBUF(layer) * XO_B); store_x1(sv, WS_XO + XBUF(layer) * XO_B); } };
    unsigned r0 = (unsigned)__builtin_amdgcn_s_memrealtime(), c0 = (unsigned)__builtin_amdgcn_s_memtime(), tann = 0;
    auto mark = [&](int cyc) {
      const unsigned r1 = (unsigned)__builtin_amdgcn_s_memrealtime(), c1 = (unsigned)__builtin_amdgcn_s_memtime();
      tann = r1 + LAND + (unsigned)((float)cyc * (float)(r1 - r0) * __builtin_amdgcn_rcpf((float)(c1 - c0)));
      r0 = r1; c0 = c1;
    };
    auto push = [&](int hop, unsigned voff) {
      CALA(hop);
      __builtin_amdgcn_raw_buffer_store_b64((u32x2){tag[0], (tag[1] & 0xfff00000u) | (tann & 0xfffffu)}, rWS, voff, WS_FLAGS + hop * HOP_B, SC1);
    };
    auto push_grp = [&](int hop) { push(hop, lane < 16 ? (tb * 16 + lane) * 128 + h * 8 : OOB); };
    auto push_mid = [&](int layer) {
      if (layer & 1) push(3 * layer, (lane < 2 && tb + 1 + lane < 16) ? ((tb + 1 + lane) * 16 + h) * 128 + lane * 8 : OOB);
      else push(3 * layer, lane < 16 ? (lane * 16 + h) * 128 + tb * 8 : OOB);
    };
    const rsrc_t rWB = __builtin_amdgcn_make_buffer_rsrc((void*)wqkvB, 0, DM * 3 * DM * 2, 0x00020000);
    auto spin = [&](int k, unsigned n) { while (__builtin_amdgcn_readfirstlane(cnt[k]) < n) __builtin_amdgcn_s_sleep(1); asm volatile("" ::: "memory"); };
    auto wait_h = [&](int hop, int mask) {
      if (w == 0) { if (mask >= 0) poll(hop, mask); cnt[1] = hop + 1; }
      else while (__builtin_amdgcn_readfirstlane(cnt[1]) != hop + 1) __builtin_amdgcn_s_sleep(1);
      asm volatile("" ::: "memory");
    };
    const unsigned pkb = WS_PK + h * 262144;
    auto ldp = [&](unsigned off) { const u32x4 v = __builtin_amdgcn_raw_buffer_load_b128(rWS, lane * 16, pkb + off + w * 1024, SC1); SB(); return v; };
    STAMPX(55);
    bar();
#pragma unroll
    for (int i = NV0; i < 7 + NV1; ++i) pA[7 + i] = ldw(rWA, i % 7, 2688, 896 + 896 * (i / 7));
    put_slab(pA + 7, 0, 0);
#pragma unroll
    for (int i = NV1; i < 7; ++i) pA[14 + i] = ldw(rWA, i, 2688, 1792);
    read_frags(pA + 7, 0, trb); sum_frags(pA + 7, 0, 0);
    __builtin_amdgcn_s_setprio(3);
    STAMPX(57);
    bar();
    load_epoch();
    const unsigned warm1 = __builtin_amdgcn_raw_buffer_load_b32(rWS, (FW && w == 0 && lane < 11) ? lane * HOP_B : OOB, WS_FLAGS + me * 128, SC1);
    SB();
    u32x4 pw[4];
    {
      const unsigned act = tb < 14 ? 0u : OOB;
#pragma unroll
      for (int j = 0; j < 2; ++j) {
        const int st = 2 * tb + j, p = (st * 43) >> 7, m = st - 3 * p;
        const int c = st < 24 ? 7 * p + ((0x520 >> (4 * m)) & 15) + hh * (1 + (m & 1)) : 7 * (2 * (st - 24) + hh) + 3;
        pw[j] = __builtin_amdgcn_raw_buffer_load_b128(rWB, ((8 * c + ((lane >> 2) & 7)) * 2688 + sub * 16) | (w < 3 ? act : OOB), col + (w == 2 ? 0 : 896 + 896 * w), 0);
        SB();
      }
      {
        const int n = 2 * tb + (w & 1), k2 = n >= 14, i = n - 14 * k2, st = i - 2, p = (st * 43) >> 7, m = st - 3 * p;
        const int c = 28 * k2 + (i < 2 ? 14 * i + 3 : 7 * p + ((0x520 >> (4 * m)) & 15)), dl = i < 2 ? 7 : 1 + (m & 1);
        const unsigned vo = (c * 7168 + (lane >> 3) * 896 + 112 * pair + (((lane & 7) ^ ((lane >> 2) & 4)) * 16)) | act;
        const rsrc_t rs = __builtin_amdgcn_make_buffer_rsrc(w >> 1 ? (void*)woB : (void*)woA, 0, DM * DM * 2, 0x00020000);
        pw[2] = __builtin_amdgcn_raw_buffer_load_b128(rs, vo, 0, 0); SB();
        pw[3] = __builtin_amdgcn_raw_buffer_load_b128(rs, vo + dl * 7168, 0, 0); SB();
      }
    }
    put_slab(pA + 14, 0, 1);
#pragma unroll
    for (int j = 0; j < NQ0; ++j) pA[j] = ldw(rWA, j, 2688, 0);
    read_frags(pA + 14, 1, trb); sum_frags(pA + 14, 0, 1);
    STAMPX(58);
    bar();
    if (w == 3) mark(TKA0);
    __builtin_amdgcn_s_setprio(0);
#pragma unroll
    for (int j = NQ0; j < 7; ++j) pA[j] = ldw(rWA, j, 2688, 0);
    {
      patch(pw, 2, false);
      const unsigned act = (tb < 14 ? 0u : OOB) | (lane * 16);
      const lptr sc = lds + L_WO + w * 4096;
#pragma unroll
      for (int j = 0; j < 4; ++j) *(LAS u32x4*)(sc + j * 1024 + lane * 16) = pw[j];
      const int q = (lane >> 2) & 3, rp = ((lane >> 2) & 4) + (lane & 3), lam = (rp & 4) | ((rp & 1) << 1) | ((rp >> 1) & 1);
      auto tr128 = [&](int pc8) {
        const lptr p = sc + 2048 + (8 * hh + q) * 128 + ((pc8 * 8) ^ ((q >> 1) * 64));
        bf16x4 a0 = __builtin_amdgcn_ds_read_tr16_b64_v4bf16((LAS bf16x4*)p), a1 = __builtin_amdgcn_ds_read_tr16_b64_v4bf16((LAS bf16x4*)(p + 512));
        return __builtin_bit_cast(u32x4, __builtin_shufflevector(a0, a1, 0, 1, 2, 3, 4, 5, 6, 7));
      };
      pw[0] = trfrag(trb + L_WO + w * 4096); pw[1] = trfrag(trb + L_WO + w * 4096 + 1024);
      pw[2] = tr128(lam); pw[3] = tr128(6 + ((0x01327654 >> (4 * lam)) & 7));
#pragma unroll
      for (int j = 0; j < 2; ++j) st128<SC1>(pw[j], rWS, w < 3 ? act : OOB, pkb + w * SLAB + (2 * tb + j) * 1024);
#pragma unroll
      for (int j = 0; j < 2; ++j) st128<SC1>(pw[2 + j], rWS, act, pkb + (3 + (w & 2)) * SLAB + (2 * tb + (w & 1)) * 2048 + j * 1024);
    }
    clean_kv(0); clean_o(0);
    make_tag();
    STAMPX(59);
    put_slab(pA, 0, 2);
    drain();
    arrive(3);
    if (w == 3) { spin(3, 4); push_mid(0); }
    clean_x(0);
    read_frags(pA, 2, trb); write_slab(pA, L_WQ); sum_frags(pA, 0, 2);
    arrive(6);
    asm volatile("" :: "v"(warm0), "v"(warm1));
    __builtin_amdgcn_s_setprio(3);
    u32x4 po[14];
    auto swap_wo = [&](bool keep) {
      lptr d = lds + L_WO + ht * 16; PIN(d);
#pragma unroll
      for (int i = 0; i < 14; ++i) { u32x4 t; if (keep) t = ldl(d + i * 4096); *(LAS u32x4*)(d + i * 4096) = po[i]; if (keep) po[i] = t; SB(); }
    };
    STAMPX(60);
#pragma unroll
    for (int layer = 0; layer < NL; ++layer) {
      const int type = layer & 1;
      const int pb = 2 + layer * 14;
      const unsigned mid = WS_MID + (h * 4 + MBUF(layer)) * MID_B;
      if (type == 0) {
        STAMPX(pb + 0);
        STAMPX(pb + 1);
        if (layer > 0) arrive(3);
        if (w == 0) {
          while (!__all(cnt[12 + (lane & 3)] == layer + 1u)) __builtin_amdgcn_s_sleep(1);
          if (layer > 0) spin(3, 8);
          poll(3 * layer, 15, FLOORK);
        }
        wait_h(3 * layer, -1);
        STAMPX(pb + 2);
        f32x16 sum;
        for (;;) {
          sum = Z16;
          unsigned m = 0;
          u32x4 pr[16];
          const unsigned g = mid + w * 1024;
#pragma unroll
          for (int i = 0; i < 16; ++i) {
            pr[i] = __builtin_amdgcn_raw_buffer_load_b128(rWS, lane * 16, g + i * 4096, SC1); SB();
            if (i < 15) PACED();
          }
          if (w == 3) { mark(layer ? TOA : TOA0); push_grp(3 * layer + 1); SB(); }
#pragma unroll
          for (int i = 0; i < 16; ++i) {
            if (layer == 0 && i < NPO) po[i] = ldp(3 * SLAB + i * 4096);
            sum = mfma(id0, pr[i], sum); m = max4(m, pr[i]); PIN(m); SB();
          }
          if (!__any(m == SENT) || budget == 0) break;
          --budget; RET(layer * 3 + 1);
          asm volatile("" ::: "memory");
        }
        *(LAS u32x4*)(lds + L_KVF + w * 1024 + lane * 16) = pack8<0>(sum);
        STAMPX(pb + 3);
        bar();
        if (layer == 0) {
#pragma unroll
          for (int i = NPO; i < 14; ++i) po[i] = ldp(3 * SLAB + i * 4096);
        } else __builtin_amdgcn_s_sleep(5);
      } else {
        gather_all(layer, true);
        STAMPX(pb + 0);
        u32x4 t[8];
        wait_h(3 * layer, tb >= 2 ? 1 : tb - 1);
        STAMPX(pb + 2);
        if (tb - 2 + (w >> 1) >= 0) {
          const unsigned g = mid + (tb * 4 - 8 + 2 * w) * 4096;
          for (;;) {
            unsigned c[2] = {0u, 0u};
#pragma unroll
            for (int i = 0; i < 8; ++i) { t[i] = __builtin_amdgcn_raw_buffer_load_b128(rWS, lane * 16 + (i & 3) * 1024, g + (i >> 2) * 4096, SC1); SB(); if (i < 7) PACED(); }
#pragma unroll
            for (int i = 0; i < 8; ++i) {
              c[i >> 2] = add4(c[i >> 2], t[i]); PIN(c[i >> 2]);
              if ((i & 3) == 1) t[i][3] &= (unsigned)hh - 1u;
              *(LAS u32x4*)(lds + L_SCR + 2 * w * 4096 + i * 1024 + lane * 16) = t[i];
              SB();
            }
            const auto cs = __builtin_amdgcn_permlane32_swap(c[0], c[1], false, false);
            if (__all(cs[0] + cs[1] == tag[0] + layer) || budget == 0) break;
            --budget; RET(layer * 3 + 1);
            __builtin_amdgcn_s_sleep(8);
            asm volatile("" ::: "memory");
          }
        } else {
#pragma unroll
          for (int i = 0; i < 8; ++i)
            *(LAS u32x4*)(lds + L_SCR + 2 * w * 4096 + i * 1024 + lane * 16) = (u32x4){0u, 0u, ((i & 3) == 1 && hh) ? 0xff80u : 0u, 0u};
        }
        drain();
        STAMPX(pb + 3);
        bar();
        if (w == 3) { mark(layer == 1 ? TOB1 : TOB); push_grp(3 * layer + 1); }
        __builtin_amdgcn_s_setprio(0);
        if (layer == 1) {
          clean_x(1); clean_kv(2);
#pragma unroll
          for (int i = 0; i < 14; ++i) po[i] = ldp(5 * SLAB + i * 4096);
          swap_wo(true);
          drain();
          spin(4, 4);
        }
      }
      __builtin_amdgcn_s_setprio(0);
      if (layer == 0) {
        swap_wo(false);
      } else if (layer < NL - 1) {
        write_slab((type == 0 ? pB : pA) + 7, L_WK);
      }
      STAMPX(pb + 4);
      bar();
      if (w == 3 && layer < NL - 1) { mark(layer == 2 ? TX2 : layer ? TX : TX0); push_grp(3 * layer + 2); }
      STAMPX(pb + 5);
      if (layer > 0 && layer < NL - 1) { __builtin_amdgcn_s_sleep(WSL); write_slab((type == 0 ? pB : pA) + 14, L_WV); }
      if (layer == 0) {
        __builtin_amdgcn_s_sleep(12);
#pragma unroll
        for (int i = 0; i < 21; ++i) pB[(i + 7) % 21] = ldp(i * 4096);
      }
      bar();
      if (layer < NL - 1) {
        __builtin_amdgcn_s_sleep(SWS);
        if (layer == 0) { write_slab(pB + 7, L_WK); write_slab(pB + 14, L_WV); sum_frags(pB + 7, 1, 0); sum_frags(pB + 14, 1, 1); }
        else swap_wo(layer == 1);
        bar();
        __builtin_amdgcn_s_sleep(WSL);
        write_slab(type == 0 ? pB : pA, L_WQ);
        if (layer == 0) sum_frags(pB, 1, 2);
        arrive(0);
        while (!__all(cnt[8 + (lane & 3)] == layer + 1u)) __builtin_amdgcn_s_sleep(1);
        if (w == 3) { mark(layer == 0 ? TKB1 : layer == 1 ? TKA : TKB); push_mid(layer + 1); }
        __builtin_amdgcn_s_sleep(CSL);
        clean_o(layer + 1);
        if (layer == 1) { clean_x(2); drain(); }
        __builtin_amdgcn_s_setprio(3);
      }
    }
  }
}
}

extern "C" void kernel_launch(void* const* d_in, const int* in_sizes, int n_in, void* d_out, int out_size, void* d_ws, size_t ws_size, hipStream_t stream) {
  (void)hipFuncSetAttribute((const void*)fused_kernel, hipFuncAttributeMaxDynamicSharedMemorySize, L_END);
  fused_kernel<<<256, 512, L_END, stream>>>((const u16*)d_in[0], (const u16*)d_in[1], (const u16*)d_in[2], (const u16*)d_in[3],
                                            (const u16*)d_in[5], (const u16*)d_in[6], (u16*)d_out, (unsigned char*)d_ws);
}
```

```cpp
#define NL 4
#include <hip/hip_runtime.h>
#include <hip/hip_bf16.h>

typedef unsigned short u16;
typedef unsigned long long u64;
typedef __bf16 bf16x8 __attribute__((ext_vector_type(8)));
typedef __bf16 bf16x4 __attribute__((ext_vector_type(4)));
typedef __bf16 bf16x2 __attribute__((ext_vector_type(2)));
typedef float f32x16 __attribute__((ext_vector_type(16)));
typedef float f32x4 __attribute__((ext_vector_type(4)));
typedef float f32x2 __attribute__((ext_vector_type(2)));
typedef unsigned u32x4 __attribute__((ext_vector_type(4)));
typedef unsigned u32x2 __attribute__((ext_vector_type(2)));
#define LAS __attribute__((address_space(3)))
#define GAS __attribute__((address_space(1)))
#define DI __device__ __forceinline__
#define SB() __builtin_amdgcn_sched_barrier(0)
#define PIN(x) asm volatile("" : "+v"(x))
typedef __amdgpu_buffer_rsrc_t rsrc_t;
typedef LAS unsigned char* lptr;

namespace {
constexpr int SEQ = 2048, DM = 448;
constexpr int SC1 = 16;
constexpr unsigned WS_EPOCH = 0;
constexpr unsigned WS_FLAGS = 4096;
constexpr unsigned HOP_B = 256 * 128;
constexpr unsigned TILE_B = 32768;
constexpr unsigned XO_B = 4 * TILE_B;
constexpr unsigned WS_XO = 1u << 20;
constexpr unsigned MID_B = 64 * 4096;
constexpr unsigned WS_MID = WS_XO + 16 * 8 * XO_B;
constexpr unsigned WS_PK = WS_MID + 64 * MID_B;
constexpr unsigned WS_PROF = WS_PK + 16 * 262144;
constexpr unsigned WS_END = WS_PROF + 256 * 2048;
constexpr unsigned SLAB = 448 * 64;
constexpr unsigned L_WK = 0, L_WV = SLAB, L_WQ = 2 * SLAB, L_WO = 3 * SLAB;
constexpr unsigned L_SCR = 0;
constexpr unsigned L_X0 = SLAB;
constexpr unsigned L_KVF = 5 * SLAB;
constexpr unsigned L_MK = L_KVF + 4096;
constexpr unsigned L_WPH = L_MK + 2048;
constexpr unsigned L_CS = L_WPH + 2048;
constexpr unsigned L_SYNC = L_CS + 6144;
constexpr unsigned L_PROF = L_SYNC + 64;
constexpr unsigned L_END = L_PROF + 2048;
static_assert(L_END <= 163840 && L_X0 + 4 * SLAB <= L_KVF, "lds");
constexpr unsigned SPIN_CAP = 1u << 15;
constexpr unsigned SENT = 0xffffffffu;
constexpr unsigned OOB = 0x7ff00000u;
constexpr unsigned ONES = 0x3f803f80u;
constexpr unsigned CODE_B = 274 * 128;
constexpr float L2E = 1.4426950408889634f;

DI f32x16 mfma(u32x4 a, u32x4 b, f32x16 c) {
  return __builtin_amdgcn_mfma_f32_32x32x16_bf16(__builtin_bit_cast(bf16x8, a), __builtin_bit_cast(bf16x8, b), c, 0, 0, 0);
}
DI unsigned pk2(float a, float b) { f32x2 t = {a, b}; return __builtin_bit_cast(unsigned, __builtin_convertvector(t, bf16x2)); }
template <int S_> DI u32x4 pack8(const f32x16& c) {
  return (u32x4){pk2(c[8 * S_], c[8 * S_ + 1]), pk2(c[8 * S_ + 2], c[8 * S_ + 3]), pk2(c[8 * S_ + 4], c[8 * S_ + 5]), pk2(c[8 * S_ + 6], c[8 * S_ + 7])};
}
DI u32x4 cat(u32x2 a, u32x2 b) { return (u32x4){a[0], a[1], b[0], b[1]}; }
DI u32x2 lo2(u32x4 v) { return (u32x2){v[0], v[1]}; }
DI unsigned umax3(unsigned a, unsigned b, unsigned c) { return max(max(a, b), c); }
DI unsigned max4(unsigned m, u32x4 v) { return umax3(umax3(m, v[0], v[1]), v[2], v[3]); }
DI unsigned add4(unsigned m, u32x4 v) { return (m + v[0] + v[1]) + v[2] + v[3]; }
DI unsigned xhalf_addu(unsigned v) { auto r = __builtin_amdgcn_permlane32_swap(v, v, false, false); return r[0] + r[1]; }
DI float fmax3(float a, float b, float c) { return fmaxf(fmaxf(a, b), c); }
template <int CTRL> DI float dppadd(float v) { return v + __builtin_bit_cast(float, __builtin_amdgcn_update_dpp(0, __builtin_bit_cast(int, v), CTRL, 0xf, 0xf, true)); }
DI float fmin3(float a, float b, float c) { return fminf(fminf(a, b), c); }
DI f32x16 splat(float v) { f32x16 z; _Pragma("unroll") for (int i = 0; i < 16; ++i) z[i] = v; return z; }
#define Z16 splat(0.f)
DI u32x4 trfrag(lptr p) {
  bf16x4 a0 = __builtin_amdgcn_ds_read_tr16_b64_v4bf16((LAS bf16x4*)p);
  bf16x4 a1 = __builtin_amdgcn_ds_read_tr16_b64_v4bf16((LAS bf16x4*)(p + 256));
  return __builtin_bit_cast(u32x4, __builtin_shufflevector(a0, a1, 0, 1, 2, 3, 4, 5, 6, 7));
}
DI u32x4 ldl(lptr p) { return *(LAS u32x4*)p; }
DI float U2F(unsigned u) { return __uint_as_float(u); }
DI unsigned F2U(float f) { return __float_as_uint(f); }
template <bool MX> DI void red2(float& a, float& b) {
  auto r = __builtin_amdgcn_permlane32_swap(F2U(a), F2U(b), false, false);
  const float t = MX ? fmaxf(U2F(r[0]), U2F(r[1])) : U2F(r[0]) + U2F(r[1]);
  r = __builtin_amdgcn_permlane32_swap(F2U(t), F2U(t), false, false);
  a = U2F(r[0]); b = U2F(r[1]);
}
DI float xhalf_max(float v) { auto r = __builtin_amdgcn_permlane32_swap(F2U(v), F2U(v), false, false); return fmaxf(U2F(r[0]), U2F(r[1])); }
DI float upper_val(float v) { auto r = __builtin_amdgcn_permlane32_swap(F2U(v), F2U(v), false, false); return U2F(r[1]); }
DI float dot2(unsigned u, unsigned v, float c) { return __builtin_amdgcn_fdot2_f32_bf16(__builtin_bit_cast(bf16x2, u), __builtin_bit_cast(bf16x2, v), c, false); }
DI float bflo(unsigned u) { return U2F(u << 16); }
DI float bfhi(unsigned u) { return U2F(u & 0xffff0000u); }
DI void split2(float v, unsigned& hi, unsigned& lo) { hi = pk2(v, v) & 0xffffu; lo = pk2(v - bflo(hi), 0.f) & 0xffffu; }
template <int AUX> DI void st128(u32x4 v, rsrc_t rs, unsigned voff, unsigned soff) {
  SB();
  __builtin_amdgcn_raw_buffer_store_b128(v, rs, voff, soff, AUX);
  SB();
  asm volatile("s_nop 1" :: "v"(v));
  SB();
}
DI void drain() { asm volatile("s_waitcnt vmcnt(0)" ::: "memory"); }
DI void bar() { SB(); asm volatile("s_waitcnt lgkmcnt(0)\n\ts_barrier" ::: "memory"); SB(); }
constexpr int chunk_of(int s, int hh) { return s < 24 ? 7 * (s / 3) + (s % 3 == 0 ? hh : (s % 3 == 1 ? 2 + 2 * hh : 5 + hh)) : 7 * (2 * (s - 24) + hh) + 3; }
constexpr int ord(int i) { return i < 4 ? 24 + i : i - 4; }

#define STAMP(i) do { } while (0)
#define RET(i) do { } while (0)
#define RSTAMP(i) do { } while (0)
#define STAMPX(i) STAMP(i)
#define CALS(hop) do { } while (0)
#define CALA(hop) do { } while (0)
#define TOT0
#define TOT1
#define DUMP(buf, acc, part, sc) do { } while (0)

template <typename F> DI void dual_softmax(const f32x16& p, float c, float wtail, u32x4 (&fp)[2], u32x4 (&fm)[2], F mid) {
  float mx = fmax3(p[0], p[1], p[2]), mn = fmax3(-p[0], -p[1], -p[2]);
#pragma unroll
  for (int i = 3; i < 15; i += 2) { mx = fmax3(mx, p[i], p[i + 1]); mn = fmax3(mn, -p[i], -p[i + 1]); }
  mx = fmax3(mx, p[15], p[15]); mn = fmax3(mn, -p[15], -p[15]);
  red2<true>(mx, mn);
  const float a = -c * mx, b = -c * mn;
  float nc = -c; PIN(nc);
  f32x16 ep = p * c + a, em = p * nc + b;
  SB();
#pragma unroll
  for (int i = 0; i < 16; ++i) ep[i] = __builtin_amdgcn_exp2f(ep[i]);
  PIN(ep);
  mid();
#pragma unroll
  for (int i = 0; i < 16; ++i) em[i] = __builtin_amdgcn_exp2f(em[i]);
  f32x2 sp = {ep[0], ep[1]}, sm = {em[0], em[1]};
#pragma unroll
  for (int i = 2; i < 12; i += 2) { sp += (f32x2){ep[i], ep[i + 1]}; sm += (f32x2){em[i], em[i + 1]}; }
  sp += wtail * ((f32x2){ep[12], ep[13]} + (f32x2){ep[14], ep[15]});
  sm += wtail * ((f32x2){em[12], em[13]} + (f32x2){em[14], em[15]});
  float u = sp[0] + sp[1], v = sm[0] + sm[1];
  red2<false>(u, v);
  ep *= __builtin_amdgcn_rcpf(u); em *= __builtin_amdgcn_rcpf(v);
  fp[0] = pack8<0>(ep); fp[1] = pack8<1>(ep); fm[0] = pack8<0>(em); fm[1] = pack8<1>(em);
}
DI u32x4 ident_frag(int s, int r, int hh) {
  const unsigned val = 0x3f80u << (16 * (r & 1));
  const int idx = (((r >> 2) ^ hh) & 1) * 8 + (r >> 4) * 4 + ((r >> 2) & 2) + ((r >> 1) & 1) - 4 * s;
  return (u32x4){idx == 0 ? val : 0u, idx == 1 ? val : 0u, idx == 2 ? val : 0u, idx == 3 ? val : 0u};
}
#define CHK1 3
#define CHK2 12
template <typename S, typename A> DI void steps28(S step, A abort) {
#pragma unroll
  for (int i = 0; i <= CHK1; ++i) step(i);
  if (abort()) return;
#pragma unroll
  for (int i = CHK1 + 1; i < 28; ++i) step(i);
}
template <int PD, bool TR, typename F, typename G, typename A> DI void gemm1(f32x16& acc, lptr wb, const u32x4 (&xf)[28], F pre, G post, A abort) {
  auto frag = [](lptr p) { return TR ? trfrag(p) : ldl(p); };
  u32x4 wr[PD];
#pragma unroll
  for (int i = 0; i < PD - 1; ++i) wr[i] = frag(wb + ord(i) * 1024);
  SB();
  steps28([&](int i) __attribute__((always_inline)) {
    pre(i);
    if (i + PD - 1 < 28) wr[(i + PD - 1) % PD] = frag(wb + ord(i + PD - 1) * 1024);
    SB();
    acc = mfma(wr[i % PD], xf[i], acc);
    SB();
    post(i);
    SB();
  }, abort);
}
template <typename H, typename F, typename G, typename A>
DI void gemm2(f32x16& ak, f32x16& av, lptr bk, const u32x4 (&xf)[28], H head, F pre, G post, A abort) {
  u32x4 wk[3], wv[3];
  head([&](int u) {
    if (u == 0) wv[0] = ldl(bk + SLAB + ord(0) * 1024);
    if (u == 1) wk[0] = ldl(bk + ord(0) * 1024);
    if (u == 2) wv[1] = ldl(bk + SLAB + ord(1) * 1024);
    if (u == 3) wk[1] = ldl(bk + ord(1) * 1024);
    SB();
  });
  steps28([&](int i) __attribute__((always_inline)) {
    pre(i);
    if (i + 2 < 28) wv[(i + 2) % 3] = ldl(bk + SLAB + ord(i + 2) * 1024);
    SB();
    av = mfma(wv[i % 3], xf[i], av);
    SB();
    if (i + 2 < 28) wk[(i + 2) % 3] = ldl(bk + ord(i + 2) * 1024);
    post(i, 0);
    SB();
    ak = mfma(wk[i % 3], xf[i], ak);
    SB();
    post(i, 1);
    SB();
  }, abort);
}
#define NOAB [] { return false; }
#define EARLYAB [&] { return __builtin_expect(__any(m == sent), 0); }
#define LA 12
#define XH 27
#define VSLEEP 8
#define QSLEEP 28
#define LAO 8
#define XBAR 26
#define DOA 3
#define DOB 6
#define DX 3
#define KVI 6
#define KVB 3
#define NKB 5
#define XM 4
#ifndef NV0
#define NV0 5
#endif
#define NK2 8
#define NK3 9
#define PACE 8
#define LAG 6
#define PACED() do { if (PACE) asm volatile("s_nop %0" :: "n"(PACE)); SB(); } while (0)
#define PSLP 4
#define RDLY 0
#define KDLY 0
#define LAND 10
#define TKA0 4500
#define TKA 3500
#define TKB1 2150
#define TKB 2400
#define TOA0 2050
#define TOA 1850
#define TOB1 2750
#define TOB 2520
#define TX0 3800
#define TX 2850
#define BACK 8
#define NQ0 2
#define NQB 0
#define TX2 3200
#define ND0 10
#define NPO 6
#define SWS 6
#define LMAX 1.8446744e19f
#define CSL 2
#define WSL 4
#define PACE2() do { asm volatile("s_nop %0" :: "n"(PACE - 2)); SB(); } while (0)
#define NQ1 4
#define REUSE 4
#define OBUF(l) ((REUSE & 1) ? 0 : 2 * (l))
#define XBUF(l) ((REUSE & 2) ? 1 + 2 * ((l) & 1) : 2 * (l) + 1)
#define MBUF(l) (((l) & 1) ? ((REUSE & 4) ? 1 : (l)) : ((REUSE & 8) ? 0 : (l)))
#define FW 1
#define FLOORK 45
#ifndef NV1
#define NV1 3
#endif
#define FLOORK0 10
#define XE 12
#define PDELAY 14

__global__ void __launch_bounds__(512, 2) fused_kernel(const u16* __restrict__ x0, const u16* __restrict__ wqkvA, const u16* __restrict__ wphi,
                                                       const u16* __restrict__ woA, const u16* __restrict__ wqkvB, const u16* __restrict__ woB,
                                                       u16* __restrict__ out, unsigned char* ws) {
  extern __shared__ __attribute__((aligned(16))) unsigned char smem[];
  const lptr lds = (lptr)smem;
  const int tid = threadIdx.x, lane = tid & 63, wave = __builtin_amdgcn_readfirstlane(tid >> 6);
  TOT0
  const int w = wave & 3;
  const int r = lane & 31, hh = lane >> 5;
  const int b = blockIdx.x;
  const int tb = ((b >> 1) & 3) * 4 + (b >> 6), h = (b & 1) * 8 + ((b >> 3) & 7);
  const int me = tb * 16 + h;
  const int odd = h & 1, pair = h >> 1;
  const int T = tb * 4 + w;
  auto dma_x0 = [&](int i0, int i1, int line) __attribute__((always_inline)) {
    const rsrc_t rX0 = __builtin_amdgcn_make_buffer_rsrc((void*)x0, 0, SEQ * DM * 2, 0x00020000);
    const int o0 = (lane >> 3) * 896 + (((lane & 7) ^ (lane >> 4)) * 16), o1 = o0 ^ 64;
#pragma unroll
    for (int i = i0; i < i1; ++i) {
      __builtin_amdgcn_raw_ptr_buffer_load_lds(rX0, (LAS void*)(lds + L_X0 + w * SLAB + i * 1024 + line * 4096), 16, (i & 1) ? o1 : o0, (T * 32 + (i & 3) * 8) * 896 + ((i >> 2) + line) * 128, 0, 0);
      SB();
    }
  };
  asm volatile("" :: "s"(x0), "s"(wqkvA), "s"(wphi), "s"(woA), "s"(wqkvB), "s"(woB), "s"(out), "s"(ws));
  dma_x0(0, 4, wave >> 2);
  const unsigned TOFF = tb * 8 * XO_B + w * TILE_B;
  const int kh = w & 1, tx = 2 * odd + (w >> 1);
  const unsigned XOFF = tb * 8 * XO_B + tx * TILE_B;
  const rsrc_t rWS = __builtin_amdgcn_make_buffer_rsrc((void*)ws, 0, WS_END, 0x00020000);
  unsigned budget = SPIN_CAP;
  u64 epoch;
  u32x2 tag;
  u32x2 ev;
  auto load_epoch = [&]() { ev = __builtin_amdgcn_raw_buffer_load_b64(rWS, 0, WS_EPOCH, SC1); SB(); };
  auto make_tag = [&]() {
    epoch = (u64)(unsigned)__builtin_amdgcn_readfirstlane(ev[0]) | ((u64)(unsigned)__builtin_amdgcn_readfirstlane(ev[1]) << 32);
    u64 z = epoch + 0x9E3779B97F4A7C15ull;
    z = (z ^ (z >> 30)) * 0xBF58476D1CE4E5B9ull; z = (z ^ (z >> 27)) * 0x94D049BB133111EBull; z ^= z >> 31;
    unsigned t0 = (unsigned)z, t1 = (unsigned)(z >> 32);
    asm volatile("" : "+s"(t0), "+s"(t1));
    tag = (u32x2){t0, t1};
  };
  volatile LAS unsigned* const cnt = (volatile LAS unsigned*)(lds + L_SYNC);
  auto arrive = [&](int k) { if (lane == 0) asm volatile("ds_add_u32 %0, %1" :: "v"(lds + L_SYNC + 4 * k), "v"(1u) : "memory"); };
  auto gather_all = [&](int layer, bool nap) {
    while (__builtin_amdgcn_readfirstlane(*cnt) < 8u * layer + 4u) { if (nap) __builtin_amdgcn_s_sleep(2); }
    asm volatile("" ::: "memory");
  };
  auto look = [&](int hop, int mask, bool on) {
    int ln = lane & mask; PIN(ln);
    const u32x2 v = __builtin_amdgcn_raw_buffer_load_b64(rWS, on ? ln * 8 : OOB, WS_FLAGS + hop * HOP_B + me * 128, SC1);
    SB();
    return v;
  };
  auto settle = [&](int hop, int mask, u32x2 v, int floor) {
    const unsigned t0 = (unsigned)__builtin_amdgcn_s_memrealtime() + floor;
    for (int n = 0; !__all(v[0] == tag[0] && ((v[1] ^ tag[1]) >> 20) == 0) && budget > 1; ++n) {
      --budget;
      if (n) __builtin_amdgcn_s_sleep(PSLP);
      asm volatile("" ::: "memory");
      v = look(hop, mask, true);
    }
    const unsigned t = (lane < 32 ? v[1] : t0) << 12;
    for (int n = 0; n < 64; ++n) {
      const unsigned now = (unsigned)__builtin_amdgcn_s_memrealtime() << 12;
      if (!__any((int)(t - now) > 0)) break;
    }
    asm volatile("" ::: "memory");
  };
  auto poll = [&](int hop, int mask, int floor = 0) { settle(hop, mask, look(hop, mask, true), floor); };
  auto make_trb = [&]() {
    const int rb = (lane >> 4) & 1, q = (lane >> 2) & 3, p = lane & 3;
    const int rp = 4 * rb + p, lam = (rp & 4) | ((rp & 1) << 1) | ((rp >> 1) & 1);
    const int pc = ((odd ? 0x01327654 : 0x76543210) >> (4 * lam)) & 7;
    return lds + (8 * hh + q) * 64 + pc * 8;
  };
  auto store_x0 = [&](u32x4 f0, unsigned base) { int ln = lane; PIN(ln); st128<SC1>(f0, rWS, ln * 16, base + XOFF + (3 * pair + 2 * kh) * 1024); };
  auto store_x1 = [&](u32x4 f1, unsigned base) {
    int ln = lane; PIN(ln);
    st128<SC1>(f1, rWS, ln * 16 + __umul24(ln >> 5, 24576 - 512 - 1024 - pair * 2048), base + XOFF + (3 * pair + 1) * 1024 + kh * 512);
  };
  auto store_o = [&](u32x4 f0, u32x4 f1, unsigned base) {
    int ln = lane; PIN(ln);
    st128<SC1>(f0, rWS, ln * 16, base + TOFF + (3 * pair + 2 * odd) * 1024);
    st128<SC1>(f1, rWS, ln * 16 + (ln >> 5) * OOB, base + TOFF + (3 * pair + 1) * 1024 + odd * 512);
    __builtin_amdgcn_raw_buffer_store_b64(lo2(f1), rWS, ln * 8 + (1 - (ln >> 5)) * OOB, base + TOFF + 24576 - 256 + h * 256, SC1);
  };
  const int ht = tid & 255, sub = lane & 3;
  const int col = h * 56 - odd * 8;
  int krow[7];
  auto mkrow = [&](int i) {
    const int s = 4 * i + w;
    const int p = (s * 43) >> 7, m = s - 3 * p;
    const int c = i < 6 ? 7 * p + ((0x520 >> (4 * m)) & 15) + hh * (1 + (m & 1)) : 7 * (2 * w + hh) + 3;
    krow[i] = __umul24(8 * c + ((lane >> 2) & 7), 2688) + sub * 16;
  };
  auto ldw = [&](rsrc_t rs, int i, int stride, int off) { const u32x4 v = __builtin_amdgcn_raw_buffer_load_b128(rs, krow[i], col + off, 0); SB(); return v; };
  auto write_slab = [&](const u32x4* p, unsigned off) {
    lptr d = lds + off + ht * 16; PIN(d);
#pragma unroll
    for (int i = 0; i < 7; ++i) *(LAS u32x4*)(d + i * 4096) = p[i];
  };
  auto patch = [&](u32x4* p, int n, bool ones) __attribute__((always_inline)) {
    unsigned k01 = (odd && sub == 0) ? 0u : ~0u, k23 = (!odd && sub == 3) ? 0u : ~0u; PIN(k01); PIN(k23);
    const unsigned o0 = ones ? ~k01 & 0x3f80u : 0u, o2 = ones ? ~k23 & 0x3f80u : 0u;
#pragma unroll
    for (int i = 0; i < n; ++i) { p[i][0] = (p[i][0] & k01) | o0; p[i][1] &= k01; p[i][2] = (p[i][2] & k23) | o2; p[i][3] &= k23; }
  };
  auto put_slab = [&](u32x4* p, int type, int part) {
    patch(p, 7, type == 0 && part == 0);
    write_slab(p, part * SLAB);
  };
  auto read_frags = [&](u32x4* p, int part, lptr trb) {
    lptr wb = trb + part * SLAB + w * 1024; PIN(wb);
#pragma unroll
    for (int i = 0; i < 7; ++i) p[i] = trfrag(wb + i * 4096);
  };
  auto sum_frags = [&](const u32x4* p, int type, int part) {
    f32x16 a = Z16;
#pragma unroll
    for (int i = 0; i < 7; ++i) a = mfma((u32x4){ONES, ONES, ONES, ONES}, p[i], a);
    unsigned hi, lo; split2(a[0], hi, lo);
    if (hh == (w >> 1)) *(LAS u32x2*)(lds + L_CS + type * 3072 + part * 1024 + lane * 16 + (w & 1) * 8) = (u32x2){hi | (lo << 16), hi};
  };
  const rsrc_t rWA = __builtin_amdgcn_make_buffer_rsrc((void*)wqkvA, 0, DM * 3 * DM * 2, 0x00020000);

  if (wave < 4) {
    RSTAMP(62); STAMP(0);
    u32x4 xf[28];
    f32x16 xs, ak, av;
    float rstd;
    u32x4 mf;
    auto stats = [&](float s1, float s2) {
      const float mean = s1 * (1.0f / DM);
      rstd = __builtin_amdgcn_rsqf(fmaxf(s2 * (1.0f / DM) - mean * mean, 0.f) + 1e-5f);
      unsigned hi, lo; split2(-mean, hi, lo);
      mf = (u32x4){hi | (hi << 16), lo, hi | (hi << 16), lo};
      PIN(rstd); PIN(mf);
    };
    u32x4 id0, id1;
    float wtail;
    unsigned lomask;
    {
      __builtin_amdgcn_s_setprio(3);
      id0 = ident_frag(0, r, hh); id1 = ident_frag(1, r, hh);
      wtail = hh ? 0.f : 1.f; lomask = hh ? 0u : ~0u;
      PIN(id0); PIN(id1); PIN(wtail); PIN(lomask);
      if (w >= 2) {
        u32x4 mk;
#pragma unroll
        for (int i = 0; i < 4; ++i) { const int k0 = ((2 * i) & 3) + 8 * (i >> 1) + 4 * hh + 16 * (w & 1); mk[i] = (k0 > r ? 0xffffu : 0u) | (k0 + 1 > r ? 0xffff0000u : 0u); }
        *(LAS u32x4*)(lds + L_MK + (w & 1) * 1024 + lane * 16) = mk;
      }
      if (w == 0) { cnt[0] = 0u; cnt[1] = 0u; cnt[2] = 0u; cnt[3] = 0u; cnt[4] = 0u; cnt[5] = 0u; cnt[6] = 0u; }
      cnt[8 + w] = 0u; cnt[12 + w] = 0u;
      const int lam = 4 * (w >> 1) + 2 * hh + (w & 1);
      const int pc = ((odd ? 0x01327654 : 0x76543210) >> (4 * lam)) & 7;
      unsigned off = lam == 7 ? OOB : ((h * 28 + 4 * pc - 4 * odd) * 28 + (r < 28 ? r : r - 8)) * 2;
      const rsrc_t rPH = __builtin_amdgcn_make_buffer_rsrc((void*)wphi, 0, 16 * 28 * 28 * 2, 0x00020000);
#pragma unroll
      for (int j = 0; j < 4; ++j) xf[27][j] = __builtin_amdgcn_raw_buffer_load_b16(rPH, off + j * 56, 0, 0);
      load_epoch();
      xf[26] = cat(ev, ev);
      __builtin_amdgcn_s_setprio(0);
      STAMPX(54);
    }
    tag = (u32x2){0u, 0u}; epoch = 0;
    asm volatile("" : "=v"(ak), "=v"(av), "=v"(rstd), "=v"(mf), "=v"(xs));
#pragma unroll
    for (int i = 0; i < 26; ++i) asm volatile("" : "=v"(xf[i]));
    unsigned sent = SENT;
    auto release = [&](int hop) { if (w == 0) poll(hop, 15); bar(); };
    for (int layer = 0; layer < NL; ++layer) {
      const int type = layer & 1;
      const int pb = 2 + layer * 14;
      if (layer > 0) {
        unsigned base = WS_XO + XBUF(layer - 1) * XO_B + TOFF;
        int v16 = lane * 16, vs = v16 + (v16 & 512);
        lptr bk = lds + L_WK + v16;
        PIN(vs);
        release(3 * layer - 1);
        STAMP(pb + 0);
        for (;;) {
          PIN(v16); PIN(vs); PIN(bk); asm volatile("" : "+s"(base));
          u32x4 sp[8];
          auto ld = [&](int s) { xf[4 + s] = __builtin_amdgcn_raw_buffer_load_b128(rWS, v16 + (s & 3) * 1024, base + (s >> 2) * 4096, SC1); SB(); };
          unsigned m = 1;
          ak = Z16; av = Z16;
          float s1 = 0.f, s2 = 0.f, mean;
          unsigned hi;
          u32x4 ck, cv;
          gemm2(ak, av, bk, xf,
                    [&](auto gap) {
#pragma unroll
                      for (int u = 0; u < 8; ++u) { sp[u] = __builtin_amdgcn_raw_buffer_load_b128(rWS, vs + ((u >> 1) & 1) * 2048 + (u & 1) * 512, base + 24576 + (u >> 2) * 4096, SC1); SB(); gap(u); if (u < 4) PACE2(); else PACED(); }
#pragma unroll
                      for (int s = 0; s < LA - 8; ++s) { ld(s); PACED(); }
                    },
                    [&](int i) {
                      if (i + LA - 8 < 24) ld(i + LA - 8);
                      if (i < 4) { PIN(sp[2 * i]); PIN(sp[2 * i + 1]); xf[i] = cat(lo2(sp[2 * i]), lo2(sp[2 * i + 1])); }
                      if (i == 22) { av = mfma(cv, mf, av); SB(); }
                      if (i == 24) { ak = mfma(ck, mf, ak); SB(); }
                    },
                    [&](int i, int hf) {
                      if (i < 4) m = max4(m, sp[2 * i + hf]); else m = umax3(m, xf[i][2 * hf], xf[i][2 * hf + 1]);
                      PIN(m);
                      if (i < 8) { if (hf) { s2 += U2F(sp[i][3]); PIN(s2); } else { s1 += U2F(sp[i][2]); PIN(s1); } }
                      if (hf) return;
                      if (i == 13) asm volatile("ds_write_b32 %0, %1" :: "v"(lds + L_SYNC + 32 + 4 * w), "v"(layer) : "memory");
                      if (i == 9) { auto t = __builtin_amdgcn_permlane32_swap(F2U(s1), F2U(s2), false, false); s1 = U2F(t[0]) + U2F(t[1]); PIN(s1); }
                      if (i == 10) { auto t = __builtin_amdgcn_permlane32_swap(F2U(s1), F2U(s1), false, false); s1 = U2F(t[0]); s2 = U2F(t[1]); PIN(s1); PIN(s2); }
                      if (i == 14) { mean = s1 * (-1.0f / DM); s2 = s2 * (1.0f / DM); PIN(mean); PIN(s2); }
                      if (i == 15) { s2 = fmaxf(fmaf(-mean, mean, s2), 0.f); PIN(s2); }
                      if (i == 16) { rstd = __builtin_amdgcn_rsqf(s2 + 1e-5f); PIN(rstd); }
                      if (i == 17) { hi = pk2(mean, mean); s1 = mean - bfhi(hi); PIN(hi); PIN(s1); }
                      if (i == 18) { const unsigned lo = pk2(s1, 0.f); mf = (u32x4){hi, lo, hi, lo}; PIN(mf); }
                      if (i == 19) { const lptr cs = lds + L_CS + type * 3072 + v16; ck = ldl(cs); cv = ldl(cs + 1024); }
                    }, EARLYAB);
          if (__builtin_expect(!__any(m == sent), 1)) break;
          if (--budget == 0) sent = 0u;
          RET(layer * 3);
          __builtin_amdgcn_s_sleep(BACK);
          asm volatile("" ::: "memory");
        }
      }
      int first = layer; asm volatile("" : "+s"(first));
      if (first == 0) {
        int ln = lane; PIN(ln);
        const int hh = ln >> 5;
        const unsigned ra = (ln & 31) * 128 + ((ln >> 1) & 7) * 16;
        const lptr stg = lds + L_X0 + w * SLAB;
        auto kof = [](int c) { return (c >> 3) * 4096 + (c & 7) * 16; };
        lptr ad[28];
#pragma unroll
        for (int i = 0; i < 28; ++i) {
          const int k0 = kof(chunk_of(ord(i), 0)), k1 = kof(chunk_of(ord(i), 1));
          ad[i] = stg + (ra ^ k0 ^ (~lomask & (k0 ^ k1))); PIN(ad[i]);
        }
        lptr pab = lds + L_X0 + tx * SLAB + (ra ^ kof(7 * pair + 5 * kh + hh)), pc = lds + L_X0 + tx * SLAB + (ra ^ kof(7 * pair + (hh ? 3 : 2 + 2 * kh))); PIN(pab); PIN(pc);
        lptr trb = make_trb(); PIN(trb);
        SB();
        *(LAS u32x2*)(lds + L_WPH + (w >> 1) * 1024 + ln * 16 + (w & 1) * 8) = (u32x2){xf[27][0] | (xf[27][1] << 16), xf[27][2] | (xf[27][3] << 16)};
        ev = lo2(xf[26]);
        make_tag();
        drain();
        STAMPX(53);
        bar();
        STAMPX(55);
#pragma unroll
        for (int i = 0; i < 28; ++i) xf[i] = ldl(ad[i]);
        u32x4 xa = ldl(pab), xc = ldl(pc);
        SB();
        float s2a = 0.f, s2b = 0.f;
#pragma unroll
        for (int i = 0; i < 28; ++i) {
          const u32x4 u = xf[i]; s2a = dot2(u[1], u[1], dot2(u[0], u[0], s2a)); s2b = dot2(u[3], u[3], dot2(u[2], u[2], s2b));
        }
        PIN(s2a); PIN(s2b);
        STAMPX(56);
        bar();
        STAMP(1);
        u32x4 cc = ldl(lds + L_CS + ln * 16);
        ak = Z16;
        lptr bk = trb + L_WK; PIN(bk);
        gemm1<5, true>(ak, bk, xf, [](int) {}, [&](int i) {
          if (i == 4) {
            const unsigned sel = hh && kh ? 0u : ~0u;
            xc[0] = (xc[0] & sel) | (xc[2] & ~sel); xc[1] = (xc[1] & sel) | (xc[3] & ~sel); PIN(xc);
          }
          if (i >= 16 && i < 24) { const unsigned u = i < 20 ? xa[i & 3] : xc[i & 3]; xs[2 * (i - 16)] = bflo(u); xs[2 * (i - 16) + 1] = bfhi(u); PIN(xs); }
        }, NOAB);
        asm volatile("" :: "v"(ak));
        STAMPX(57);
        bar();
        STAMP(2);
        const u32x4 cv = ldl(lds + L_CS + 1024 + ln * 16);
        av = Z16;
        gemm1<5, true>(av, bk + SLAB, xf, [&](int i) { if (i == 8) { ak = mfma(cc, mf, ak); SB(); } if (i == 10) { av = mfma(cv, mf, av); SB(); } }, [&](int i) {
          if (i == 2) {
            float s1 = U2F(F2U(ak[12]) & ~lomask), s2 = s2a + s2b;
            red2<false>(s1, s2);
            stats(s1, s2);
          }
        }, NOAB);
      }
      asm volatile("" ::: "memory");
      arrive(0);
      STAMPX(pb + 1);
      const unsigned mid = WS_MID + (h * 4 + MBUF(layer)) * MID_B;
      int ln16 = lane * 16; PIN(ln16);
      u32x4 wph[2], kf0, kf1;
      if (type == 0) {
        wph[0] = ldl(lds + L_WPH + ln16); wph[1] = ldl(lds + L_WPH + 1024 + ln16);
        DUMP(0, ak, 1, rstd); DUMP(0, av, 2, rstd);
        kf0 = pack8<0>(ak); kf1 = pack8<1>(ak);
        f32x16 p = mfma(wph[0], kf0, Z16);
        p = mfma(wph[1], kf1, p);
        SB();
#pragma unroll
        for (int i = 0; i < 16; ++i) { float t = av[i] * rstd; PIN(t); av[i] = t; }
        u32x4 vb0 = pack8<0>(av), vb1 = pack8<1>(av);
        vb1[2] |= ~lomask & 0x3f80u;
        PIN(vb0); PIN(vb1);
        lptr ta0, ta1, tb0, tb1;
        {
          const int q = (ln16 >> 6) & 3, rp = ((ln16 >> 6) & 4) + ((ln16 >> 4) & 3), lam = (rp & 4) | ((rp & 1) << 1) | ((rp >> 1) & 1);
          const int o0 = (8 * (ln16 >> 9) + q) * 64 + (((lam >> 1) ^ (q >> 1)) * 16) + (lam & 1) * 8, o1 = (o0 ^ 32) + 256;
          tb0 = lds + L_SCR + o0; tb1 = lds + L_SCR + o1; ta0 = tb0 + (w >> 1) * 8192; ta1 = tb1 + (w >> 1) * 8192;
          PIN(ta0); PIN(ta1); PIN(tb0); PIN(tb1);
        }
        const int rr = (ln16 >> 4) & 31, pc = (ln16 >> 9) ^ ((rr >> 1) & 3);
        lptr d0 = lds + L_SCR + w * 2048 + rr * 64 + pc * 16, d1 = lds + L_SCR + w * 2048 + rr * 64 + (pc ^ 2) * 16;
        lptr ma = lds + L_SYNC + ((ln16 & 64) ? 12 : 48 + ((ln16 >> 2) & 12)), mw = lds + L_SYNC + 48 + 4 * w;
        unsigned mv = (ln16 & 64) ? 4u : layer + 1u, ml = layer + 1u, got = *cnt;
        PIN(d0); PIN(d1); PIN(ma); PIN(mw); PIN(mv); PIN(ml);
        SB();
        u32x4 fp[2], fm[2];
        dual_softmax(p, rstd * L2E, wtail, fp, fm, [&] {
          if (__builtin_amdgcn_readfirstlane(got) < 8u * layer + 4u) gather_all(layer, false);
          asm volatile("" ::: "memory");
          *(LAS u32x4*)(d0 + 16384) = vb0; *(LAS u32x4*)(d1 + 16384) = vb1;
          SB();
        });
        fp[1][2] &= lomask; fp[1][3] &= lomask; fm[1][2] &= lomask; fm[1][3] &= lomask;
        *(LAS u32x4*)(d0) = fp[0]; *(LAS u32x4*)(d1) = fp[1];
        *(LAS u32x4*)(d0 + 8192) = fm[0]; *(LAS u32x4*)(d1 + 8192) = fm[1];
        asm volatile("ds_write_b32 %0, %1" :: "v"(mw), "v"(ml) : "memory");
        while (!__all(*(volatile LAS unsigned*)ma >= mv)) { }
        asm volatile("" ::: "memory");
        {
          auto tr2 = [](lptr p0, lptr p1) {
            bf16x4 a0 = __builtin_amdgcn_ds_read_tr16_b64_v4bf16((LAS bf16x4*)p0), a1 = __builtin_amdgcn_ds_read_tr16_b64_v4bf16((LAS bf16x4*)p1);
            return __builtin_bit_cast(u32x4, __builtin_shufflevector(a0, a1, 0, 1, 2, 3, 4, 5, 6, 7));
          };
          u32x4 fa[4], fb[4];
          f32x16 kv = Z16;
#pragma unroll
          for (int i = 0; i < 3; ++i) { fa[i] = tr2(ta0 + i * 1024, ta1 + i * 1024); fb[i] = tr2(tb0 + 16384 + i * 1024, tb1 + 16384 + i * 1024); SB(); }
#pragma unroll
          for (int i = 0; i < 8; ++i) {
            kv = mfma(fa[i & 3], fb[i & 3], kv);
            SB();
            if (i + 3 < 8) { fa[(i + 3) & 3] = tr2(ta0 + (i + 3) * 1024, ta1 + (i + 3) * 1024); fb[(i + 3) & 3] = tr2(tb0 + 16384 + (i + 3) * 1024, tb1 + 16384 + (i + 3) * 1024); }
            SB();
          }
          st128<SC1>((w & 1) ? pack8<1>(kv) : pack8<0>(kv), rWS, ln16, mid + (tb * 4 + w) * 1024);
          CALS(3 * layer);
        }
        while (__builtin_amdgcn_readfirstlane(cnt[6]) < 4u) __builtin_amdgcn_s_sleep(2);
        asm volatile("" ::: "memory");
      } else {
        av *= rstd;
        DUMP(0, av, 2, 1.f);
        u32x4 vb1 = pack8<1>(av);
        vb1[2] |= ~lomask & 0x3f80u;
        f32x16 vt = mfma(pack8<0>(av), id0, Z16);
        vt = mfma(vb1, id1, vt);
        SB();
#pragma unroll
        for (int i = 0; i < 16; ++i) { float t = ak[i] * rstd; PIN(t); ak[i] = t; }
        DUMP(0, ak, 1, 1.f);
        kf0 = pack8<0>(ak); kf1 = pack8<1>(ak);
        const unsigned g = mid + T * 4096;
        const lptr d = lds + L_SCR + (8 + w) * 4096 + ln16;
        st128<SC1>(kf0, rWS, ln16, g);
        const u32x4 vf0 = pack8<0>(vt), vf1 = pack8<1>(vt);
        st128<SC1>(vf0, rWS, ln16 + 2048, g);
        st128<SC1>(vf1, rWS, ln16 + 3072, g);
        {
          u32x4 kc = kf1;
          kc[3] |= ~lomask & (tag[0] + layer - xhalf_addu(add4(add4(0u, kf0), kf1) + add4(add4(0u, vf0), vf1)));
          st128<SC1>(kc, rWS, ln16 + 1024, g);
          CALS(3 * layer);
        }
        gather_all(layer, false);
        *(LAS u32x4*)(d) = kf0; *(LAS u32x4*)(d + 1024) = kf1; *(LAS u32x4*)(d + 2048) = vf0; *(LAS u32x4*)(d + 3072) = vf1;
      }
      STAMPX(pb + 2);
      u32x4 qf0, qf1;
      {
        f32x16 aq = mfma(ldl(lds + L_CS + type * 3072 + 2048 + ln16), mf, Z16);
        lptr bq = lds + L_WQ + ln16; PIN(bq);
        gemm1<5, false>(aq, bq, xf, [](int) {}, [](int) {}, NOAB);
        aq *= type ? rstd * (0.18898223650461361f * L2E) : 1.0f;
        DUMP(0, aq, 0, type ? 1.f : rstd);
        qf0 = pack8<0>(aq); qf1 = pack8<1>(aq);
        qf1[2] |= ~lomask & 0x3f80u;
      }
      f32x16 o;
      if (type == 0) {
        u32x4 fp[2], fm[2];
        {
          f32x16 p = mfma(wph[0], qf0, Z16);
          p = mfma(wph[1], qf1, p);
          dual_softmax(p, rstd * L2E, wtail, fp, fm, [] {});
        }
        PIN(fp[0]); PIN(fp[1]); PIN(fm[0]); PIN(fm[1]);
        STAMPX(pb + 3);
        bar();
        STAMP(pb + 4);
        const lptr kvf = lds + L_KVF + ln16;
        const u32x4 a0 = ldl(kvf), a1 = ldl(kvf + 1024), a2 = ldl(kvf + 2048), a3 = ldl(kvf + 3072);
        SB();
        o = mfma(a0, fp[0], Z16);
        o = mfma(a1, fp[1], o);
        o = mfma(a2, fm[0], o);
        o = mfma(a3, fm[1], o);
        o *= __builtin_amdgcn_rcpf(upper_val(o[12]) + 1e-6f);
      } else {
        lptr kv = lds + L_SCR + w * 4096 + ln16; PIN(kv);
        auto ex4 = [&](f32x16& s, int i0) {
#pragma unroll
          for (int i = i0; i < i0 + 4; ++i) s[i] = __builtin_amdgcn_exp2f(s[i]);
          unsigned a = pk2(s[i0], s[i0 + 1]), c = pk2(s[i0 + 2], s[i0 + 3]); PIN(a); PIN(c);
          SB();
          return (u32x2){a, c};
        };
        auto tile_max = [&](int j, int side, float m) {
          f32x16 t = mfma(ldl(kv + j * 4096 + 1024), qf1, mfma(ldl(kv + j * 4096), qf0, Z16));
          int lim = ((ln16 >> 4) & 31) - ((ln16 >> 7) & 4); PIN(lim);
#pragma unroll
          for (int i = 0; i < 16; ++i) { const bool up = (i & 3) + 8 * (i >> 2) > lim; m = fmaxf(m, (side == 0 || up == (side == 1)) ? t[i] : m); }
          return m;
        };
        float m = -__builtin_inff(), l;
        for (int pass = 0;; ++pass) {
          const u32x4 mk0 = ldl(lds + L_MK + ln16), mk1 = ldl(lds + L_MK + 1024 + ln16);
          f32x16 negm;
          {
            const u32x4 v0 = ldl(kv + 8 * 4096 + 2048), v1 = ldl(kv + 8 * 4096 + 3072);
            f32x16 s;
#pragma unroll
            for (int i = 0; i < 16; ++i) { unsigned k = (i < 8 ? mk0 : mk1)[(i >> 1) & 3]; if (!(i & 1)) { k <<= 16; PIN(k); } s[i] = U2F(k & 0xff800000u); }
            s = mfma(ldl(kv + 8 * 4096 + 1024), qf1, mfma(ldl(kv + 8 * 4096), qf0, s));
#pragma unroll
            for (int i = 0; i < 16; i += 2) m = fmax3(m, s[i], s[i + 1]);
            m = xhalf_max(m);
            s -= m;
            negm = splat(-m);
            PIN(negm);
            const u32x2 a = ex4(s, 0), c = ex4(s, 4), e = ex4(s, 8), f = ex4(s, 12);
            o = mfma(v1, cat(e, f), mfma(v0, cat(a, c), Z16));
            PIN(o);
          }
          if (pass == 0) {
            STAMPX(pb + 3);
            bar();
            STAMP(pb + 4);
          }
          u32x4 kk[2][2], vv[2][2], pa, pb2;
          f32x16 st[2];
          kk[0][0] = ldl(kv); kk[0][1] = ldl(kv + 1024); kk[1][0] = ldl(kv + 4096); kk[1][1] = ldl(kv + 4096 + 1024);
          SB();
          st[0] = mfma(kk[0][1], qf1, mfma(kk[0][0], qf0, negm));
          SB();
#pragma unroll
          for (int j = 0; j < 8; ++j) {
            f32x16& s = st[j & 1];
            vv[j & 1][0] = ldl(kv + j * 4096 + 2048); vv[j & 1][1] = ldl(kv + j * 4096 + 3072);
            if (j < 6) { kk[j & 1][0] = ldl(kv + (j + 2) * 4096); kk[j & 1][1] = ldl(kv + (j + 2) * 4096 + 1024); }
            SB();
            if (j == 0) { st[1] = mfma(kk[1][0], qf0, negm); SB(); }
            const u32x2 a = ex4(s, 0);
            if (j == 0) st[1] = mfma(kk[1][1], qf1, st[1]); else o = mfma(vv[(j - 1) & 1][0], pa, o);
            SB();
            const u32x2 c = ex4(s, 4);
            if (j == 7) o = mfma(vv[0][1], pb2, o); else if (j > 0) st[(j + 1) & 1] = mfma(kk[(j + 1) & 1][0], qf0, negm);
            SB();
            const u32x2 e = ex4(s, 8);
            if (j == 7) o = mfma(vv[1][0], cat(a, c), o); else if (j > 0) st[(j + 1) & 1] = mfma(kk[(j + 1) & 1][1], qf1, st[(j + 1) & 1]);
            SB();
            const u32x2 f = ex4(s, 12);
            if (j == 7) o = mfma(vv[1][1], cat(e, f), o); else if (j > 0) o = mfma(vv[(j - 1) & 1][1], pb2, o);
            SB();
            pa = cat(a, c); pb2 = cat(e, f);
            if (j == 0) { pa &= mk0; pb2 &= mk1; PIN(pa); PIN(pb2); }
          }
          l = upper_val(o[12]);
          if (pass > 0 || __all(l < LMAX)) break;
          RET(13);
          m = tile_max(0, 1, m);
#pragma unroll 1
          for (int j = 1; j < 8; ++j) m = tile_max(j, 0, m);
        }
        arrive(4);
        o *= __builtin_amdgcn_rcpf(l);
      }
      DUMP(1, o, 0, 1.f);
      store_o(pack8<0>(o), pack8<1>(o), WS_XO + OBUF(layer) * XO_B);
      CALS(3 * layer + 1);
      STAMPX(pb + 6);
      lptr bown = lds + L_WO + kh * (SLAB + 1024) + ln16, both = lds + L_WO + kh * (SLAB - 1024) + 1024 + ln16;
      PIN(bown); PIN(both);
      unsigned base = WS_XO + OBUF(layer) * XO_B + XOFF + kh * 12288;
      int v16 = ln16, vs = (v16 >> 1) + ((v16 >> 1) & 256);
      PIN(vs);
      release(3 * layer + 1);
      STAMP(pb + 7);
      f32x16 xn, xo;
      u32x4 wa[4];
      auto tr = [](lptr p) { return ldl(p); };
      for (;;) {
        PIN(v16); PIN(vs); asm volatile("" : "+s"(base));
        u32x2 sp[4];
        u32x4 wb[4];
#pragma unroll
        for (int u = 0; u < 4; ++u) { sp[u] = __builtin_amdgcn_raw_buffer_load_b64(rWS, vs + (u >> 1) * 1024 + (u & 1) * 256, base + 24576 - kh * 10240, SC1); SB(); if (u & 1) PACED(); }
#pragma unroll
        for (int s = 0; s < LAO; ++s) { xf[2 + s] = __builtin_amdgcn_raw_buffer_load_b128(rWS, v16 + (s & 3) * 1024, base + (s >> 2) * 4096, SC1); SB(); if (s < 3) wb[s] = tr(both + s * 2048); else if (s < 6) wa[s - 3] = tr(bown + (s - 3) * 2048); SB(); if (s < 6) PACE2(); else PACED(); }
        xf[0] = cat(sp[0], sp[1]); xf[1] = cat(sp[2], sp[3]);
        xn = xs; xo = Z16;
        unsigned m = 1;
#pragma unroll
        for (int i = 0; i < 14; ++i) {
          if (i + LAO < 12) { const int s = i + LAO; xf[2 + s] = __builtin_amdgcn_raw_buffer_load_b128(rWS, v16 + (s & 3) * 1024, base + (s >> 2) * 4096, SC1); SB(); }
          if (i + 3 < 14) wb[(i + 3) & 3] = tr(both + (i + 3) * 2048);
          SB();
          xo = mfma(wb[i & 3], xf[i], xo);
          SB();
          m = max4(m, xf[i]); PIN(m);
          if (i >= LAG) {
            wa[(i - LAG + 3) & 3] = tr(bown + (i - LAG + 3) * 2048);
            SB();
            xn = mfma(wa[(i - LAG) & 3], xf[i - LAG], xn);
          }
          SB();
        }
        if (__builtin_expect(!__any(m == sent), 1)) break;
        if (--budget == 0) sent = 0u;
        RET(layer * 3 + 2);
        __builtin_amdgcn_s_sleep(BACK);
        asm volatile("" ::: "memory");
      }
      {
        const lptr ex = lds + L_WQ + ln16;
        f32x4 t[4];
#pragma unroll
        for (int i = 14 - LAG; i < 14; ++i) {
          if (i + 3 < 14) wa[(i + 3) & 3] = tr(bown + (i + 3) * 2048);
          SB();
          xn = mfma(wa[i & 3], xf[i], xn);
          SB();
          if (i == 15 - LAG) {
#pragma unroll
            for (int g = 0; g < 4; ++g) *(LAS f32x4*)(ex + w * 4096 + g * 1024) = (f32x4){xo[4 * g], xo[4 * g + 1], xo[4 * g + 2], xo[4 * g + 3]};
          }
          if (i == 11) {
            bar();
#pragma unroll
            for (int g = 0; g < 4; ++g) t[g] = *(LAS f32x4*)(ex + (w ^ 1) * 4096 + g * 1024);
          }
          SB();
        }
#pragma unroll
        for (int g = 0; g < 4; ++g) {
#pragma unroll
          for (int j = 0; j < 4; ++j) xs[4 * g + j] = xn[4 * g + j] + t[g][j];
        }
      }
      STAMPX(pb + 8);
      if (layer == NL - 1) {
        const rsrc_t rOUT = __builtin_amdgcn_make_buffer_rsrc((void*)out, 0, SEQ * DM * 2, 0x00020000);
        const u32x4 f0 = pack8<0>(xs), f1 = pack8<1>(xs);
        int ln = lane; PIN(ln);
        const int row = (ln & 31) * 896, up = ln >> 5;
        const int ob = (tb * 4 + tx) * 32 * 896 + 7 * pair * 16;
        st128<0>(f0, rOUT, row + up * 16, ob + 5 * kh * 16);
        st128<0>(f1, rOUT, row + up * OOB, ob + (2 + 2 * kh) * 16);
        __builtin_amdgcn_raw_buffer_store_b64(lo2(f1), rOUT, row + (1 - up) * OOB, ob + 3 * 16 + kh * 8, 0);
      } else {
        const u32x4 f0 = pack8<0>(xs);
        store_x0(f0, WS_XO + XBUF(layer) * XO_B);
        u32x4 f1 = pack8<1>(xs);
        f1[2] &= lomask; f1[3] &= lomask;
        float s1 = 0.f, s2 = 0.f, t1 = 0.f, t2 = 0.f;
#pragma unroll
        for (int j = 0; j < 4; ++j) { const unsigned u = f0[j], v = f1[j]; s1 = dot2(u, ONES, s1); s2 = dot2(u, u, s2); t1 = dot2(v, ONES, t1); t2 = dot2(v, v, t2); }
        s1 += t1; s2 += t2;
        red2<false>(s1, s2);
        f1[2] |= ~lomask & F2U(s1); f1[3] |= ~lomask & F2U(s2);
        store_x1(f1, WS_XO + XBUF(layer) * XO_B);
        CALS(3 * layer + 2);
        STAMPX(pb + 9);
      }
      asm volatile("" : "=v"(ak), "=v"(av), "=v"(rstd), "=v"(mf));
#pragma unroll
      for (int i = 14; i < 28; ++i) asm volatile("" : "=v"(xf[i]));
    }
    STAMP(61); RSTAMP(63);
    TOT1
    if (b == 0 && w == 0) __builtin_amdgcn_raw_buffer_store_b64((u32x2){(unsigned)(epoch + 1), (unsigned)((epoch + 1) >> 32)}, rWS, WS_EPOCH, 0, SC1);
  } else {
    STAMPX(53);
    dma_x0(8, XE, 0);
    u32x4 pA[21], pB[21];
    unsigned tch0, tch1;
    asm volatile("s_load_dword %0, %2, 0x0\n\ts_load_dword %1, %3, 0x0" : "=&s"(tch0), "=&s"(tch1)
                 : "s"(w == 0 ? (const void*)ws : w == 1 ? (const void*)wqkvB : w == 2 ? (const void*)woA : (const void*)wphi), "s"(w < 2 ? (const void*)woB : (const void*)out));
    unsigned warm0;
    {
      u64 pc; asm volatile("s_getpc_b64 %0" : "=s"(pc));
      const rsrc_t rs = __builtin_amdgcn_make_buffer_rsrc(w == 3 ? (void*)(pc & ~127ull) : (void*)wqkvA, 0, w == 3 ? CODE_B : DM * 3 * DM * 2, 0x00020000);
      const int n = w == 3 ? 9 : 13;
      warm0 = __builtin_amdgcn_raw_buffer_load_b32(rs, lane < n ? (lane + (w == 3 ? (b >> 3) : b * 3 + w) * n) * 128 : OOB, 0, 0);
      SB();
    }
#pragma unroll
    for (int i = 0; i < 7; ++i) { mkrow(i); PIN(krow[i]); }
    lptr trb = make_trb(); PIN(trb);
    u32x4 id0 = ident_frag(0, r, hh); PIN(id0);
    SB();
    dma_x0(XE, 28, 0);
#pragma unroll
    for (int i = 0; i < NV0; ++i) pA[7 + i] = ldw(rWA, i, 2688, 896);
    STAMPX(54);
    asm volatile("s_waitcnt vmcnt(%2) lgkmcnt(0)" :: "s"(tch0), "s"(tch1), "n"(NV0) : "memory");
    const u32x4 sv = {SENT, SENT, SENT, SENT};
    auto clean_kv = [&](int layer) { if (layer < NL) st128<SC1>(sv, rWS, lane * 16, WS_MID + (h * 4 + MBUF(layer)) * MID_B + (tb * 4 + w) * 1024); };
    auto clean_o = [&](int layer) { if (layer < NL) store_o(sv, sv, WS_XO + OBUF(layer) * XO_B); };
    auto clean_x = [&](int layer) { if (layer < NL - 1) { store_x0(sv, WS_XO + XBUF(layer) * XO_B); store_x1(sv, WS_XO + XBUF(layer) * XO_B); } };
    unsigned r0 = (unsigned)__builtin_amdgcn_s_memrealtime(), c0 = (unsigned)__builtin_amdgcn_s_memtime(), tann = 0;
    auto mark = [&](int cyc) {
      const unsigned r1 = (unsigned)__builtin_amdgcn_s_memrealtime(), c1 = (unsigned)__builtin_amdgcn_s_memtime();
      tann = r1 + LAND + (unsigned)((float)cyc * (float)(r1 - r0) * __builtin_amdgcn_rcpf((float)(c1 - c0)));
      r0 = r1; c0 = c1;
    };
    auto push = [&](int hop, unsigned voff) {
      CALA(hop);
      __builtin_amdgcn_raw_buffer_store_b64((u32x2){tag[0], (tag[1] & 0xfff00000u) | (tann & 0xfffffu)}, rWS, voff, WS_FLAGS + hop * HOP_B, SC1);
    };
    auto push_grp = [&](int hop) { push(hop, lane < 16 ? (tb * 16 + lane) * 128 + h * 8 : OOB); };
    auto push_mid = [&](int layer) {
      if (layer & 1) push(3 * layer, (lane < 2 && tb + 1 + lane < 16) ? ((tb + 1 + lane) * 16 + h) * 128 + lane * 8 : OOB);
      else push(3 * layer, lane < 16 ? (lane * 16 + h) * 128 + tb * 8 : OOB);
    };
    const rsrc_t rWB = __builtin_amdgcn_make_buffer_rsrc((void*)wqkvB, 0, DM * 3 * DM * 2, 0x00020000);
    auto spin = [&](int k, unsigned n) { while (__builtin_amdgcn_readfirstlane(cnt[k]) < n) __builtin_amdgcn_s_sleep(1); asm volatile("" ::: "memory"); };
    auto wait_h = [&](int hop, int mask) {
      if (w == 0) { if (mask >= 0) poll(hop, mask); cnt[1] = hop + 1; }
      else while (__builtin_amdgcn_readfirstlane(cnt[1]) != hop + 1) __builtin_amdgcn_s_sleep(1);
      asm volatile("" ::: "memory");
    };
    const unsigned pkb = WS_PK + h * 262144;
    auto ldp = [&](unsigned off) { const u32x4 v = __builtin_amdgcn_raw_buffer_load_b128(rWS, lane * 16, pkb + off + w * 1024, SC1); SB(); return v; };
    STAMPX(55);
    bar();
#pragma unroll
    for (int i = NV0; i < 7 + NV1; ++i) pA[7 + i] = ldw(rWA, i % 7, 2688, 896 + 896 * (i / 7));
    put_slab(pA + 7, 0, 0);
#pragma unroll
    for (int i = NV1; i < 7; ++i) pA[14 + i] = ldw(rWA, i, 2688, 1792);
    read_frags(pA + 7, 0, trb); sum_frags(pA + 7, 0, 0);
    __builtin_amdgcn_s_setprio(3);
    STAMPX(57);
    bar();
    load_epoch();
    const unsigned warm1 = __builtin_amdgcn_raw_buffer_load_b32(rWS, (FW && w == 0 && lane < 11) ? lane * HOP_B : OOB, WS_FLAGS + me * 128, SC1);
    SB();
    u32x4 pw[4];
    {
      const unsigned act = tb < 14 ? 0u : OOB;
#pragma unroll
      for (int j = 0; j < 2; ++j) {
        const int st = 2 * tb + j, p = (st * 43) >> 7, m = st - 3 * p;
        const int c = st < 24 ? 7 * p + ((0x520 >> (4 * m)) & 15) + hh * (1 + (m & 1)) : 7 * (2 * (st - 24) + hh) + 3;
        pw[j] = __builtin_amdgcn_raw_buffer_load_b128(rWB, ((8 * c + ((lane >> 2) & 7)) * 2688 + sub * 16) | (w < 3 ? act : OOB), col + (w == 2 ? 0 : 896 + 896 * w), 0);
        SB();
      }
      {
        const int n = 2 * tb + (w & 1), k2 = n >= 14, i = n - 14 * k2, st = i - 2, p = (st * 43) >> 7, m = st - 3 * p;
        const int c = 28 * k2 + (i < 2 ? 14 * i + 3 : 7 * p + ((0x520 >> (4 * m)) & 15)), dl = i < 2 ? 7 : 1 + (m & 1);
        const unsigned vo = (c * 7168 + (lane >> 3) * 896 + 112 * pair + (((lane & 7) ^ ((lane >> 2) & 4)) * 16)) | act;
        const rsrc_t rs = __builtin_amdgcn_make_buffer_rsrc(w >> 1 ? (void*)woB : (void*)woA, 0, DM * DM * 2, 0x00020000);
        pw[2] = __builtin_amdgcn_raw_buffer_load_b128(rs, vo, 0, 0); SB();
        pw[3] = __builtin_amdgcn_raw_buffer_load_b128(rs, vo + dl * 7168, 0, 0); SB();
      }
    }
    put_slab(pA + 14, 0, 1);
#pragma unroll
    for (int j = 0; j < NQ0; ++j) pA[j] = ldw(rWA, j, 2688, 0);
    read_frags(pA + 14, 1, trb); sum_frags(pA + 14, 0, 1);
    STAMPX(58);
    bar();
    if (w == 3) mark(TKA0);
    __builtin_amdgcn_s_setprio(0);
#pragma unroll
    for (int j = NQ0; j < 7; ++j) pA[j] = ldw(rWA, j, 2688, 0);
    {
      patch(pw, 2, false);
      const unsigned act = (tb < 14 ? 0u : OOB) | (lane * 16);
      const lptr sc = lds + L_WO + w * 4096;
#pragma unroll
      for (int j = 0; j < 4; ++j) *(LAS u32x4*)(sc + j * 1024 + lane * 16) = pw[j];
      const int q = (lane >> 2) & 3, rp = ((lane >> 2) & 4) + (lane & 3), lam = (rp & 4) | ((rp & 1) << 1) | ((rp >> 1) & 1);
      auto tr128 = [&](int pc8) {
        const lptr p = sc + 2048 + (8 * hh + q) * 128 + ((pc8 * 8) ^ ((q >> 1) * 64));
        bf16x4 a0 = __builtin_amdgcn_ds_read_tr16_b64_v4bf16((LAS bf16x4*)p), a1 = __builtin_amdgcn_ds_read_tr16_b64_v4bf16((LAS bf16x4*)(p + 512));
        return __builtin_bit_cast(u32x4, __builtin_shufflevector(a0, a1, 0, 1, 2, 3, 4, 5, 6, 7));
      };
      pw[0] = trfrag(trb + L_WO + w * 4096); pw[1] = trfrag(trb + L_WO + w * 4096 + 1024);
      pw[2] = tr128(lam); pw[3] = tr128(6 + ((0x01327654 >> (4 * lam)) & 7));
#pragma unroll
      for (int j = 0; j < 2; ++j) st128<SC1>(pw[j], rWS, w < 3 ? act : OOB, pkb + w * SLAB + (2 * tb + j) * 1024);
#pragma unroll
      for (int j = 0; j < 2; ++j) st128<SC1>(pw[2 + j], rWS, act, pkb + (3 + (w & 2)) * SLAB + (2 * tb + (w & 1)) * 2048 + j * 1024);
    }
    clean_kv(0); clean_o(0);
    make_tag();
    STAMPX(59);
    put_slab(pA, 0, 2);
    drain();
    arrive(3);
    if (w == 3) { spin(3, 4); push_mid(0); }
    clean_x(0);
    read_frags(pA, 2, trb); write_slab(pA, L_WQ);
    const u32x2 fk0 = look(0, 15, w == 0);
    sum_frags(pA, 0, 2);
    arrive(6);
    asm volatile("" :: "v"(warm0), "v"(warm1));
    __builtin_amdgcn_s_setprio(3);
    u32x4 po[14];
    auto swap_wo = [&](bool keep) {
      lptr d = lds + L_WO + ht * 16; PIN(d);
#pragma unroll
      for (int i = 0; i < 14; ++i) { u32x4 t; if (keep) t = ldl(d + i * 4096); *(LAS u32x4*)(d + i * 4096) = po[i]; if (keep) po[i] = t; SB(); }
    };
    STAMPX(60);
#pragma unroll
    for (int layer = 0; layer < NL; ++layer) {
      const int type = layer & 1;
      const int pb = 2 + layer * 14;
      const unsigned mid = WS_MID + (h * 4 + MBUF(layer)) * MID_B;
      if (type == 0) {
        STAMPX(pb + 0);
        STAMPX(pb + 1);
        if (layer > 0) arrive(3);
        if (w == 0) {
          int fl = FLOORK0;
          while (!__all(cnt[12 + (lane & 3)] == layer + 1u)) { __builtin_amdgcn_s_sleep(1); fl = FLOORK; }
          if (layer > 0) { spin(3, 8); poll(3 * layer, 15, fl); }
          else settle(0, 15, fk0, fl);
        }
        wait_h(3 * layer, -1);
        STAMPX(pb + 2);
        f32x16 sum;
        for (;;) {
          sum = Z16;
          unsigned m = 0;
          u32x4 pr[16];
          const unsigned g = mid + w * 1024;
#pragma unroll
          for (int i = 0; i < 16; ++i) {
            pr[i] = __builtin_amdgcn_raw_buffer_load_b128(rWS, lane * 16, g + i * 4096, SC1); SB();
            if (i < 15) PACED();
          }
          if (w == 3) { mark(layer ? TOA : TOA0); push_grp(3 * layer + 1); SB(); }
#pragma unroll
          for (int i = 0; i < 16; ++i) {
            if (layer == 0 && i < NPO) po[i] = ldp(3 * SLAB + i * 4096);
            sum = mfma(id0, pr[i], sum); m = max4(m, pr[i]); PIN(m); SB();
          }
          if (!__any(m == SENT) || budget == 0) break;
          --budget; RET(layer * 3 + 1);
          asm volatile("" ::: "memory");
        }
        *(LAS u32x4*)(lds + L_KVF + w * 1024 + lane * 16) = pack8<0>(sum);
        STAMPX(pb + 3);
        bar();
        if (layer == 0) {
#pragma unroll
          for (int i = NPO; i < 14; ++i) po[i] = ldp(3 * SLAB + i * 4096);
        } else __builtin_amdgcn_s_sleep(5);
      } else {
        gather_all(layer, true);
        STAMPX(pb + 0);
        u32x4 t[8];
        wait_h(3 * layer, tb >= 2 ? 1 : tb - 1);
        STAMPX(pb + 2);
        if (tb - 2 + (w >> 1) >= 0) {
          const unsigned g = mid + (tb * 4 - 8 + 2 * w) * 4096;
          for (;;) {
            unsigned c[2] = {0u, 0u};
#pragma unroll
            for (int i = 0; i < 8; ++i) { t[i] = __builtin_amdgcn_raw_buffer_load_b128(rWS, lane * 16 + (i & 3) * 1024, g + (i >> 2) * 4096, SC1); SB(); if (i < 7) PACED(); }
#pragma unroll
            for (int i = 0; i < 8; ++i) {
              c[i >> 2] = add4(c[i >> 2], t[i]); PIN(c[i >> 2]);
              if ((i & 3) == 1) t[i][3] &= (unsigned)hh - 1u;
              *(LAS u32x4*)(lds + L_SCR + 2 * w * 4096 + i * 1024 + lane * 16) = t[i];
              SB();
            }
            const auto cs = __builtin_amdgcn_permlane32_swap(c[0], c[1], false, false);
            if (__all(cs[0] + cs[1] == tag[0] + layer) || budget == 0) break;
            --budget; RET(layer * 3 + 1);
            __builtin_amdgcn_s_sleep(8);
            asm volatile("" ::: "memory");
          }
        } else {
#pragma unroll
          for (int i = 0; i < 8; ++i)
            *(LAS u32x4*)(lds + L_SCR + 2 * w * 4096 + i * 1024 + lane * 16) = (u32x4){0u, 0u, ((i & 3) == 1 && hh) ? 0xff80u : 0u, 0u};
        }
        drain();
        STAMPX(pb + 3);
        bar();
        if (w == 3) { mark(layer == 1 ? TOB1 : TOB); push_grp(3 * layer + 1); }
        __builtin_amdgcn_s_setprio(0);
        if (layer == 1) {
          clean_x(1); clean_kv(2);
#pragma unroll
          for (int i = 0; i < 14; ++i) po[i] = ldp(5 * SLAB + i * 4096);
          swap_wo(true);
          drain();
          spin(4, 4);
        }
      }
      __builtin_amdgcn_s_setprio(0);
      if (layer == 0) {
        swap_wo(false);
      } else if (layer < NL - 1) {
        write_slab((type == 0 ? pB : pA) + 7, L_WK);
      }
      STAMPX(pb + 4);
      bar();
      if (w == 3 && layer < NL - 1) { mark(layer == 2 ? TX2 : layer ? TX : TX0); push_grp(3 * layer + 2); }
      STAMPX(pb + 5);
      if (layer > 0 && layer < NL - 1) { __builtin_amdgcn_s_sleep(WSL); write_slab((type == 0 ? pB : pA) + 14, L_WV); }
      if (layer == 0) {
        __builtin_amdgcn_s_sleep(12);
#pragma unroll
        for (int i = 0; i < 21; ++i) pB[(i + 7) % 21] = ldp(i * 4096);
      }
      bar();
      if (layer < NL - 1) {
        __builtin_amdgcn_s_sleep(SWS);
        if (layer == 0) { write_slab(pB + 7, L_WK); write_slab(pB + 14, L_WV); sum_frags(pB + 7, 1, 0); sum_frags(pB + 14, 1, 1); }
        else swap_wo(layer == 1);
        bar();
        __builtin_amdgcn_s_sleep(WSL);
        write_slab(type == 0 ? pB : pA, L_WQ);
        if (layer == 0) sum_frags(pB, 1, 2);
        arrive(0);
        while (!__all(cnt[8 + (lane & 3)] == layer + 1u)) __builtin_amdgcn_s_sleep(1);
        if (w == 3) { mark(layer == 0 ? TKB1 : layer == 1 ? TKA : TKB); push_mid(layer + 1); }
        __builtin_amdgcn_s_sleep(CSL);
        clean_o(layer + 1);
        if (layer == 1) { clean_x(2); drain(); }
        __builtin_amdgcn_s_setprio(3);
      }
    }
  }
}
}

extern "C" void kernel_launch(void* const* d_in, const int* in_sizes, int n_in, void* d_out, int out_size, void* d_ws, size_t ws_size, hipStream_t stream) {
  (void)hipFuncSetAttribute((const void*)fused_kernel, hipFuncAttributeMaxDynamicSharedMemorySize, L_END);
  fused_kernel<<<256, 512, L_END, stream>>>((const u16*)d_in[0], (const u16*)d_in[1], (const u16*)d_in[2], (const u16*)d_in[3],
                                            (const u16*)d_in[5], (const u16*)d_in[6], (u16*)d_out, (unsigned char*)d_ws);
}
```
